```python
import torch

def _softmax(x, axis=-1):
    return torch.softmax(x, dim=axis)
def _gelu(x):
    return torch.nn.functional.gelu(x, approximate="tanh")
def _silu(x):
    return torch.nn.functional.silu(x)
def _sigmoid(x):
    return torch.sigmoid(x)
def _layernorm(x, axis=-1, eps=1e-5):
    m = x.mean(dim=axis, keepdim=True)
    v = x.var(dim=axis, keepdim=True, unbiased=False)
    return (x - m) / torch.sqrt(v + eps)
def _scatter_add(tgt, idx, src, axis=0):
    return tgt.clone().index_add_(axis, idx, src)
def _segment_sum(src, seg, n):
    out = torch.zeros((n,) + src.shape[1:], dtype=src.dtype)
    return out.index_add_(0, seg, src)
def _pad(x, pw):
    flat = tuple(v for pair in reversed(pw) for v in pair)
    return torch.nn.functional.pad(x, flat)

kernel_name = 'standalone__mha_softmax__near_ridge'

def setup_inputs():

    _w = (lambda a: (lambda t: t.to(torch.bfloat16) if t.is_floating_point() else t)(torch.as_tensor(a)))
    B, H, Sq, Sk, D = 32, 32, 384, 384, 192
    return {
        "q": _w((lambda *s: torch.randn(*s, dtype=torch.bfloat16) * 0.8)(B, H, Sq, D)),
        "k": _w((lambda *s: torch.randn(*s, dtype=torch.bfloat16) * 0.8)(B, H, Sk, D)),
        "v": _w((lambda *s: torch.randn(*s, dtype=torch.bfloat16) * 0.05)(B, H, Sk, D)),
    }

def reference(q, k, v):
    scale = q.shape[-1] ** -0.5
    scores = torch.einsum("bhqd,bhkd->bhqk", q, k) * scale
    p = _softmax(scores, axis=-1)
    return torch.einsum("bhqk,bhkd->bhqd", p, v)
```

```cpp
#include <hip/hip_runtime.h>
#include <hip/hip_bf16.h>
#include <stdint.h>

typedef short bf16x8 __attribute__((ext_vector_type(8)));
typedef short s16x4 __attribute__((ext_vector_type(4)));
typedef float f32x16 __attribute__((ext_vector_type(16)));
typedef unsigned u32x4 __attribute__((ext_vector_type(4)));
typedef unsigned u32x2 __attribute__((ext_vector_type(2)));
typedef __attribute__((address_space(3))) const char* lds_cptr;
typedef short v4i16_t __attribute__((ext_vector_type(4)));

constexpr int NB = 32, NH = 32, S = 384, D = 192;
constexpr int ROWB = D * 2;
constexpr int TILEB = 32 * ROWB;
constexpr int NT = S / 32;
constexpr float CSC = 0.07216878364870322f * 1.4426950408889634f;

#define MFMA(a, b, c) __builtin_amdgcn_mfma_f32_32x32x16_bf16(a, b, c, 0, 0, 0)
#define SBAR() __builtin_amdgcn_sched_barrier(0)

typedef float f32x2 __attribute__((ext_vector_type(2)));
typedef __bf16 bf16x2 __attribute__((ext_vector_type(2)));
__device__ __forceinline__ unsigned cvtpk(float lo, float hi) { f32x2 v = {lo, hi}; bf16x2 b = __builtin_convertvector(v, bf16x2); return __builtin_bit_cast(unsigned, b); }
__device__ __forceinline__ void glds16(const void* g, unsigned lds_base) {
    unsigned sv; asm volatile("s_mov_b32 %0, m0\n\ts_mov_b32 m0, %2\n\ts_nop 0\n\tglobal_load_lds_dwordx4 %1, off\n\ts_mov_b32 m0, %0" : "=&s"(sv) : "v"(g), "s"(lds_base) : "memory"); }
__device__ __forceinline__ s16x4 vtr(lds_cptr p) { return __builtin_bit_cast(s16x4, __builtin_amdgcn_ds_read_tr16_b64_v4i16((__attribute__((address_space(3))) v4i16_t*)p)); }

__global__ __launch_bounds__(256, 2) void attn_v0(const unsigned short* __restrict__ Q, const unsigned short* __restrict__ K,
                                                  const unsigned short* __restrict__ V, unsigned short* __restrict__ O) {
    extern __shared__ __attribute__((aligned(16))) char lds[];
    const int tid = threadIdx.x, lane = tid & 63, r32 = lane & 31, hi = lane >> 5;
    const int wid = __builtin_amdgcn_readfirstlane(tid >> 6);
    const int bid = blockIdx.x;
    const int item = (bid & 7) * (NB * NH * 3 / 8) + (bid >> 3);
    const int head = item / 3, qb = item - head * 3;
    const size_t hoff = (size_t)head * S * D;
    const unsigned short* Kh = K + hoff; const unsigned short* Vh = V + hoff;
    const unsigned short* Qw = Q + hoff + (size_t)(qb * 128 + wid * 32) * D;
    unsigned short* Ow = O + hoff + (size_t)(qb * 128 + wid * 32) * D;
    const unsigned lds0 = (unsigned)(uintptr_t)lds;
    const int row7 = (lane >> 2) & 7, sub = lane >> 5, x = lane & 3;
    const int rowt = 8 * wid + row7;
    const int srcoff = rowt * ROWB + (sub * 4 + (x ^ ((rowt >> 2) & 3))) * 16;
    const char* ksrc = (const char*)Kh + srcoff; const char* vsrc = (const char*)Vh + srcoff;
    const unsigned kdst = lds0 + wid * 3072, vdst = lds0 + 3 * TILEB + wid * 3072;
    auto dma = [&](int t, int slot) {
        #pragma unroll
        for (int j = 0; j < 3; ++j) glds16(ksrc + t * TILEB + j * 128, (unsigned)__builtin_amdgcn_readfirstlane(kdst + slot * TILEB + j * 1024));
        #pragma unroll
        for (int j = 0; j < 3; ++j) glds16(vsrc + t * TILEB + j * 128, (unsigned)__builtin_amdgcn_readfirstlane(vdst + slot * TILEB + j * 1024));
    };
    dma(0, 0); dma(1, 1);
    bf16x8 qr[12];
    #pragma unroll
    for (int s = 0; s < 12; ++s) qr[s] = *reinterpret_cast<const bf16x8*>(&Qw[(size_t)r32 * D + s * 16 + hi * 8]);
    #pragma unroll
    for (int s = 0; s < 12; ++s) asm volatile("" : "+v"(qr[s]));
    const int kb0 = 3072 * (r32 >> 3) + 64 * (r32 & 7) + 16 * ((0 + hi) ^ ((r32 >> 2) & 3));
    const int kb1 = 3072 * (r32 >> 3) + 64 * (r32 & 7) + 16 * ((2 + hi) ^ ((r32 >> 2) & 3));
    const int blk = (lane >> 4) & 1, q4 = (lane & 15) >> 2, p = lane & 3;
    const int vb0 = 64 * (4 * hi + q4) + 16 * ((2 * blk + (p >> 1)) ^ (0 + hi)) + 8 * (p & 1);
    const int vb1 = 64 * (4 * hi + q4) + 16 * ((2 * blk + (p >> 1)) ^ (2 + hi)) + 8 * (p & 1) + 3072;
    f32x16 o[6];
    #pragma unroll
    for (int c = 0; c < 6; ++c) o[c] = f32x16{};
    float m = -1e30f, l = 0.f;
    int slot = 0, slot2 = 2;
    for (int t = 0; t < NT; ++t) {
        if (t + 1 < NT) asm volatile("s_waitcnt vmcnt(6) lgkmcnt(0)\n\ts_barrier" ::: "memory");
        else            asm volatile("s_waitcnt vmcnt(0) lgkmcnt(0)\n\ts_barrier" ::: "memory");
        if (t + 2 < NT) dma(t + 2, slot2);
        const lds_cptr kp = (lds_cptr)lds + slot * TILEB;
        const lds_cptr vp = (lds_cptr)lds + 3 * TILEB + slot * TILEB;
        f32x16 s = f32x16{};
        #pragma unroll
        for (int i = 0; i < 12; ++i) {
            bf16x8 kf = *(const __attribute__((address_space(3))) bf16x8*)(kp + ((i & 1) ? kb1 : kb0) + 512 * (i >> 1));
            s = MFMA(kf, qr[i], s);
        }
        float mx = s[0];
        #pragma unroll
        for (int r = 1; r < 16; ++r) mx = fmaxf(mx, s[r]);
        { auto rr = __builtin_amdgcn_permlane32_swap(__float_as_uint(mx), __float_as_uint(mx), false, false);
          mx = fmaxf(__uint_as_float(rr[0]), __uint_as_float(rr[1])); }
        const float mn = fmaxf(m, mx * CSC);
        const float alpha = __builtin_amdgcn_exp2f(m - mn);
        m = mn;
        float ps = 0.f;
        #pragma unroll
        for (int r = 0; r < 16; ++r) { s[r] = __builtin_amdgcn_exp2f(__builtin_fmaf(s[r], CSC, -mn)); ps += s[r]; }
        l = l * alpha + ps;
        if (__any(alpha < 1.f)) {
            #pragma unroll
            for (int c = 0; c < 6; ++c)
                #pragma unroll
                for (int r = 0; r < 16; ++r) o[c][r] *= alpha;
        }
        u32x4 pw0 = {cvtpk(s[0], s[1]), cvtpk(s[2], s[3]), cvtpk(s[4], s[5]), cvtpk(s[6], s[7])};
        u32x4 pw1 = {cvtpk(s[8], s[9]), cvtpk(s[10], s[11]), cvtpk(s[12], s[13]), cvtpk(s[14], s[15])};
        const bf16x8 p0 = __builtin_bit_cast(bf16x8, pw0), p1 = __builtin_bit_cast(bf16x8, pw1);
        #pragma unroll
        for (int c = 0; c < 6; ++c) {
            s16x4 a0 = vtr(vp + vb0 + 512 * c), a1 = vtr(vp + vb1 + 512 * c);
            s16x4 b0 = vtr(vp + vb0 + 6144 + 512 * c), b1 = vtr(vp + vb1 + 6144 + 512 * c);
            bf16x8 va = __builtin_shufflevector(a0, a1, 0, 1, 2, 3, 4, 5, 6, 7);
            bf16x8 vb = __builtin_shufflevector(b0, b1, 0, 1, 2, 3, 4, 5, 6, 7);
            o[c] = MFMA(va, p0, o[c]);
            o[c] = MFMA(vb, p1, o[c]);
        }
        slot = (slot == 2) ? 0 : slot + 1; slot2 = (slot2 == 2) ? 0 : slot2 + 1;
    }
    { auto rr = __builtin_amdgcn_permlane32_swap(__float_as_uint(l), __float_as_uint(l), false, false);
      l = __uint_as_float(rr[0]) + __uint_as_float(rr[1]); }
    const float il = 1.0f / l;
    char* orow = (char*)(Ow + (size_t)r32 * D);
    #pragma unroll
    for (int c = 0; c < 6; ++c) {
        #pragma unroll
        for (int g = 0; g < 4; g += 2) {
            unsigned a0 = cvtpk(o[c][4 * g + 0] * il, o[c][4 * g + 1] * il), a1 = cvtpk(o[c][4 * g + 2] * il, o[c][4 * g + 3] * il);
            unsigned b0 = cvtpk(o[c][4 * g + 4] * il, o[c][4 * g + 5] * il), b1 = cvtpk(o[c][4 * g + 6] * il, o[c][4 * g + 7] * il);
            auto r0 = __builtin_amdgcn_permlane32_swap(a0, b0, false, false);
            auto r1 = __builtin_amdgcn_permlane32_swap(a1, b1, false, false);
            u32x4 w = {r0[0], r1[0], r0[1], r1[1]};
            *(u32x4*)(orow + 64 * c + 16 * g + 16 * hi) = w;
        }
    }
}

extern "C" void kernel_launch(void* const* d_in, const int* in_sizes, int n_in, void* d_out, int out_size, void* d_ws, size_t ws_size, hipStream_t stream) {
    constexpr int LDSB = 6 * TILEB;
    (void)hipFuncSetAttribute((const void*)attn_v0, hipFuncAttributeMaxDynamicSharedMemorySize, LDSB);
    hipLaunchKernelGGL(attn_v0, dim3(NB * NH * 3), dim3(256), LDSB, stream, (const unsigned short*)d_in[0], (const unsigned short*)d_in[1],
                       (const unsigned short*)d_in[2], (unsigned short*)d_out);
}
```

```cpp
#include <hip/hip_runtime.h>
#include <stdint.h>

typedef short bf16x8 __attribute__((ext_vector_type(8)));
typedef float f32x16 __attribute__((ext_vector_type(16)));
typedef float f32x2 __attribute__((ext_vector_type(2)));
typedef __bf16 bf16x2 __attribute__((ext_vector_type(2)));
typedef unsigned u32x4 __attribute__((ext_vector_type(4)));
typedef unsigned u32x2 __attribute__((ext_vector_type(2)));

constexpr int SEQ = 384, DH = 192, NHEADS = 1024, NWG = 256;
constexpr float CSC = 0.07216878364870322f * 1.4426950408889634f;

__device__ __forceinline__ unsigned cvtpk(float lo, float hi) { f32x2 v = {lo, hi}; bf16x2 b = __builtin_convertvector(v, bf16x2); return __builtin_bit_cast(unsigned, b); }

__device__ __forceinline__ int group_row(int w, int n) {
    const int off = w == 0 ? 0 : w == 1 ? 88 : w == 2 ? 168 : 256;
    const int cnt = w == 1 ? 10 : 11;
    const int lo = w == 0 ? 344 : w == 1 ? 352 : w == 2 ? 368 : 376;
    return n < cnt ? off + 8 * n : lo + 8 * (n - cnt);
}
__device__ __forceinline__ void slow_block(const unsigned short* Qr, const unsigned short* Kh, const unsigned short* Vh, unsigned short* Or, int lane) {
    const int r32 = lane & 31, hi = lane >> 5;
    f32x16 o[6];
#pragma unroll
    for (int c = 0; c < 6; ++c) o[c] = f32x16{};
    float m = -INFINITY, l = 0.f;
    for (int t = 0; t < SEQ / 32; ++t) {
        f32x16 s = f32x16{};
#pragma unroll
        for (int i = 0; i < 12; ++i) {
            const bf16x8 kf = *reinterpret_cast<const bf16x8*>(Kh + (size_t)(32 * t + r32) * DH + 16 * i + 8 * hi);
            const bf16x8 qf = *reinterpret_cast<const bf16x8*>(Qr + 16 * i + 8 * hi);
            s = __builtin_amdgcn_mfma_f32_32x32x16_bf16(kf, qf, s, 0, 0, 0);
        }
        float mx = s[0];
#pragma unroll
        for (int r = 1; r < 16; ++r) mx = fmaxf(mx, s[r]);
        mx = fmaxf(mx, __shfl_xor(mx, 32));
        const float mn = fmaxf(m, mx);
        const float alpha = __builtin_amdgcn_exp2f((m - mn) * CSC);
        m = mn;
        float ps = 0.f;
#pragma unroll
        for (int r = 0; r < 16; ++r) { s[r] = __builtin_amdgcn_exp2f((s[r] - mn) * CSC); ps += s[r]; }
        l = l * alpha + ps;
#pragma unroll
        for (int c = 0; c < 6; ++c)
#pragma unroll
            for (int r = 0; r < 16; ++r) o[c][r] *= alpha;
#pragma unroll
        for (int ks = 0; ks < 2; ++ks) {
            const u32x4 pw = {cvtpk(s[8 * ks], s[8 * ks + 1]), cvtpk(s[8 * ks + 2], s[8 * ks + 3]), cvtpk(s[8 * ks + 4], s[8 * ks + 5]), cvtpk(s[8 * ks + 6], s[8 * ks + 7])};
            const bf16x8 pf = __builtin_bit_cast(bf16x8, pw);
#pragma unroll
            for (int c = 0; c < 6; ++c) {
                bf16x8 vf;
#pragma unroll
                for (int j = 0; j < 8; ++j) vf[j] = (short)Vh[(size_t)(32 * t + 16 * ks + 8 * (j >> 2) + 4 * hi + (j & 3)) * DH + 32 * c + r32];
                o[c] = __builtin_amdgcn_mfma_f32_32x32x16_bf16(vf, pf, o[c], 0, 0, 0);
            }
        }
    }
    l += __shfl_xor(l, 32);
    const float il = 1.0f / l;
#pragma unroll
    for (int c = 0; c < 6; ++c)
#pragma unroll
        for (int g = 0; g < 4; ++g) {
            const u32x2 w = {cvtpk(o[c][4 * g] * il, o[c][4 * g + 1] * il), cvtpk(o[c][4 * g + 2] * il, o[c][4 * g + 3] * il)};
            *reinterpret_cast<u32x2*>(Or + 32 * c + 8 * g + 4 * hi) = w;
        }
}

__global__ __launch_bounds__(256, 1) void attn_fwd(const unsigned short* __restrict__ Q, const unsigned short* __restrict__ K,
                                                   const unsigned short* __restrict__ V, unsigned short* __restrict__ O
                                                   ) {
    extern __shared__ __attribute__((aligned(16))) char lds[];
    const int wid = __builtin_amdgcn_readfirstlane(threadIdx.x >> 6);
    const unsigned bid = blockIdx.x;
    const size_t h0 = (size_t)bid * SEQ * DH;
    const unsigned ldsb = (unsigned)(uintptr_t)lds;
    unsigned flag;
    asm volatile(
    "s_mov_b64 s[36:37], %1\n\t"
    "s_and_b32 s37, s37, 0xffff\n\t"
    "s_mov_b32 s38, 147456\n\t"
    "s_mov_b32 s39, 0x20000\n\t"
    "s_mov_b64 s[40:41], %2\n\t"
    "s_and_b32 s41, s41, 0xffff\n\t"
    "s_mov_b32 s42, 147456\n\t"
    "s_mov_b32 s43, 0x20000\n\t"
    "s_mov_b64 s[44:45], %3\n\t"
    "s_and_b32 s45, s45, 0xffff\n\t"
    "s_mov_b32 s46, 147456\n\t"
    "s_mov_b32 s47, 0x20000\n\t"
    "s_mov_b32 s74, %6\n\t"
    "s_mul_i32 s52, %5, 3072\n\t"
    "s_add_u32 s52, s52, s74\n\t"
    "v_mbcnt_lo_u32_b32 v16, -1, 0\n\t"
    "v_mbcnt_hi_u32_b32 v16, -1, v16\n\t"
    "v_bfe_u32 v17, v16, 2, 3\n\t"
    "s_lshl_b32 s73, %5, 3\n\t"
    "s_lshr_b32 s73, 0x20150b00, s73\n\t"
    "s_and_b32 s73, s73, 255\n\t"
    "s_mul_i32 s72, s73, 8\n\t"
    "s_sub_u32 s36, s36, 98304\n\t"
    "s_subb_u32 s37, s37, 0\n\t"
    "s_mov_b32 s38, 245760\n\t"
    "v_add_u32 v17, s72, v17\n\t"
    "s_and_b32 s72, %5, 1\n\t"
    "s_lshl_b32 s72, s72, 1\n\t"
    "v_and_b32 v19, 3, v16\n\t"
    "v_xor_b32 v18, s72, v19\n\t"
    "v_mul_u32_u24 v1, 384, v17\n\t"
    "v_lshl_add_u32 v1, v18, 4, v1\n\t"
    "v_lshrrev_b32 v18, 5, v16\n\t"
    "v_lshl_add_u32 v1, v18, 6, v1\n\t"
    "s_getreg_b32 s72, hwreg(HW_REG_XCC_ID, 0, 1)\n\t"
    "s_xor_b32 s72, s72, 1\n\t"
    "s_bfe_u32 s73, %7, 0x10003\n\t"
    "s_lshl_b32 s72, s72, 1\n\t"
    "s_or_b32 s72, s72, s73\n\t"
    "s_lshl_b32 s72, s72, 3\n\t"
    "s_lshr_b32 s72, 0x64640000, s72\n\t"
    "s_and_b32 s72, s72, 255\n\t"
    "s_lshl_b32 s72, s72, 1\n\t"
    "s_cbranch_scc0 Lgo_%=\n\t"
    "s_memrealtime s[66:67]\n\t"
    "s_waitcnt lgkmcnt(0)\n\t"
    "s_add_u32 s72, s72, s66\n\t"
    "Lstag_%=:\n\t"
    "s_memrealtime s[66:67]\n\t"
    "s_waitcnt lgkmcnt(0)\n\t"
    "s_sub_u32 s73, s66, s72\n\t"
    "s_cmp_lt_i32 s73, 0\n\t"
    "s_cbranch_scc1 Lstag_%=\n\t"
    "Lgo_%=:\n\t"
    "s_lshl_b32 s73, %5, 3\n\t"
    "s_lshr_b32 s73, 0x30d1620, s73\n\t"
    "s_and_b32 s73, s73, 255\n\t"
    "s_mul_i32 s68, s73, 3072\n\t"
    "s_lshl_b32 s73, %5, 3\n\t"
    "s_lshr_b32 s73, 0xe18212b, s73\n\t"
    "s_and_b32 s73, s73, 255\n\t"
    "s_mul_i32 s69, s73, 3072\n\t"
    "s_lshl_b32 s73, %5, 3\n\t"
    "s_lshr_b32 s73, 0x18222b35, s73\n\t"
    "s_and_b32 s73, s73, 255\n\t"
    "s_mul_i32 s70, s73, 3072\n\t"
    "s_lshl_b32 s73, %5, 3\n\t"
    "s_lshr_b32 s73, 0x232d3640, s73\n\t"
    "s_and_b32 s73, s73, 255\n\t"
    "s_mul_i32 s71, s73, 3072\n\t"
    "s_mov_b32 s53, s68\n\t"
    "s_add_u32 m0, s52, 24576\n\t"
    "s_nop 0\n\t"
    "buffer_load_dwordx4 v1, s[36:39], s53 offen nt lds\n\t"
    "s_addk_i32 m0, 896\n\t"
    "s_nop 0\n\t"
    "buffer_load_dwordx4 v1, s[36:39], s53 offen offset:128 nt lds\n\t"
    "s_addk_i32 m0, 896\n\t"
    "s_nop 0\n\t"
    "buffer_load_dwordx4 v1, s[36:39], s53 offen offset:256 nt lds\n\t"
    "s_mov_b32 s53, s69\n\t"
    "s_add_u32 m0, s52, 49152\n\t"
    "s_nop 0\n\t"
    "buffer_load_dwordx4 v1, s[36:39], s53 offen nt lds\n\t"
    "s_addk_i32 m0, 896\n\t"
    "s_nop 0\n\t"
    "buffer_load_dwordx4 v1, s[36:39], s53 offen offset:128 nt lds\n\t"
    "s_addk_i32 m0, 896\n\t"
    "s_nop 0\n\t"
    "buffer_load_dwordx4 v1, s[36:39], s53 offen offset:256 nt lds\n\t"
    "s_mov_b32 s53, s70\n\t"
    "s_add_u32 m0, s52, 61440\n\t"
    "s_nop 0\n\t"
    "buffer_load_dwordx4 v1, s[36:39], s53 offen nt lds\n\t"
    "s_addk_i32 m0, 896\n\t"
    "s_nop 0\n\t"
    "buffer_load_dwordx4 v1, s[36:39], s53 offen offset:128 nt lds\n\t"
    "s_addk_i32 m0, 896\n\t"
    "s_nop 0\n\t"
    "buffer_load_dwordx4 v1, s[36:39], s53 offen offset:256 nt lds\n\t"
    "s_mov_b32 s53, s71\n\t"
    "s_add_u32 m0, s52, 73728\n\t"
    "s_nop 0\n\t"
    "buffer_load_dwordx4 v1, s[36:39], s53 offen nt lds\n\t"
    "s_addk_i32 m0, 896\n\t"
    "s_nop 0\n\t"
    "buffer_load_dwordx4 v1, s[36:39], s53 offen offset:128 nt lds\n\t"
    "s_addk_i32 m0, 896\n\t"
    "s_nop 0\n\t"
    "buffer_load_dwordx4 v1, s[36:39], s53 offen offset:256 nt lds\n\t"
    "s_getpc_b64 s[48:49]\n\t"
    "Lpc_%=:\n\t"
    "s_add_u32 s48, s48, Lloop_%=-Lpc_%=\n\t"
    "s_addc_u32 s49, s49, 0\n\t"
    "s_and_b32 s49, s49, 0xffff\n\t"
    "s_mov_b32 s50, Lloopend_%=-Lloop_%=\n\t"
    "s_mov_b32 s51, 0x20000\n\t"
    "v_lshlrev_b32 v20, 4, v16\n\t"
    "s_bfe_u32 s72, %7, 0x20003\n\t"
    "s_lshl_b32 s72, s72, 2\n\t"
    "s_add_u32 s72, s72, %5\n\t"
    "s_mul_i32 s72, s72, 2304\n\t"
    "buffer_load_dwordx4 v[64:67], v20, s[48:51], s72 offen offset:0\n\t"
    "buffer_load_dwordx4 v[68:71], v20, s[48:51], s72 offen offset:768\n\t"
    "buffer_load_dwordx4 v[72:75], v20, s[48:51], s72 offen offset:1536\n\t"
    "s_lshl_b32 s73, %5, 3\n\t"
    "s_lshr_b32 s73, 0x7111a24, s73\n\t"
    "s_and_b32 s73, s73, 255\n\t"
    "s_mul_i32 s81, s73, 3072\n\t"
    "s_lshl_b32 s73, %5, 3\n\t"
    "s_lshr_b32 s73, 0x121c252f, s73\n\t"
    "s_and_b32 s73, s73, 255\n\t"
    "s_mul_i32 s82, s73, 3072\n\t"
    "s_lshl_b32 s73, %5, 3\n\t"
    "s_lshr_b32 s73, 0x1c262f39, s73\n\t"
    "s_and_b32 s73, s73, 255\n\t"
    "s_mul_i32 s83, s73, 3072\n\t"
    "s_lshl_b32 s73, %5, 3\n\t"
    "s_lshr_b32 s73, 0x27313a44, s73\n\t"
    "s_and_b32 s73, s73, 255\n\t"
    "s_mul_i32 s89, s73, 3072\n\t"
    "s_lshl_b32 s73, %5, 3\n\t"
    "s_lshr_b32 s73, 0x2b151e28, s73\n\t"
    "s_and_b32 s73, s73, 255\n\t"
    "s_mul_i32 s90, s73, 3072\n\t"
    "s_lshl_b32 s73, %5, 3\n\t"
    "s_lshr_b32 s73, 0x2d372933, s73\n\t"
    "s_and_b32 s73, s73, 255\n\t"
    "s_mul_i32 s91, s73, 3072\n\t"
    "s_lshl_b32 s73, %5, 3\n\t"
    "s_lshr_b32 s73, 0x2e2a333d, s73\n\t"
    "s_and_b32 s73, s73, 255\n\t"
    "s_mul_i32 s92, s73, 3072\n\t"
    "s_lshl_b32 s73, %5, 3\n\t"
    "s_lshr_b32 s73, 0x2f353e48, s73\n\t"
    "s_and_b32 s73, s73, 255\n\t"
    "s_mul_i32 s93, s73, 3072\n\t"
    "s_mov_b32 s53, s81\n\t"
    "s_add_u32 m0, s52, 86016\n\t"
    "s_nop 0\n\t"
    "buffer_load_dwordx4 v1, s[36:39], s53 offen nt lds\n\t"
    "s_addk_i32 m0, 896\n\t"
    "s_nop 0\n\t"
    "buffer_load_dwordx4 v1, s[36:39], s53 offen offset:128 nt lds\n\t"
    "s_addk_i32 m0, 896\n\t"
    "s_nop 0\n\t"
    "buffer_load_dwordx4 v1, s[36:39], s53 offen offset:256 nt lds\n\t"
    "s_mov_b32 s53, s82\n\t"
    "s_add_u32 m0, s52, 98304\n\t"
    "s_nop 0\n\t"
    "buffer_load_dwordx4 v1, s[36:39], s53 offen nt lds\n\t"
    "s_addk_i32 m0, 896\n\t"
    "s_nop 0\n\t"
    "buffer_load_dwordx4 v1, s[36:39], s53 offen offset:128 nt lds\n\t"
    "s_addk_i32 m0, 896\n\t"
    "s_nop 0\n\t"
    "buffer_load_dwordx4 v1, s[36:39], s53 offen offset:256 nt lds\n\t"
    "s_mov_b32 s53, s83\n\t"
    "s_add_u32 m0, s52, 110592\n\t"
    "s_nop 0\n\t"
    "buffer_load_dwordx4 v1, s[36:39], s53 offen nt lds\n\t"
    "s_addk_i32 m0, 896\n\t"
    "s_nop 0\n\t"
    "buffer_load_dwordx4 v1, s[36:39], s53 offen offset:128 nt lds\n\t"
    "s_addk_i32 m0, 896\n\t"
    "s_nop 0\n\t"
    "buffer_load_dwordx4 v1, s[36:39], s53 offen offset:256 nt lds\n\t"
    "s_mov_b32 s53, s89\n\t"
    "s_add_u32 m0, s52, 122880\n\t"
    "s_nop 0\n\t"
    "buffer_load_dwordx4 v1, s[36:39], s53 offen nt lds\n\t"
    "s_addk_i32 m0, 896\n\t"
    "s_nop 0\n\t"
    "buffer_load_dwordx4 v1, s[36:39], s53 offen offset:128 nt lds\n\t"
    "s_addk_i32 m0, 896\n\t"
    "s_nop 0\n\t"
    "buffer_load_dwordx4 v1, s[36:39], s53 offen offset:256 nt lds\n\t"
    "s_mov_b32 s53, 0\n\t"
    "s_add_u32 m0, s52, 0\n\t"
    "s_nop 0\n\t"
    "buffer_load_dwordx4 v1, s[40:43], s53 offen nt lds\n\t"
    "s_addk_i32 m0, 896\n\t"
    "s_nop 0\n\t"
    "buffer_load_dwordx4 v1, s[40:43], s53 offen offset:128 nt lds\n\t"
    "s_addk_i32 m0, 896\n\t"
    "s_nop 0\n\t"
    "buffer_load_dwordx4 v1, s[40:43], s53 offen offset:256 nt lds\n\t"
    "s_mov_b32 s53, s90\n\t"
    "s_add_u32 m0, s52, 135168\n\t"
    "s_nop 0\n\t"
    "buffer_load_dwordx4 v1, s[36:39], s53 offen nt lds\n\t"
    "s_addk_i32 m0, 896\n\t"
    "s_nop 0\n\t"
    "buffer_load_dwordx4 v1, s[36:39], s53 offen offset:128 nt lds\n\t"
    "s_addk_i32 m0, 896\n\t"
    "s_nop 0\n\t"
    "buffer_load_dwordx4 v1, s[36:39], s53 offen offset:256 nt lds\n\t"
    "s_mov_b32 s53, s91\n\t"
    "s_add_u32 m0, s52, 147456\n\t"
    "s_nop 0\n\t"
    "buffer_load_dwordx4 v1, s[36:39], s53 offen nt lds\n\t"
    "s_addk_i32 m0, 896\n\t"
    "s_nop 0\n\t"
    "buffer_load_dwordx4 v1, s[36:39], s53 offen offset:128 nt lds\n\t"
    "s_addk_i32 m0, 896\n\t"
    "s_nop 0\n\t"
    "buffer_load_dwordx4 v1, s[36:39], s53 offen offset:256 nt lds\n\t"
    "s_mov_b32 s53, s92\n\t"
    "s_add_u32 m0, s52, 36864\n\t"
    "s_nop 0\n\t"
    "buffer_load_dwordx4 v1, s[36:39], s53 offen nt lds\n\t"
    "s_addk_i32 m0, 896\n\t"
    "s_nop 0\n\t"
    "buffer_load_dwordx4 v1, s[36:39], s53 offen offset:128 nt lds\n\t"
    "s_addk_i32 m0, 896\n\t"
    "s_nop 0\n\t"
    "buffer_load_dwordx4 v1, s[36:39], s53 offen offset:256 nt lds\n\t"
    "s_mov_b64 s[48:49], %4\n\t"
    "s_mov_b32 s51, 0x20000\n\t"
    "s_sub_u32 s48, s48, 37748736\n\t"
    "s_subb_u32 s49, s49, 0\n\t"
    "s_and_b32 s49, s49, 0xffff\n\t"
    "s_mov_b32 s50, 0\n\t"
    "s_mul_i32 s64, %5, 9216\n\t"
    "s_add_u32 s64, s64, s74\n\t"
    "s_add_u32 s64, s64, 122880\n\t"
    "s_lshl_b32 s73, %5, 3\n\t"
    "s_lshr_b32 s73, 0x20150b00, s73\n\t"
    "s_and_b32 s73, s73, 255\n\t"
    "s_mul_i32 s65, s73, 3072\n\t"
    "s_mov_b32 s55, 0x3dd53b95\n\t"
    "s_mov_b32 s56, 0xbdd53b95\n\t"
    "s_mov_b32 s57, 0x53800000\n\t"
    "s_mov_b32 s58, 0\n\t"
    "s_mov_b32 s59, 1\n\t"
    "s_mov_b32 s60, 0\n\t"
    "s_lshl_b32 s72, %5, 2\n\t"
    "s_lshr_b32 s72, 0x4703, s72\n\t"
    "s_and_b32 s72, s72, 15\n\t"
    "s_mul_i32 s61, s72, 12288\n\t"
    "s_lshl_b32 s72, %5, 2\n\t"
    "s_lshr_b32 s72, 0x2cba, s72\n\t"
    "s_and_b32 s72, s72, 15\n\t"
    "s_mul_i32 s62, s72, 12288\n\t"
    "s_lshl_b32 s72, %5, 2\n\t"
    "s_lshr_b32 s72, 0x4298, s72\n\t"
    "s_and_b32 s72, s72, 15\n\t"
    "s_mul_i32 s63, s72, 12288\n\t"
    "v_bfe_u32 v20, v16, 2, 1\n\t"
    "v_bfe_u32 v21, v16, 3, 1\n\t"
    "v_lshrrev_b32 v22, 4, v16\n\t"
    "v_and_b32 v23, 3, v16\n\t"
    "v_bfe_u32 v24, v16, 4, 1\n\t"
    "v_lshrrev_b32 v25, 5, v16\n\t"
    "v_bfe_u32 v26, v16, 2, 2\n\t"
    "v_mul_u32_u24 v17, 3072, v20\n\t"
    "v_lshl_add_u32 v18, v21, 2, v23\n\t"
    "v_lshl_add_u32 v17, v18, 6, v17\n\t"
    "v_lshlrev_b32 v18, 1, v20\n\t"
    "v_xor_b32 v18, v18, v22\n\t"
    "v_lshl_add_u32 v17, v18, 4, v17\n\t"
    "v_add_u32 v17, s74, v17\n\t"
    "v_mov_b32 v0, v17\n\t"
    "v_mul_u32_u24 v18, 3072, v24\n\t"
    "v_lshl_add_u32 v19, v25, 2, v26\n\t"
    "v_lshl_add_u32 v18, v19, 6, v18\n\t"
    "v_bfe_u32 v19, v16, 1, 1\n\t"
    "v_lshl_add_u32 v19, v24, 1, v19\n\t"
    "v_lshl_add_u32 v18, v19, 4, v18\n\t"
    "v_and_b32 v19, 1, v16\n\t"
    "v_lshl_add_u32 v18, v19, 3, v18\n\t"
    "v_add_u32 v18, s74, v18\n\t"
    "v_add_u32 v18, 36864, v18\n\t"
    "v_xor_b32 v59, 32, v18\n\t"
    "v_lshl_add_u32 v18, v21, 2, v23\n\t"
    "v_lshl_add_u32 v18, v20, 3, v18\n\t"
    "v_mul_u32_u24 v19, 576, v25\n\t"
    "v_lshl_add_u32 v18, v18, 4, v19\n\t"
    "v_lshl_add_u32 v18, v24, 3, v18\n\t"
    "v_xor_b32 v19, 1, v20\n\t"
    "v_mul_u32_u24 v19, 2304, v19\n\t"
    "v_mul_u32_u24 v27, 576, v23\n\t"
    "v_add_u32 v19, v19, v27\n\t"
    "v_lshrrev_b32 v27, 3, v16\n\t"
    "v_lshl_add_u32 v19, v27, 4, v19\n\t"
    "v_lshl_add_u32 v62, v19, 16, v18\n\t"
    "v_mul_u32_u24 v18, 384, v27\n\t"
    "v_and_b32 v19, 7, v16\n\t"
    "v_lshl_add_u32 v18, v19, 4, v18\n\t"
    "v_add_u32 v63, s65, v18\n\t"
    "s_lshl_b32 s73, %5, 3\n\t"
    "s_lshr_b32 s73, 0xa0a210a, s73\n\t"
    "s_and_b32 s73, s73, 255\n\t"
    "s_mul_i32 s65, s73, 3072\n\t"
    "s_lshl_b32 s73, %5, 3\n\t"
    "s_lshr_b32 s73, 0xf19222b, s73\n\t"
    "s_and_b32 s73, s73, 255\n\t"
    "s_mul_i32 s75, s73, 3072\n\t"
    "v_and_b32 v14, 0xffff, v62\n\t"
    "v_lshrrev_b32 v15, 16, v62\n\t"
    "v_add_u32 v14, s64, v14\n\t"
    "v_add_u32 v15, s64, v15\n\t"
    "v_mov_b32 v3, v63\n\t"
    "v_mov_b32 v5, v62\n\t"
    "v_mov_b32 v2, v61\n\t"
    "v_mov_b32 v8, 0\n\t"
    "v_mov_b32 v9, 0\n\t"
    "v_mov_b32 v10, 0\n\t"
    "v_mov_b32 v11, 0\n\t"
    "v_mov_b32 v12, 0\n\t"
    "v_mov_b32 v13, 0\n\t"
    "s_waitcnt vmcnt(27)\n\t"
    "s_barrier\n\t"
    "s_lshl_b32 s72, %5, 2\n\t"
    "s_lshr_b32 s72, 0x6542, s72\n\t"
    "s_and_b32 s72, s72, 15\n\t"
    "s_mul_i32 s73, s72, 12288\n\t"
    "v_add_u32 v16, s73, v17\n\t"
    "ds_read_b128 a[0:3], v16 offset:0\n\t"
    "ds_read_b128 a[24:27], v16 offset:6144\n\t"
    "ds_read_b128 a[4:7], v16 offset:512\n\t"
    "ds_read_b128 a[28:31], v16 offset:6656\n\t"
    "ds_read_b128 a[8:11], v16 offset:1024\n\t"
    "ds_read_b128 a[32:35], v16 offset:7168\n\t"
    "ds_read_b128 a[12:15], v16 offset:1536\n\t"
    "ds_read_b128 a[36:39], v16 offset:7680\n\t"
    "ds_read_b128 a[16:19], v16 offset:2048\n\t"
    "ds_read_b128 a[40:43], v16 offset:8192\n\t"
    "ds_read_b128 a[20:23], v16 offset:2560\n\t"
    "ds_read_b128 a[44:47], v16 offset:8704\n\t"
    "s_waitcnt lgkmcnt(0)\n\t"
    "s_barrier\n\t"
    "s_mov_b32 s53, s93\n\t"
    "s_add_u32 m0, s52, 24576\n\t"
    "s_nop 0\n\t"
    "buffer_load_dwordx4 v1, s[36:39], s53 offen nt lds\n\t"
    "s_addk_i32 m0, 896\n\t"
    "s_nop 0\n\t"
    "buffer_load_dwordx4 v1, s[36:39], s53 offen offset:128 nt lds\n\t"
    "s_addk_i32 m0, 896\n\t"
    "s_nop 0\n\t"
    "buffer_load_dwordx4 v1, s[36:39], s53 offen offset:256 nt lds\n\t"
    "s_mov_b32 s53, 3072\n\t"
    "s_add_u32 m0, s52, 12288\n\t"
    "s_nop 0\n\t"
    "buffer_load_dwordx4 v1, s[40:43], s53 offen nt lds\n\t"
    "s_addk_i32 m0, 896\n\t"
    "s_nop 0\n\t"
    "buffer_load_dwordx4 v1, s[40:43], s53 offen offset:128 nt lds\n\t"
    "s_addk_i32 m0, 896\n\t"
    "s_nop 0\n\t"
    "buffer_load_dwordx4 v1, s[40:43], s53 offen offset:256 nt lds\n\t"
    "s_add_u32 s36, s36, 37748736\n\t"
    "s_addc_u32 s37, s37, 0\n\t"
    "s_mov_b32 s38, 245760\n\t"
    "s_waitcnt vmcnt(18)\n\t"
    "s_barrier\n\t"
    "s_lshl_b32 s72, %5, 2\n\t"
    "s_lshr_b32 s72, 0xa987, s72\n\t"
    "s_and_b32 s72, s72, 15\n\t"
    "s_mul_i32 s73, s72, 12288\n\t"
    "v_add_u32 v16, s73, v17\n\t"
    "ds_read_b128 a[48:51], v16 offset:0\n\t"
    "ds_read_b128 a[72:75], v16 offset:6144\n\t"
    "ds_read_b128 a[52:55], v16 offset:512\n\t"
    "ds_read_b128 a[76:79], v16 offset:6656\n\t"
    "ds_read_b128 a[56:59], v16 offset:1024\n\t"
    "ds_read_b128 a[80:83], v16 offset:7168\n\t"
    "ds_read_b128 a[60:63], v16 offset:1536\n\t"
    "ds_read_b128 a[84:87], v16 offset:7680\n\t"
    "ds_read_b128 a[64:67], v16 offset:2048\n\t"
    "ds_read_b128 a[88:91], v16 offset:8192\n\t"
    "ds_read_b128 a[68:71], v16 offset:2560\n\t"
    "ds_read_b128 a[92:95], v16 offset:8704\n\t"
    "s_waitcnt vmcnt(3)\n\t"
    "s_barrier\n\t"
    "s_lshl_b32 s72, %5, 2\n\t"
    "s_lshr_b32 s72, 0x23cb, s72\n\t"
    "s_and_b32 s72, s72, 15\n\t"
    "s_mul_i32 s73, s72, 12288\n\t"
    "v_add_u32 v16, s73, v17\n\t"
    "ds_read_b128 a[96:99], v16 offset:0\n\t"
    "ds_read_b128 a[120:123], v16 offset:6144\n\t"
    "ds_read_b128 a[100:103], v16 offset:512\n\t"
    "ds_read_b128 a[124:127], v16 offset:6656\n\t"
    "ds_read_b128 a[104:107], v16 offset:1024\n\t"
    "ds_read_b128 a[128:131], v16 offset:7168\n\t"
    "ds_read_b128 a[108:111], v16 offset:1536\n\t"
    "ds_read_b128 a[132:135], v16 offset:7680\n\t"
    "ds_read_b128 a[112:115], v16 offset:2048\n\t"
    "ds_read_b128 a[136:139], v16 offset:8192\n\t"
    "ds_read_b128 a[116:119], v16 offset:2560\n\t"
    "ds_read_b128 a[140:143], v16 offset:8704\n\t"
    "s_waitcnt vmcnt(0) lgkmcnt(0)\n\t"
    "s_barrier\n\t"
    "s_mov_b32 s53, 6144\n\t"
    "s_add_u32 m0, s52, 36864\n\t"
    "s_nop 0\n\t"
    "buffer_load_dwordx4 v1, s[40:43], s53 offen nt lds\n\t"
    "s_addk_i32 m0, 896\n\t"
    "s_nop 0\n\t"
    "buffer_load_dwordx4 v1, s[40:43], s53 offen offset:128 nt lds\n\t"
    "s_addk_i32 m0, 896\n\t"
    "s_nop 0\n\t"
    "buffer_load_dwordx4 v1, s[40:43], s53 offen offset:256 nt lds\n\t"
    "ds_read_b128 a[240:243], v0 offset:0\n\t"
    "ds_read_b128 a[244:247], v0 offset:512\n\t"
    "ds_read_b128 a[248:251], v0 offset:1024\n\t"
    "ds_read_b128 a[252:255], v0 offset:1536\n\t"
    "s_waitcnt lgkmcnt(2)\n\t"
    ".p2align 6\n\t"
    "Lloop_%=:\n\t"
    "v_mfma_f32_16x16x32_bf16 v[64:67], a[240:243], a[0:3], 0\n\t"
    "s_mov_b32 s53, 9216\n\t"
    "v_mov_b32 v62, v5\n\t"
    "v_mov_b32 v63, v3\n\t"
    "v_mfma_f32_16x16x32_bf16 v[64:67], a[244:247], a[4:7], v[64:67]\n\t"
    "s_add_u32 m0, s52, 49152\n\t"
    "ds_write_b64 v14, v[40:41] offset:0\n\t"
    "v_mul_f32 v16, v16, v8\n\t"
    "v_mul_f32 v17, v17, v8\n\t"
    "v_mfma_f32_16x16x32_bf16 v[68:71], a[240:243], a[24:27], 0\n\t"
    "v_mul_f32 v18, v18, v8\n\t"
    "v_mul_f32 v19, v19, v8\n\t"
    "v_mfma_f32_16x16x32_bf16 v[68:71], a[244:247], a[28:31], v[68:71]\n\t"
    "buffer_load_dwordx4 v1, s[40:43], s53 offen nt lds\n\t"
    "v_cvt_pk_bf16_f32 v16, v16, v17\n\t"
    "v_cvt_pk_bf16_f32 v17, v18, v19\n\t"
    "v_mfma_f32_16x16x32_bf16 v[72:75], a[240:243], a[48:51], 0\n\t"
    "s_addk_i32 m0, 896\n\t"
    "v_mul_f32 v20, v20, v9\n\t"
    "v_mul_f32 v21, v21, v9\n\t"
    "v_mfma_f32_16x16x32_bf16 v[72:75], a[244:247], a[52:55], v[72:75]\n\t"
    "ds_write_b64 v14, v[44:45] offset:256\n\t"
    "v_mul_f32 v22, v22, v9\n\t"
    "v_mul_f32 v23, v23, v9\n\t"
    "v_mfma_f32_16x16x32_bf16 v[76:79], a[240:243], a[72:75], 0\n\t"
    "v_cvt_pk_bf16_f32 v20, v20, v21\n\t"
    "v_cvt_pk_bf16_f32 v21, v22, v23\n\t"
    "v_mfma_f32_16x16x32_bf16 v[76:79], a[244:247], a[76:79], v[76:79]\n\t"
    "ds_write_b64 v14, v[16:17] offset:1152\n\t"
    "v_mul_f32 v24, v24, v10\n\t"
    "v_mul_f32 v25, v25, v10\n\t"
    "v_mfma_f32_16x16x32_bf16 v[80:83], a[240:243], a[96:99], 0\n\t"
    "v_mul_f32 v26, v26, v10\n\t"
    "v_mul_f32 v27, v27, v10\n\t"
    "v_mfma_f32_16x16x32_bf16 v[80:83], a[244:247], a[100:103], v[80:83]\n\t"
    "v_cvt_pk_bf16_f32 v24, v24, v25\n\t"
    "v_cvt_pk_bf16_f32 v25, v26, v27\n\t"
    "v_mul_f32 v28, v28, v11\n\t"
    "v_mfma_f32_16x16x32_bf16 v[84:87], a[240:243], a[120:123], 0\n\t"
    "v_mul_f32 v29, v29, v11\n\t"
    "v_mul_f32 v30, v30, v11\n\t"
    "v_mfma_f32_16x16x32_bf16 v[84:87], a[244:247], a[124:127], v[84:87]\n\t"
    "s_waitcnt lgkmcnt(3)\n\t"
    "ds_read_b128 a[240:243], v0 offset:2048\n\t"
    "ds_read_b128 a[244:247], v0 offset:2560\n\t"
    "v_mul_f32 v31, v31, v11\n\t"
    "v_cvt_pk_bf16_f32 v28, v28, v29\n\t"
    "v_mfma_f32_16x16x32_bf16 v[64:67], a[248:251], a[8:11], v[64:67]\n\t"
    "v_cvt_pk_bf16_f32 v29, v30, v31\n\t"
    "v_mul_f32 v32, v32, v12\n\t"
    "v_mfma_f32_16x16x32_bf16 v[64:67], a[252:255], a[12:15], v[64:67]\n\t"
    "buffer_load_dwordx4 v1, s[40:43], s53 offen offset:128 nt lds\n\t"
    "v_mul_f32 v33, v33, v12\n\t"
    "v_mul_f32 v34, v34, v12\n\t"
    "v_mfma_f32_16x16x32_bf16 v[68:71], a[248:251], a[32:35], v[68:71]\n\t"
    "s_addk_i32 m0, 896\n\t"
    "v_mul_f32 v35, v35, v12\n\t"
    "v_cvt_pk_bf16_f32 v32, v32, v33\n\t"
    "v_mfma_f32_16x16x32_bf16 v[68:71], a[252:255], a[36:39], v[68:71]\n\t"
    "ds_write_b64 v14, v[20:21] offset:1408\n\t"
    "v_cvt_pk_bf16_f32 v33, v34, v35\n\t"
    "v_mul_f32 v36, v36, v13\n\t"
    "v_mfma_f32_16x16x32_bf16 v[72:75], a[248:251], a[56:59], v[72:75]\n\t"
    "ds_read_b128 v[208:211], v15 offset:0\n\t"
    "ds_read_b128 v[212:215], v15 offset:128\n\t"
    "v_mfma_f32_16x16x32_bf16 v[72:75], a[252:255], a[60:63], v[72:75]\n\t"
    "ds_read_b128 v[216:219], v15 offset:256\n\t"
    "ds_read_b128 v[220:223], v15 offset:384\n\t"
    "v_mul_f32 v37, v37, v13\n\t"
    "v_mul_f32 v38, v38, v13\n\t"
    "v_mul_f32 v39, v39, v13\n\t"
    "v_cvt_pk_bf16_f32 v36, v36, v37\n\t"
    "v_cvt_pk_bf16_f32 v37, v38, v39\n\t"
    "v_mfma_f32_16x16x32_bf16 v[76:79], a[248:251], a[80:83], v[76:79]\n\t"
    "v_mov_b32 v41, 0x3f803f80\n\t"
    "v_mfma_f32_16x16x32_bf16 v[76:79], a[252:255], a[84:87], v[76:79]\n\t"
    "ds_write_b64 v14, v[48:49] offset:2304\n\t"
    "s_mov_b32 vcc_lo, 0x11111111\n\t"
    "s_mov_b32 vcc_hi, 0x11111111\n\t"
    "v_cndmask_b32 v40, 0, v41, vcc\n\t"
    "v_mfma_f32_16x16x32_bf16 v[80:83], a[248:251], a[104:107], v[80:83]\n\t"
    "s_lshl_b64 vcc, vcc, 1\n\t"
    "v_cndmask_b32 v44, 0, v41, vcc\n\t"
    "v_mov_b32 v41, v40\n\t"
    "v_mfma_f32_16x16x32_bf16 v[80:83], a[252:255], a[108:111], v[80:83]\n\t"
    "ds_write_b64 v14, v[52:53] offset:2560\n\t"
    "v_mov_b32 v45, v44\n\t"
    "v_mov_b32 v42, v40\n\t"
    "v_mfma_f32_16x16x32_bf16 v[84:87], a[248:251], a[128:131], v[84:87]\n\t"
    "v_mov_b32 v46, v44\n\t"
    "v_mov_b32 v43, v40\n\t"
    "v_mfma_f32_16x16x32_bf16 v[84:87], a[252:255], a[132:135], v[84:87]\n\t"
    "s_waitcnt lgkmcnt(7)\n\t"
    "ds_read_b128 a[248:251], v0 offset:6144\n\t"
    "ds_read_b128 a[252:255], v0 offset:6656\n\t"
    "v_mov_b32 v47, v44\n\t"
    "v_mfma_f32_16x16x32_bf16 v[64:67], a[240:243], a[16:19], v[64:67]\n\t"
    "v_mfma_f32_16x16x32_bf16 v[64:67], a[244:247], a[20:23], v[64:67]\n\t"
    "buffer_load_dwordx4 v1, s[40:43], s53 offen offset:256 nt lds\n\t"
    "v_mfma_f32_16x16x32_bf16 v[68:71], a[240:243], a[40:43], v[68:71]\n\t"
    "s_mov_b32 s53, 12288\n\t"
    "v_mfma_f32_16x16x32_bf16 v[68:71], a[244:247], a[44:47], v[68:71]\n\t"
    "s_add_u32 m0, s52, 24576\n\t"
    "ds_write_b64 v14, v[24:25] offset:3456\n\t"
    "v_mfma_f32_16x16x32_bf16 v[72:75], a[240:243], a[64:67], v[72:75]\n\t"
    "v_mfma_f32_16x16x32_bf16 v[72:75], a[244:247], a[68:71], v[72:75]\n\t"
    "ds_write_b64 v14, v[28:29] offset:3712\n\t"
    "v_max3_f32 v2, v64, v65, v66\n\t"
    "v_max_f32 v2, v2, v67\n\t"
    "v_mfma_f32_16x16x32_bf16 v[76:79], a[240:243], a[88:91], v[76:79]\n\t"
    "ds_read_b128 v[224:227], v15 offset:2304\n\t"
    "ds_read_b128 v[228:231], v15 offset:2432\n\t"
    "v_mfma_f32_16x16x32_bf16 v[76:79], a[244:247], a[92:95], v[76:79]\n\t"
    "ds_read_b128 v[232:235], v15 offset:2560\n\t"
    "ds_read_b128 v[236:239], v15 offset:2688\n\t"
    "v_max3_f32 v3, v68, v69, v70\n\t"
    "v_max_f32 v3, v3, v71\n\t"
    "v_max3_f32 v4, v72, v73, v74\n\t"
    "v_max_f32 v4, v4, v75\n\t"
    "v_mfma_f32_16x16x32_bf16 v[80:83], a[240:243], a[112:115], v[80:83]\n\t"
    "v_mfma_f32_16x16x32_bf16 v[80:83], a[244:247], a[116:119], v[80:83]\n\t"
    "ds_write_b64 v14, v[56:57] offset:4608\n\t"
    "v_max3_f32 v5, v76, v77, v78\n\t"
    "v_max_f32 v5, v5, v79\n\t"
    "v_mfma_f32_16x16x32_bf16 v[84:87], a[240:243], a[136:139], v[84:87]\n\t"
    "v_mfma_f32_16x16x32_bf16 v[84:87], a[244:247], a[140:143], v[84:87]\n\t"
    "s_waitcnt lgkmcnt(7)\n\t"
    "ds_read_b128 a[240:243], v0 offset:7168\n\t"
    "ds_read_b128 a[244:247], v0 offset:7680\n\t"
    "v_max3_f32 v6, v80, v81, v82\n\t"
    "v_max_f32 v6, v6, v83\n\t"
    "v_mfma_f32_16x16x32_bf16 v[16:19], a[248:251], a[0:3], 0\n\t"
    "v_mfma_f32_16x16x32_bf16 v[16:19], a[252:255], a[4:7], v[16:19]\n\t"
    "buffer_load_dwordx4 v1, s[40:43], s53 offen nt lds\n\t"
    "v_max3_f32 v7, v84, v85, v86\n\t"
    "v_max_f32 v7, v7, v87\n\t"
    "v_permlane16_swap_b32 v2, v3\n\t"
    "v_permlane16_swap_b32 v4, v5\n\t"
    "v_permlane16_swap_b32 v6, v7\n\t"
    "v_max_f32 v2, v2, v3\n\t"
    "v_max_f32 v4, v4, v5\n\t"
    "v_max_f32 v6, v6, v7\n\t"
    "v_mfma_f32_16x16x32_bf16 v[20:23], a[248:251], a[24:27], 0\n\t"
    "s_addk_i32 m0, 896\n\t"
    "v_mov_b32 v7, v6\n\t"
    "v_permlane32_swap_b32 v2, v4\n\t"
    "v_mfma_f32_16x16x32_bf16 v[20:23], a[252:255], a[28:31], v[20:23]\n\t"
    "ds_write_b64 v14, v[60:61] offset:4864\n\t"
    "v_permlane32_swap_b32 v6, v7\n\t"
    "v_max_f32 v2, v2, v4\n\t"
    "v_mfma_f32_16x16x32_bf16 v[24:27], a[248:251], a[48:51], 0\n\t"
    "v_max_f32 v6, v6, v7\n\t"
    "v_mul_f32 v2, s56, v2\n\t"
    "v_mfma_f32_16x16x32_bf16 v[24:27], a[252:255], a[52:55], v[24:27]\n\t"
    "ds_write_b64 v14, v[32:33] offset:5760\n\t"
    "v_mul_f32 v6, s56, v6\n\t"
    "v_add_f32 v2, 0xc2040000, v2\n\t"
    "v_mfma_f32_16x16x32_bf16 v[28:31], a[248:251], a[72:75], 0\n\t"
    "v_add_f32 v6, 0xc2040000, v6\n\t"
    "v_mov_b32 v4, v2\n\t"
    "v_mfma_f32_16x16x32_bf16 v[28:31], a[252:255], a[76:79], v[28:31]\n\t"
    "ds_write_b64 v14, v[36:37] offset:6016\n\t"
    "v_mov_b32 v7, v6\n\t"
    "v_permlane32_swap_b32 v2, v4\n\t"
    "v_mfma_f32_16x16x32_bf16 v[32:35], a[248:251], a[96:99], 0\n\t"
    "ds_read_b128 v[240:243], v15 offset:4608\n\t"
    "ds_read_b128 v[244:247], v15 offset:4736\n\t"
    "v_mfma_f32_16x16x32_bf16 v[32:35], a[252:255], a[100:103], v[32:35]\n\t"
    "ds_read_b128 v[248:251], v15 offset:4864\n\t"
    "ds_read_b128 v[252:255], v15 offset:4992\n\t"
    "v_permlane16_swap_b32 v6, v7\n\t"
    "v_mov_b32 v3, v2\n\t"
    "v_mov_b32 v5, v4\n\t"
    "v_mfma_f32_16x16x32_bf16 v[36:39], a[248:251], a[120:123], 0\n\t"
    "s_mov_b32 s54, 0\n\t"
    "v_permlane16_swap_b32 v2, v3\n\t"
    "v_permlane16_swap_b32 v4, v5\n\t"
    "v_mfma_f32_16x16x32_bf16 v[36:39], a[252:255], a[124:127], v[36:39]\n\t"
    "s_waitcnt lgkmcnt(7)\n\t"
    "ds_read_b128 a[248:251], v0 offset:8192\n\t"
    "ds_read_b128 a[252:255], v0 offset:8704\n\t"
    "v_fma_f32 v64, v64, s55, v2\n\t"
    "v_fma_f32 v65, v65, s55, v2\n\t"
    "v_exp_f32 v64, v64\n\t"
    "v_mfma_f32_16x16x32_bf16 v[16:19], a[240:243], a[8:11], v[16:19]\n\t"
    "v_exp_f32 v65, v65\n\t"
    "v_mfma_f32_16x16x32_bf16 v[16:19], a[244:247], a[12:15], v[16:19]\n\t"
    "buffer_store_dwordx4 v[208:211], v63, s[48:51], s54 offen offset:256 sc1\n\t"
    "v_fma_f32 v66, v66, s55, v2\n\t"
    "v_fma_f32 v67, v67, s55, v2\n\t"
    "v_mfma_f32_16x16x32_bf16 v[20:23], a[240:243], a[32:35], v[20:23]\n\t"
    "v_exp_f32 v66, v66\n\t"
    "v_mfma_f32_16x16x32_bf16 v[20:23], a[244:247], a[36:39], v[20:23]\n\t"
    "v_exp_f32 v67, v67\n\t"
    "v_fma_f32 v68, v68, s55, v3\n\t"
    "v_fma_f32 v69, v69, s55, v3\n\t"
    "v_mfma_f32_16x16x32_bf16 v[24:27], a[240:243], a[56:59], v[24:27]\n\t"
    "v_exp_f32 v68, v68\n\t"
    "v_mfma_f32_16x16x32_bf16 v[24:27], a[244:247], a[60:63], v[24:27]\n\t"
    "v_cvt_pk_bf16_f32 v64, v64, v65\n\t"
    "v_cvt_pk_bf16_f32 v65, v66, v67\n\t"
    "v_mfma_f32_16x16x32_bf16 v[28:31], a[240:243], a[80:83], v[28:31]\n\t"
    "v_exp_f32 v69, v69\n\t"
    "v_mfma_f32_16x16x32_bf16 v[28:31], a[244:247], a[84:87], v[28:31]\n\t"
    "v_fma_f32 v70, v70, s55, v3\n\t"
    "v_fma_f32 v71, v71, s55, v3\n\t"
    "v_mfma_f32_16x16x32_bf16 v[32:35], a[240:243], a[104:107], v[32:35]\n\t"
    "v_exp_f32 v70, v70\n\t"
    "v_mfma_f32_16x16x32_bf16 v[32:35], a[244:247], a[108:111], v[32:35]\n\t"
    "buffer_load_dwordx4 v1, s[40:43], s53 offen offset:128 nt lds\n\t"
    "v_exp_f32 v71, v71\n\t"
    "v_fma_f32 v72, v72, s55, v4\n\t"
    "v_fma_f32 v73, v73, s55, v4\n\t"
    "v_mfma_f32_16x16x32_bf16 v[36:39], a[240:243], a[128:131], v[36:39]\n\t"
    "s_addk_i32 m0, 896\n\t"
    "v_exp_f32 v72, v72\n\t"
    "v_mfma_f32_16x16x32_bf16 v[36:39], a[244:247], a[132:135], v[36:39]\n\t"
    "s_waitcnt lgkmcnt(0)\n\t"
    "ds_read_b128 a[240:243], v0 offset:12288\n\t"
    "ds_read_b128 a[244:247], v0 offset:12800\n\t"
    "v_cvt_pk_bf16_f32 v68, v68, v69\n\t"
    "v_cvt_pk_bf16_f32 v69, v70, v71\n\t"
    "v_mfma_f32_16x16x32_bf16 v[16:19], a[248:251], a[16:19], v[16:19]\n\t"
    "v_exp_f32 v73, v73\n\t"
    "v_mfma_f32_16x16x32_bf16 v[16:19], a[252:255], a[20:23], v[16:19]\n\t"
    "v_fma_f32 v74, v74, s55, v4\n\t"
    "v_fma_f32 v75, v75, s55, v4\n\t"
    "v_mfma_f32_16x16x32_bf16 v[20:23], a[248:251], a[40:43], v[20:23]\n\t"
    "v_exp_f32 v74, v74\n\t"
    "v_mfma_f32_16x16x32_bf16 v[20:23], a[252:255], a[44:47], v[20:23]\n\t"
    "v_exp_f32 v75, v75\n\t"
    "v_mfma_f32_16x16x32_bf16 v[24:27], a[248:251], a[64:67], v[24:27]\n\t"
    "v_fma_f32 v76, v76, s55, v5\n\t"
    "v_fma_f32 v77, v77, s55, v5\n\t"
    "v_mfma_f32_16x16x32_bf16 v[24:27], a[252:255], a[68:71], v[24:27]\n\t"
    "v_exp_f32 v76, v76\n\t"
    "v_cvt_pk_bf16_f32 v72, v72, v73\n\t"
    "v_cvt_pk_bf16_f32 v73, v74, v75\n\t"
    "v_mfma_f32_16x16x32_bf16 v[28:31], a[248:251], a[88:91], v[28:31]\n\t"
    "v_exp_f32 v77, v77\n\t"
    "v_mfma_f32_16x16x32_bf16 v[28:31], a[252:255], a[92:95], v[28:31]\n\t"
    "buffer_load_dwordx4 v1, s[40:43], s53 offen offset:256 nt lds\n\t"
    "v_fma_f32 v78, v78, s55, v5\n\t"
    "v_fma_f32 v79, v79, s55, v5\n\t"
    "v_mfma_f32_16x16x32_bf16 v[32:35], a[248:251], a[112:115], v[32:35]\n\t"
    "s_mov_b32 s53, 15360\n\t"
    "v_exp_f32 v78, v78\n\t"
    "v_mfma_f32_16x16x32_bf16 v[32:35], a[252:255], a[116:119], v[32:35]\n\t"
    "s_add_u32 m0, s52, 110592\n\t"
    "v_exp_f32 v79, v79\n\t"
    "v_mfma_f32_16x16x32_bf16 v[36:39], a[248:251], a[136:139], v[36:39]\n\t"
    "v_fma_f32 v80, v80, s55, v6\n\t"
    "v_fma_f32 v81, v81, s55, v6\n\t"
    "v_mfma_f32_16x16x32_bf16 v[36:39], a[252:255], a[140:143], v[36:39]\n\t"
    "s_waitcnt lgkmcnt(0)\n\t"
    "ds_read_b128 a[248:251], v0 offset:13312\n\t"
    "ds_read_b128 a[252:255], v0 offset:13824\n\t"
    "v_exp_f32 v80, v80\n\t"
    "v_cvt_pk_bf16_f32 v76, v76, v77\n\t"
    "v_cvt_pk_bf16_f32 v77, v78, v79\n\t"
    "v_mfma_f32_16x16x32_bf16 v[184:187], a[240:243], a[0:3], 0\n\t"
    "v_exp_f32 v81, v81\n\t"
    "v_mfma_f32_16x16x32_bf16 v[184:187], a[244:247], a[4:7], v[184:187]\n\t"
    "v_fma_f32 v82, v82, s55, v6\n\t"
    "v_fma_f32 v83, v83, s55, v6\n\t"
    "v_mfma_f32_16x16x32_bf16 v[188:191], a[240:243], a[24:27], 0\n\t"
    "v_exp_f32 v82, v82\n\t"
    "v_mfma_f32_16x16x32_bf16 v[188:191], a[244:247], a[28:31], v[188:191]\n\t"
    "v_exp_f32 v83, v83\n\t"
    "v_mfma_f32_16x16x32_bf16 v[192:195], a[240:243], a[48:51], 0\n\t"
    "v_fma_f32 v84, v84, s55, v7\n\t"
    "v_fma_f32 v85, v85, s55, v7\n\t"
    "v_mfma_f32_16x16x32_bf16 v[192:195], a[244:247], a[52:55], v[192:195]\n\t"
    "v_exp_f32 v84, v84\n\t"
    "v_mfma_f32_16x16x32_bf16 v[196:199], a[240:243], a[72:75], 0\n\t"
    "v_cvt_pk_bf16_f32 v80, v80, v81\n\t"
    "v_cvt_pk_bf16_f32 v81, v82, v83\n\t"
    "v_mfma_f32_16x16x32_bf16 v[196:199], a[244:247], a[76:79], v[196:199]\n\t"
    "buffer_load_dwordx4 v1, s[40:43], s53 offen nt lds\n\t"
    "v_exp_f32 v85, v85\n\t"
    "v_fma_f32 v86, v86, s55, v7\n\t"
    "v_fma_f32 v87, v87, s55, v7\n\t"
    "v_mfma_f32_16x16x32_bf16 v[200:203], a[240:243], a[96:99], 0\n\t"
    "s_addk_i32 m0, 896\n\t"
    "v_exp_f32 v86, v86\n\t"
    "v_mfma_f32_16x16x32_bf16 v[200:203], a[244:247], a[100:103], v[200:203]\n\t"
    "v_exp_f32 v87, v87\n\t"
    "v_mfma_f32_16x16x32_bf16 v[204:207], a[240:243], a[120:123], 0\n\t"
    "v_cvt_pk_bf16_f32 v84, v84, v85\n\t"
    "v_cvt_pk_bf16_f32 v85, v86, v87\n\t"
    "v_mfma_f32_16x16x32_bf16 v[204:207], a[244:247], a[124:127], v[204:207]\n\t"
    "s_waitcnt lgkmcnt(0)\n\t"
    "ds_read_b128 a[240:243], v0 offset:14336\n\t"
    "ds_read_b128 a[244:247], v0 offset:14848\n\t"
    "v_fma_f32 v16, v16, s55, v2\n\t"
    "v_fma_f32 v17, v17, s55, v2\n\t"
    "v_mfma_f32_16x16x32_bf16 v[184:187], a[248:251], a[8:11], v[184:187]\n\t"
    "v_exp_f32 v16, v16\n\t"
    "v_mfma_f32_16x16x32_bf16 v[184:187], a[252:255], a[12:15], v[184:187]\n\t"
    "v_exp_f32 v17, v17\n\t"
    "v_fma_f32 v18, v18, s55, v2\n\t"
    "v_fma_f32 v19, v19, s55, v2\n\t"
    "v_mfma_f32_16x16x32_bf16 v[188:191], a[248:251], a[32:35], v[188:191]\n\t"
    "v_exp_f32 v18, v18\n\t"
    "v_mfma_f32_16x16x32_bf16 v[188:191], a[252:255], a[36:39], v[188:191]\n\t"
    "v_exp_f32 v19, v19\n\t"
    "v_mfma_f32_16x16x32_bf16 v[192:195], a[248:251], a[56:59], v[192:195]\n\t"
    "v_fma_f32 v20, v20, s55, v3\n\t"
    "v_fma_f32 v21, v21, s55, v3\n\t"
    "v_mfma_f32_16x16x32_bf16 v[192:195], a[252:255], a[60:63], v[192:195]\n\t"
    "buffer_load_dwordx4 v1, s[40:43], s53 offen offset:128 nt lds\n\t"
    "v_exp_f32 v20, v20\n\t"
    "v_mfma_f32_16x16x32_bf16 v[196:199], a[248:251], a[80:83], v[196:199]\n\t"
    "s_addk_i32 m0, 896\n\t"
    "v_cvt_pk_bf16_f32 v66, v16, v17\n\t"
    "v_cvt_pk_bf16_f32 v67, v18, v19\n\t"
    "v_mfma_f32_16x16x32_bf16 v[196:199], a[252:255], a[84:87], v[196:199]\n\t"
    "v_exp_f32 v21, v21\n\t"
    "v_fma_f32 v22, v22, s55, v3\n\t"
    "v_fma_f32 v23, v23, s55, v3\n\t"
    "v_mfma_f32_16x16x32_bf16 v[200:203], a[248:251], a[104:107], v[200:203]\n\t"
    "v_exp_f32 v22, v22\n\t"
    "v_mfma_f32_16x16x32_bf16 v[200:203], a[252:255], a[108:111], v[200:203]\n\t"
    "v_exp_f32 v23, v23\n\t"
    "v_mfma_f32_16x16x32_bf16 v[204:207], a[248:251], a[128:131], v[204:207]\n\t"
    "v_fma_f32 v24, v24, s55, v4\n\t"
    "v_fma_f32 v25, v25, s55, v4\n\t"
    "v_mfma_f32_16x16x32_bf16 v[204:207], a[252:255], a[132:135], v[204:207]\n\t"
    "s_waitcnt lgkmcnt(0)\n\t"
    "ds_read_b128 a[248:251], v0 offset:18432\n\t"
    "ds_read_b128 a[252:255], v0 offset:18944\n\t"
    "v_exp_f32 v24, v24\n\t"
    "v_mfma_f32_16x16x32_bf16 v[184:187], a[240:243], a[16:19], v[184:187]\n\t"
    "v_cvt_pk_bf16_f32 v70, v20, v21\n\t"
    "v_cvt_pk_bf16_f32 v71, v22, v23\n\t"
    "v_mfma_f32_16x16x32_bf16 v[184:187], a[244:247], a[20:23], v[184:187]\n\t"
    "v_exp_f32 v25, v25\n\t"
    "v_fma_f32 v26, v26, s55, v4\n\t"
    "v_fma_f32 v27, v27, s55, v4\n\t"
    "v_mfma_f32_16x16x32_bf16 v[8:11], v[40:43], v[64:67], 0\n\t"
    "v_exp_f32 v26, v26\n\t"
    "v_mfma_f32_16x16x32_bf16 v[8:11], v[44:47], v[68:71], v[8:11]\n\t"
    "v_exp_f32 v27, v27\n\t"
    "v_mfma_f32_16x16x32_bf16 v[188:191], a[240:243], a[40:43], v[188:191]\n\t"
    "v_fma_f32 v28, v28, s55, v5\n\t"
    "v_fma_f32 v29, v29, s55, v5\n\t"
    "v_mfma_f32_16x16x32_bf16 v[188:191], a[244:247], a[44:47], v[188:191]\n\t"
    "v_exp_f32 v28, v28\n\t"
    "v_mfma_f32_16x16x32_bf16 v[192:195], a[240:243], a[64:67], v[192:195]\n\t"
    "v_cvt_pk_bf16_f32 v74, v24, v25\n\t"
    "v_cvt_pk_bf16_f32 v75, v26, v27\n\t"
    "v_mfma_f32_16x16x32_bf16 v[192:195], a[244:247], a[68:71], v[192:195]\n\t"
    "buffer_store_dwordx4 v[212:215], v63, s[48:51], s54 offen offset:3328 sc1\n\t"
    "v_exp_f32 v29, v29\n\t"
    "v_fma_f32 v30, v30, s55, v5\n\t"
    "v_fma_f32 v31, v31, s55, v5\n\t"
    "v_mfma_f32_16x16x32_bf16 v[196:199], a[240:243], a[88:91], v[196:199]\n\t"
    "s_mov_b32 s54, 6144\n\t"
    "v_exp_f32 v30, v30\n\t"
    "v_mfma_f32_16x16x32_bf16 v[196:199], a[244:247], a[92:95], v[196:199]\n\t"
    "v_exp_f32 v31, v31\n\t"
    "v_mfma_f32_16x16x32_bf16 v[200:203], a[240:243], a[112:115], v[200:203]\n\t"
    "v_fma_f32 v32, v32, s55, v6\n\t"
    "v_fma_f32 v33, v33, s55, v6\n\t"
    "v_mfma_f32_16x16x32_bf16 v[200:203], a[244:247], a[116:119], v[200:203]\n\t"
    "v_exp_f32 v32, v32\n\t"
    "v_mfma_f32_16x16x32_bf16 v[204:207], a[240:243], a[136:139], v[204:207]\n\t"
    "v_cvt_pk_bf16_f32 v78, v28, v29\n\t"
    "v_cvt_pk_bf16_f32 v79, v30, v31\n\t"
    "v_mfma_f32_16x16x32_bf16 v[204:207], a[244:247], a[140:143], v[204:207]\n\t"
    "s_waitcnt lgkmcnt(0)\n\t"
    "ds_read_b128 a[240:243], v0 offset:19456\n\t"
    "ds_read_b128 a[244:247], v0 offset:19968\n\t"
    "v_exp_f32 v33, v33\n\t"
    "v_fma_f32 v34, v34, s55, v6\n\t"
    "v_fma_f32 v35, v35, s55, v6\n\t"
    "v_mfma_f32_16x16x32_bf16 v[10:13], v[40:43], v[72:75], 0\n\t"
    "v_exp_f32 v34, v34\n\t"
    "v_mfma_f32_16x16x32_bf16 v[10:13], v[44:47], v[76:79], v[10:13]\n\t"
    "v_exp_f32 v35, v35\n\t"
    "v_mfma_f32_16x16x32_bf16 v[16:19], a[248:251], a[0:3], 0\n\t"
    "v_fma_f32 v36, v36, s55, v7\n\t"
    "v_fma_f32 v37, v37, s55, v7\n\t"
    "v_mfma_f32_16x16x32_bf16 v[16:19], a[252:255], a[4:7], v[16:19]\n\t"
    "v_exp_f32 v36, v36\n\t"
    "v_mfma_f32_16x16x32_bf16 v[20:23], a[248:251], a[24:27], 0\n\t"
    "v_cvt_pk_bf16_f32 v82, v32, v33\n\t"
    "v_cvt_pk_bf16_f32 v83, v34, v35\n\t"
    "v_mfma_f32_16x16x32_bf16 v[20:23], a[252:255], a[28:31], v[20:23]\n\t"
    "buffer_load_dwordx4 v1, s[40:43], s53 offen offset:256 nt lds\n\t"
    "v_exp_f32 v37, v37\n\t"
    "v_fma_f32 v38, v38, s55, v7\n\t"
    "v_fma_f32 v39, v39, s55, v7\n\t"
    "v_mfma_f32_16x16x32_bf16 v[24:27], a[248:251], a[48:51], 0\n\t"
    "s_mov_b32 s53, 18432\n\t"
    "v_exp_f32 v38, v38\n\t"
    "v_mfma_f32_16x16x32_bf16 v[24:27], a[252:255], a[52:55], v[24:27]\n\t"
    "s_add_u32 m0, s52, 73728\n\t"
    "v_exp_f32 v39, v39\n\t"
    "v_mfma_f32_16x16x32_bf16 v[28:31], a[248:251], a[72:75], 0\n\t"
    "v_cvt_pk_bf16_f32 v86, v36, v37\n\t"
    "v_cvt_pk_bf16_f32 v87, v38, v39\n\t"
    "v_mfma_f32_16x16x32_bf16 v[28:31], a[252:255], a[76:79], v[28:31]\n\t"
    "v_fma_f32 v184, v184, s55, v2\n\t"
    "v_fma_f32 v185, v185, s55, v2\n\t"
    "v_mfma_f32_16x16x32_bf16 v[12:15], v[40:43], v[80:83], 0\n\t"
    "v_exp_f32 v184, v184\n\t"
    "v_mfma_f32_16x16x32_bf16 v[12:15], v[44:47], v[84:87], v[12:15]\n\t"
    "v_exp_f32 v185, v185\n\t"
    "v_mfma_f32_16x16x32_bf16 v[32:35], a[248:251], a[96:99], 0\n\t"
    "v_fma_f32 v186, v186, s55, v2\n\t"
    "v_fma_f32 v187, v187, s55, v2\n\t"
    "v_mfma_f32_16x16x32_bf16 v[32:35], a[252:255], a[100:103], v[32:35]\n\t"
    "v_exp_f32 v186, v186\n\t"
    "v_exp_f32 v187, v187\n\t"
    "v_mfma_f32_16x16x32_bf16 v[36:39], a[248:251], a[120:123], 0\n\t"
    "v_fma_f32 v188, v188, s55, v3\n\t"
    "v_fma_f32 v189, v189, s55, v3\n\t"
    "v_mfma_f32_16x16x32_bf16 v[36:39], a[252:255], a[124:127], v[36:39]\n\t"
    "s_waitcnt lgkmcnt(0)\n\t"
    "ds_read_b128 a[248:251], v0 offset:20480\n\t"
    "ds_read_b128 a[252:255], v0 offset:20992\n\t"
    "v_exp_f32 v188, v188\n\t"
    "v_mfma_f32_16x16x32_bf16 v[16:19], a[240:243], a[8:11], v[16:19]\n\t"
    "v_cvt_pk_bf16_f32 v184, v184, v185\n\t"
    "v_cvt_pk_bf16_f32 v186, v186, v187\n\t"
    "v_mfma_f32_16x16x32_bf16 v[16:19], a[244:247], a[12:15], v[16:19]\n\t"
    "v_exp_f32 v189, v189\n\t"
    "v_mfma_f32_16x16x32_bf16 v[20:23], a[240:243], a[32:35], v[20:23]\n\t"
    "v_fma_f32 v190, v190, s55, v3\n\t"
    "v_fma_f32 v191, v191, s55, v3\n\t"
    "v_mfma_f32_16x16x32_bf16 v[20:23], a[244:247], a[36:39], v[20:23]\n\t"
    "buffer_load_dwordx4 v1, s[40:43], s53 offen nt lds\n\t"
    "v_accvgpr_write_b32 a144, v184\n\t"
    "v_accvgpr_write_b32 a145, v186\n\t"
    "v_mfma_f32_16x16x32_bf16 v[24:27], a[240:243], a[56:59], v[24:27]\n\t"
    "s_addk_i32 m0, 896\n\t"
    "v_exp_f32 v190, v190\n\t"
    "v_mfma_f32_16x16x32_bf16 v[24:27], a[244:247], a[60:63], v[24:27]\n\t"
    "v_exp_f32 v191, v191\n\t"
    "v_fma_f32 v192, v192, s55, v4\n\t"
    "v_fma_f32 v193, v193, s55, v4\n\t"
    "v_mfma_f32_16x16x32_bf16 v[28:31], a[240:243], a[80:83], v[28:31]\n\t"
    "v_exp_f32 v192, v192\n\t"
    "v_mfma_f32_16x16x32_bf16 v[28:31], a[244:247], a[84:87], v[28:31]\n\t"
    "v_cvt_pk_bf16_f32 v188, v188, v189\n\t"
    "v_cvt_pk_bf16_f32 v190, v190, v191\n\t"
    "v_mfma_f32_16x16x32_bf16 v[32:35], a[240:243], a[104:107], v[32:35]\n\t"
    "v_exp_f32 v193, v193\n\t"
    "v_mfma_f32_16x16x32_bf16 v[32:35], a[244:247], a[108:111], v[32:35]\n\t"
    "v_fma_f32 v194, v194, s55, v4\n\t"
    "v_fma_f32 v195, v195, s55, v4\n\t"
    "v_mfma_f32_16x16x32_bf16 v[36:39], a[240:243], a[128:131], v[36:39]\n\t"
    "v_accvgpr_write_b32 a148, v188\n\t"
    "v_accvgpr_write_b32 a149, v190\n\t"
    "v_mfma_f32_16x16x32_bf16 v[36:39], a[244:247], a[132:135], v[36:39]\n\t"
    "s_waitcnt vmcnt(9) lgkmcnt(0)\n\t"
    "s_barrier\n\t"
    "ds_read_b128 a[240:243], v0 offset:36864\n\t"
    "ds_read_b128 a[244:247], v0 offset:37376\n\t"
    "v_exp_f32 v194, v194\n\t"
    "v_exp_f32 v195, v195\n\t"
    "v_mfma_f32_16x16x32_bf16 v[16:19], a[248:251], a[16:19], v[16:19]\n\t"
    "v_fma_f32 v196, v196, s55, v5\n\t"
    "v_fma_f32 v197, v197, s55, v5\n\t"
    "v_mfma_f32_16x16x32_bf16 v[16:19], a[252:255], a[20:23], v[16:19]\n\t"
    "buffer_load_dwordx4 v1, s[40:43], s53 offen offset:128 nt lds\n\t"
    "v_exp_f32 v196, v196\n\t"
    "v_mfma_f32_16x16x32_bf16 v[20:23], a[248:251], a[40:43], v[20:23]\n\t"
    "s_addk_i32 m0, 896\n\t"
    "v_cvt_pk_bf16_f32 v192, v192, v193\n\t"
    "v_cvt_pk_bf16_f32 v194, v194, v195\n\t"
    "v_mfma_f32_16x16x32_bf16 v[20:23], a[252:255], a[44:47], v[20:23]\n\t"
    "v_exp_f32 v197, v197\n\t"
    "v_mfma_f32_16x16x32_bf16 v[24:27], a[248:251], a[64:67], v[24:27]\n\t"
    "v_fma_f32 v198, v198, s55, v5\n\t"
    "v_fma_f32 v199, v199, s55, v5\n\t"
    "v_mfma_f32_16x16x32_bf16 v[24:27], a[252:255], a[68:71], v[24:27]\n\t"
    "v_accvgpr_write_b32 a152, v192\n\t"
    "v_accvgpr_write_b32 a153, v194\n\t"
    "v_exp_f32 v198, v198\n\t"
    "v_mfma_f32_16x16x32_bf16 v[28:31], a[248:251], a[88:91], v[28:31]\n\t"
    "v_exp_f32 v199, v199\n\t"
    "v_mfma_f32_16x16x32_bf16 v[28:31], a[252:255], a[92:95], v[28:31]\n\t"
    "v_fma_f32 v200, v200, s55, v6\n\t"
    "v_fma_f32 v201, v201, s55, v6\n\t"
    "v_mfma_f32_16x16x32_bf16 v[32:35], a[248:251], a[112:115], v[32:35]\n\t"
    "v_exp_f32 v200, v200\n\t"
    "v_mfma_f32_16x16x32_bf16 v[32:35], a[252:255], a[116:119], v[32:35]\n\t"
    "v_cvt_pk_bf16_f32 v196, v196, v197\n\t"
    "v_cvt_pk_bf16_f32 v198, v198, v199\n\t"
    "v_mfma_f32_16x16x32_bf16 v[36:39], a[248:251], a[136:139], v[36:39]\n\t"
    "v_exp_f32 v201, v201\n\t"
    "v_mfma_f32_16x16x32_bf16 v[36:39], a[252:255], a[140:143], v[36:39]\n\t"
    "s_waitcnt lgkmcnt(0)\n\t"
    "ds_read_b128 a[248:251], v0 offset:37888\n\t"
    "ds_read_b128 a[252:255], v0 offset:38400\n\t"
    "v_fma_f32 v202, v202, s55, v6\n\t"
    "v_fma_f32 v203, v203, s55, v6\n\t"
    "v_accvgpr_write_b32 a156, v196\n\t"
    "v_accvgpr_write_b32 a157, v198\n\t"
    "v_mfma_f32_16x16x32_bf16 v[88:91], a[240:243], a[0:3], 0\n\t"
    "v_exp_f32 v202, v202\n\t"
    "v_mfma_f32_16x16x32_bf16 v[88:91], a[244:247], a[4:7], v[88:91]\n\t"
    "buffer_load_dwordx4 v1, s[40:43], s53 offen offset:256 nt lds\n\t"
    "v_exp_f32 v203, v203\n\t"
    "v_mfma_f32_16x16x32_bf16 v[92:95], a[240:243], a[24:27], 0\n\t"
    "s_mov_b32 s53, 21504\n\t"
    "v_fma_f32 v204, v204, s55, v7\n\t"
    "v_fma_f32 v205, v205, s55, v7\n\t"
    "v_mfma_f32_16x16x32_bf16 v[92:95], a[244:247], a[28:31], v[92:95]\n\t"
    "s_add_u32 m0, s52, 98304\n\t"
    "v_exp_f32 v204, v204\n\t"
    "v_mfma_f32_16x16x32_bf16 v[96:99], a[240:243], a[48:51], 0\n\t"
    "v_cvt_pk_bf16_f32 v200, v200, v201\n\t"
    "v_cvt_pk_bf16_f32 v202, v202, v203\n\t"
    "v_mfma_f32_16x16x32_bf16 v[96:99], a[244:247], a[52:55], v[96:99]\n\t"
    "v_exp_f32 v205, v205\n\t"
    "v_fma_f32 v206, v206, s55, v7\n\t"
    "v_fma_f32 v207, v207, s55, v7\n\t"
    "v_mfma_f32_16x16x32_bf16 v[100:103], a[240:243], a[72:75], 0\n\t"
    "v_accvgpr_write_b32 a160, v200\n\t"
    "v_accvgpr_write_b32 a161, v202\n\t"
    "v_mfma_f32_16x16x32_bf16 v[100:103], a[244:247], a[76:79], v[100:103]\n\t"
    "v_exp_f32 v206, v206\n\t"
    "v_mfma_f32_16x16x32_bf16 v[104:107], a[240:243], a[96:99], 0\n\t"
    "v_exp_f32 v207, v207\n\t"
    "v_mfma_f32_16x16x32_bf16 v[104:107], a[244:247], a[100:103], v[104:107]\n\t"
    "v_cvt_pk_bf16_f32 v204, v204, v205\n\t"
    "v_cvt_pk_bf16_f32 v206, v206, v207\n\t"
    "v_accvgpr_write_b32 a164, v204\n\t"
    "v_accvgpr_write_b32 a165, v206\n\t"
    "v_mfma_f32_16x16x32_bf16 v[108:111], a[240:243], a[120:123], 0\n\t"
    "v_fma_f32 v16, v16, s55, v2\n\t"
    "v_fma_f32 v17, v17, s55, v2\n\t"
    "v_mfma_f32_16x16x32_bf16 v[108:111], a[244:247], a[124:127], v[108:111]\n\t"
    "s_waitcnt lgkmcnt(0)\n\t"
    "ds_read_b128 a[240:243], v0 offset:38912\n\t"
    "ds_read_b128 a[244:247], v0 offset:39424\n\t"
    "v_exp_f32 v16, v16\n\t"
    "v_mfma_f32_16x16x32_bf16 v[88:91], a[248:251], a[8:11], v[88:91]\n\t"
    "v_exp_f32 v17, v17\n\t"
    "v_mfma_f32_16x16x32_bf16 v[88:91], a[252:255], a[12:15], v[88:91]\n\t"
    "buffer_load_dwordx4 v1, s[40:43], s53 offen nt lds\n\t"
    "v_fma_f32 v18, v18, s55, v2\n\t"
    "v_fma_f32 v19, v19, s55, v2\n\t"
    "v_mfma_f32_16x16x32_bf16 v[92:95], a[248:251], a[32:35], v[92:95]\n\t"
    "s_addk_i32 m0, 896\n\t"
    "v_exp_f32 v18, v18\n\t"
    "v_mfma_f32_16x16x32_bf16 v[92:95], a[252:255], a[36:39], v[92:95]\n\t"
    "v_exp_f32 v19, v19\n\t"
    "v_fma_f32 v20, v20, s55, v3\n\t"
    "v_fma_f32 v21, v21, s55, v3\n\t"
    "v_mfma_f32_16x16x32_bf16 v[96:99], a[248:251], a[56:59], v[96:99]\n\t"
    "v_exp_f32 v20, v20\n\t"
    "v_mfma_f32_16x16x32_bf16 v[96:99], a[252:255], a[60:63], v[96:99]\n\t"
    "v_cvt_pk_bf16_f32 v16, v16, v17\n\t"
    "v_cvt_pk_bf16_f32 v18, v18, v19\n\t"
    "v_mfma_f32_16x16x32_bf16 v[100:103], a[248:251], a[80:83], v[100:103]\n\t"
    "v_exp_f32 v21, v21\n\t"
    "v_mfma_f32_16x16x32_bf16 v[100:103], a[252:255], a[84:87], v[100:103]\n\t"
    "v_fma_f32 v22, v22, s55, v3\n\t"
    "v_fma_f32 v23, v23, s55, v3\n\t"
    "v_accvgpr_write_b32 a146, v16\n\t"
    "v_accvgpr_write_b32 a147, v18\n\t"
    "v_mfma_f32_16x16x32_bf16 v[104:107], a[248:251], a[104:107], v[104:107]\n\t"
    "v_exp_f32 v22, v22\n\t"
    "v_mfma_f32_16x16x32_bf16 v[104:107], a[252:255], a[108:111], v[104:107]\n\t"
    "v_exp_f32 v23, v23\n\t"
    "v_mfma_f32_16x16x32_bf16 v[108:111], a[248:251], a[128:131], v[108:111]\n\t"
    "v_fma_f32 v24, v24, s55, v4\n\t"
    "v_fma_f32 v25, v25, s55, v4\n\t"
    "v_mfma_f32_16x16x32_bf16 v[108:111], a[252:255], a[132:135], v[108:111]\n\t"
    "s_waitcnt lgkmcnt(0)\n\t"
    "ds_read_b128 a[248:251], v0 offset:43008\n\t"
    "ds_read_b128 a[252:255], v0 offset:43520\n\t"
    "v_exp_f32 v24, v24\n\t"
    "v_cvt_pk_bf16_f32 v20, v20, v21\n\t"
    "v_cvt_pk_bf16_f32 v22, v22, v23\n\t"
    "v_mfma_f32_16x16x32_bf16 v[88:91], a[240:243], a[16:19], v[88:91]\n\t"
    "v_exp_f32 v25, v25\n\t"
    "v_mfma_f32_16x16x32_bf16 v[88:91], a[244:247], a[20:23], v[88:91]\n\t"
    "buffer_store_dwordx4 v[216:219], v63, s[48:51], s54 offen offset:256 sc1\n\t"
    "v_fma_f32 v26, v26, s55, v4\n\t"
    "v_fma_f32 v27, v27, s55, v4\n\t"
    "v_accvgpr_write_b32 a150, v20\n\t"
    "v_accvgpr_write_b32 a151, v22\n\t"
    "v_mfma_f32_16x16x32_bf16 v[92:95], a[240:243], a[40:43], v[92:95]\n\t"
    "v_exp_f32 v26, v26\n\t"
    "v_mfma_f32_16x16x32_bf16 v[92:95], a[244:247], a[44:47], v[92:95]\n\t"
    "v_exp_f32 v27, v27\n\t"
    "v_mfma_f32_16x16x32_bf16 v[8:11], v[40:43], a[144:147], v[8:11]\n\t"
    "v_fma_f32 v28, v28, s55, v5\n\t"
    "v_fma_f32 v29, v29, s55, v5\n\t"
    "v_mfma_f32_16x16x32_bf16 v[8:11], v[44:47], a[148:151], v[8:11]\n\t"
    "v_exp_f32 v28, v28\n\t"
    "v_cvt_pk_bf16_f32 v24, v24, v25\n\t"
    "v_cvt_pk_bf16_f32 v26, v26, v27\n\t"
    "v_mfma_f32_16x16x32_bf16 v[96:99], a[240:243], a[64:67], v[96:99]\n\t"
    "v_exp_f32 v29, v29\n\t"
    "v_mfma_f32_16x16x32_bf16 v[96:99], a[244:247], a[68:71], v[96:99]\n\t"
    "v_fma_f32 v30, v30, s55, v5\n\t"
    "v_fma_f32 v31, v31, s55, v5\n\t"
    "v_mfma_f32_16x16x32_bf16 v[100:103], a[240:243], a[88:91], v[100:103]\n\t"
    "v_accvgpr_write_b32 a154, v24\n\t"
    "v_accvgpr_write_b32 a155, v26\n\t"
    "v_mfma_f32_16x16x32_bf16 v[100:103], a[244:247], a[92:95], v[100:103]\n\t"
    "v_exp_f32 v30, v30\n\t"
    "v_mfma_f32_16x16x32_bf16 v[104:107], a[240:243], a[112:115], v[104:107]\n\t"
    "v_exp_f32 v31, v31\n\t"
    "v_mfma_f32_16x16x32_bf16 v[104:107], a[244:247], a[116:119], v[104:107]\n\t"
    "buffer_load_dwordx4 v1, s[40:43], s53 offen offset:128 nt lds\n\t"
    "v_fma_f32 v32, v32, s55, v6\n\t"
    "v_fma_f32 v33, v33, s55, v6\n\t"
    "v_exp_f32 v32, v32\n\t"
    "v_mfma_f32_16x16x32_bf16 v[108:111], a[240:243], a[136:139], v[108:111]\n\t"
    "s_addk_i32 m0, 896\n\t"
    "v_cvt_pk_bf16_f32 v28, v28, v29\n\t"
    "v_cvt_pk_bf16_f32 v30, v30, v31\n\t"
    "v_mfma_f32_16x16x32_bf16 v[108:111], a[244:247], a[140:143], v[108:111]\n\t"
    "s_waitcnt lgkmcnt(0)\n\t"
    "ds_read_b128 a[240:243], v0 offset:44032\n\t"
    "ds_read_b128 a[244:247], v0 offset:44544\n\t"
    "v_exp_f32 v33, v33\n\t"
    "v_mfma_f32_16x16x32_bf16 v[16:19], a[248:251], a[0:3], 0\n\t"
    "v_fma_f32 v34, v34, s55, v6\n\t"
    "v_fma_f32 v35, v35, s55, v6\n\t"
    "v_mfma_f32_16x16x32_bf16 v[16:19], a[252:255], a[4:7], v[16:19]\n\t"
    "v_accvgpr_write_b32 a158, v28\n\t"
    "v_accvgpr_write_b32 a159, v30\n\t"
    "v_exp_f32 v34, v34\n\t"
    "v_mfma_f32_16x16x32_bf16 v[10:13], v[40:43], a[152:155], v[10:13]\n\t"
    "v_exp_f32 v35, v35\n\t"
    "v_mfma_f32_16x16x32_bf16 v[10:13], v[44:47], a[156:159], v[10:13]\n\t"
    "v_fma_f32 v36, v36, s55, v7\n\t"
    "v_fma_f32 v37, v37, s55, v7\n\t"
    "v_mfma_f32_16x16x32_bf16 v[20:23], a[248:251], a[24:27], 0\n\t"
    "v_exp_f32 v36, v36\n\t"
    "v_mfma_f32_16x16x32_bf16 v[20:23], a[252:255], a[28:31], v[20:23]\n\t"
    "v_cvt_pk_bf16_f32 v32, v32, v33\n\t"
    "v_cvt_pk_bf16_f32 v34, v34, v35\n\t"
    "v_mfma_f32_16x16x32_bf16 v[24:27], a[248:251], a[48:51], 0\n\t"
    "v_exp_f32 v37, v37\n\t"
    "v_mfma_f32_16x16x32_bf16 v[24:27], a[252:255], a[52:55], v[24:27]\n\t"
    "v_fma_f32 v38, v38, s55, v7\n\t"
    "v_fma_f32 v39, v39, s55, v7\n\t"
    "v_mfma_f32_16x16x32_bf16 v[28:31], a[248:251], a[72:75], 0\n\t"
    "v_accvgpr_write_b32 a162, v32\n\t"
    "v_accvgpr_write_b32 a163, v34\n\t"
    "v_mfma_f32_16x16x32_bf16 v[28:31], a[252:255], a[76:79], v[28:31]\n\t"
    "v_exp_f32 v38, v38\n\t"
    "v_exp_f32 v39, v39\n\t"
    "v_mfma_f32_16x16x32_bf16 v[32:35], a[248:251], a[96:99], 0\n\t"
    "v_cvt_pk_bf16_f32 v36, v36, v37\n\t"
    "v_cvt_pk_bf16_f32 v38, v38, v39\n\t"
    "v_mfma_f32_16x16x32_bf16 v[32:35], a[252:255], a[100:103], v[32:35]\n\t"
    "buffer_load_dwordx4 v1, s[40:43], s53 offen offset:256 nt lds\n\t"
    "v_accvgpr_write_b32 a166, v36\n\t"
    "v_accvgpr_write_b32 a167, v38\n\t"
    "v_mfma_f32_16x16x32_bf16 v[36:39], a[248:251], a[120:123], 0\n\t"
    "s_mov_b32 s53, s81\n\t"
    "v_fma_f32 v88, v88, s55, v2\n\t"
    "v_fma_f32 v89, v89, s55, v2\n\t"
    "v_mfma_f32_16x16x32_bf16 v[36:39], a[252:255], a[124:127], v[36:39]\n\t"
    "s_waitcnt lgkmcnt(0)\n\t"
    "ds_read_b128 a[248:251], v0 offset:45056\n\t"
    "ds_read_b128 a[252:255], v0 offset:45568\n\t"
    "v_exp_f32 v88, v88\n\t"
    "v_mfma_f32_16x16x32_bf16 v[12:15], v[40:43], a[160:163], v[12:15]\n\t"
    "s_add_u32 m0, s52, 122880\n\t"
    "v_exp_f32 v89, v89\n\t"
    "v_mfma_f32_16x16x32_bf16 v[12:15], v[44:47], a[164:167], v[12:15]\n\t"
    "v_fma_f32 v90, v90, s55, v2\n\t"
    "v_fma_f32 v91, v91, s55, v2\n\t"
    "v_mfma_f32_16x16x32_bf16 v[16:19], a[240:243], a[8:11], v[16:19]\n\t"
    "v_exp_f32 v90, v90\n\t"
    "v_mfma_f32_16x16x32_bf16 v[16:19], a[244:247], a[12:15], v[16:19]\n\t"
    "v_exp_f32 v91, v91\n\t"
    "v_fma_f32 v92, v92, s55, v3\n\t"
    "v_fma_f32 v93, v93, s55, v3\n\t"
    "v_mfma_f32_16x16x32_bf16 v[20:23], a[240:243], a[32:35], v[20:23]\n\t"
    "v_exp_f32 v92, v92\n\t"
    "v_mfma_f32_16x16x32_bf16 v[20:23], a[244:247], a[36:39], v[20:23]\n\t"
    "v_cvt_pk_bf16_f32 v88, v88, v89\n\t"
    "v_cvt_pk_bf16_f32 v89, v90, v91\n\t"
    "v_mfma_f32_16x16x32_bf16 v[24:27], a[240:243], a[56:59], v[24:27]\n\t"
    "v_exp_f32 v93, v93\n\t"
    "v_mfma_f32_16x16x32_bf16 v[24:27], a[244:247], a[60:63], v[24:27]\n\t"
    "v_fma_f32 v94, v94, s55, v3\n\t"
    "v_fma_f32 v95, v95, s55, v3\n\t"
    "v_mfma_f32_16x16x32_bf16 v[28:31], a[240:243], a[80:83], v[28:31]\n\t"
    "v_exp_f32 v94, v94\n\t"
    "v_mfma_f32_16x16x32_bf16 v[28:31], a[244:247], a[84:87], v[28:31]\n\t"
    "buffer_load_dwordx4 v1, s[36:39], s53 offen nt lds\n\t"
    "v_exp_f32 v95, v95\n\t"
    "v_mfma_f32_16x16x32_bf16 v[32:35], a[240:243], a[104:107], v[32:35]\n\t"
    "s_addk_i32 m0, 896\n\t"
    "v_fma_f32 v96, v96, s55, v4\n\t"
    "v_fma_f32 v97, v97, s55, v4\n\t"
    "v_mfma_f32_16x16x32_bf16 v[32:35], a[244:247], a[108:111], v[32:35]\n\t"
    "v_exp_f32 v96, v96\n\t"
    "v_cvt_pk_bf16_f32 v92, v92, v93\n\t"
    "v_cvt_pk_bf16_f32 v93, v94, v95\n\t"
    "v_mfma_f32_16x16x32_bf16 v[36:39], a[240:243], a[128:131], v[36:39]\n\t"
    "v_exp_f32 v97, v97\n\t"
    "v_mfma_f32_16x16x32_bf16 v[36:39], a[244:247], a[132:135], v[36:39]\n\t"
    "s_waitcnt lgkmcnt(0)\n\t"
    "ds_read_b128 a[240:243], v0 offset:49152\n\t"
    "ds_read_b128 a[244:247], v0 offset:49664\n\t"
    "v_fma_f32 v98, v98, s55, v4\n\t"
    "v_fma_f32 v99, v99, s55, v4\n\t"
    "v_mfma_f32_16x16x32_bf16 v[16:19], a[248:251], a[16:19], v[16:19]\n\t"
    "v_exp_f32 v98, v98\n\t"
    "v_mfma_f32_16x16x32_bf16 v[16:19], a[252:255], a[20:23], v[16:19]\n\t"
    "v_exp_f32 v99, v99\n\t"
    "v_mfma_f32_16x16x32_bf16 v[20:23], a[248:251], a[40:43], v[20:23]\n\t"
    "v_fma_f32 v100, v100, s55, v5\n\t"
    "v_fma_f32 v101, v101, s55, v5\n\t"
    "v_mfma_f32_16x16x32_bf16 v[20:23], a[252:255], a[44:47], v[20:23]\n\t"
    "v_exp_f32 v100, v100\n\t"
    "v_mfma_f32_16x16x32_bf16 v[24:27], a[248:251], a[64:67], v[24:27]\n\t"
    "v_cvt_pk_bf16_f32 v96, v96, v97\n\t"
    "v_cvt_pk_bf16_f32 v97, v98, v99\n\t"
    "v_mfma_f32_16x16x32_bf16 v[24:27], a[252:255], a[68:71], v[24:27]\n\t"
    "v_exp_f32 v101, v101\n\t"
    "v_fma_f32 v102, v102, s55, v5\n\t"
    "v_fma_f32 v103, v103, s55, v5\n\t"
    "v_mfma_f32_16x16x32_bf16 v[28:31], a[248:251], a[88:91], v[28:31]\n\t"
    "v_exp_f32 v102, v102\n\t"
    "v_mfma_f32_16x16x32_bf16 v[28:31], a[252:255], a[92:95], v[28:31]\n\t"
    "buffer_load_dwordx4 v1, s[36:39], s53 offen offset:128 nt lds\n\t"
    "v_exp_f32 v103, v103\n\t"
    "v_mfma_f32_16x16x32_bf16 v[32:35], a[248:251], a[112:115], v[32:35]\n\t"
    "s_addk_i32 m0, 896\n\t"
    "v_fma_f32 v104, v104, s55, v6\n\t"
    "v_fma_f32 v105, v105, s55, v6\n\t"
    "v_mfma_f32_16x16x32_bf16 v[32:35], a[252:255], a[116:119], v[32:35]\n\t"
    "v_exp_f32 v104, v104\n\t"
    "v_mfma_f32_16x16x32_bf16 v[36:39], a[248:251], a[136:139], v[36:39]\n\t"
    "v_cvt_pk_bf16_f32 v100, v100, v101\n\t"
    "v_cvt_pk_bf16_f32 v101, v102, v103\n\t"
    "v_mfma_f32_16x16x32_bf16 v[36:39], a[252:255], a[140:143], v[36:39]\n\t"
    "s_waitcnt lgkmcnt(0)\n\t"
    "ds_read_b128 a[248:251], v0 offset:50176\n\t"
    "ds_read_b128 a[252:255], v0 offset:50688\n\t"
    "v_exp_f32 v105, v105\n\t"
    "v_mfma_f32_16x16x32_bf16 v[184:187], a[240:243], a[0:3], 0\n\t"
    "v_fma_f32 v106, v106, s55, v6\n\t"
    "v_fma_f32 v107, v107, s55, v6\n\t"
    "v_mfma_f32_16x16x32_bf16 v[184:187], a[244:247], a[4:7], v[184:187]\n\t"
    "v_exp_f32 v106, v106\n\t"
    "v_exp_f32 v107, v107\n\t"
    "v_mfma_f32_16x16x32_bf16 v[188:191], a[240:243], a[24:27], 0\n\t"
    "v_fma_f32 v108, v108, s55, v7\n\t"
    "v_fma_f32 v109, v109, s55, v7\n\t"
    "v_mfma_f32_16x16x32_bf16 v[188:191], a[244:247], a[28:31], v[188:191]\n\t"
    "v_exp_f32 v108, v108\n\t"
    "v_mfma_f32_16x16x32_bf16 v[192:195], a[240:243], a[48:51], 0\n\t"
    "v_cvt_pk_bf16_f32 v104, v104, v105\n\t"
    "v_cvt_pk_bf16_f32 v105, v106, v107\n\t"
    "v_mfma_f32_16x16x32_bf16 v[192:195], a[244:247], a[52:55], v[192:195]\n\t"
    "buffer_load_dwordx4 v1, s[36:39], s53 offen offset:256 nt lds\n\t"
    "v_exp_f32 v109, v109\n\t"
    "v_mfma_f32_16x16x32_bf16 v[196:199], a[240:243], a[72:75], 0\n\t"
    "s_mov_b32 s53, s82\n\t"
    "v_fma_f32 v110, v110, s55, v7\n\t"
    "v_fma_f32 v111, v111, s55, v7\n\t"
    "v_mfma_f32_16x16x32_bf16 v[196:199], a[244:247], a[76:79], v[196:199]\n\t"
    "s_add_u32 m0, s52, 135168\n\t"
    "v_exp_f32 v110, v110\n\t"
    "v_mfma_f32_16x16x32_bf16 v[200:203], a[240:243], a[96:99], 0\n\t"
    "v_exp_f32 v111, v111\n\t"
    "v_mfma_f32_16x16x32_bf16 v[200:203], a[244:247], a[100:103], v[200:203]\n\t"
    "v_cvt_pk_bf16_f32 v108, v108, v109\n\t"
    "v_cvt_pk_bf16_f32 v109, v110, v111\n\t"
    "v_fma_f32 v16, v16, s55, v2\n\t"
    "v_fma_f32 v17, v17, s55, v2\n\t"
    "v_mfma_f32_16x16x32_bf16 v[204:207], a[240:243], a[120:123], 0\n\t"
    "v_exp_f32 v16, v16\n\t"
    "v_mfma_f32_16x16x32_bf16 v[204:207], a[244:247], a[124:127], v[204:207]\n\t"
    "s_waitcnt lgkmcnt(0)\n\t"
    "ds_read_b128 a[240:243], v0 offset:51200\n\t"
    "ds_read_b128 a[244:247], v0 offset:51712\n\t"
    "v_exp_f32 v17, v17\n\t"
    "v_mfma_f32_16x16x32_bf16 v[184:187], a[248:251], a[8:11], v[184:187]\n\t"
    "v_fma_f32 v18, v18, s55, v2\n\t"
    "v_fma_f32 v19, v19, s55, v2\n\t"
    "v_mfma_f32_16x16x32_bf16 v[184:187], a[252:255], a[12:15], v[184:187]\n\t"
    "v_exp_f32 v18, v18\n\t"
    "v_mfma_f32_16x16x32_bf16 v[188:191], a[248:251], a[32:35], v[188:191]\n\t"
    "v_exp_f32 v19, v19\n\t"
    "v_mfma_f32_16x16x32_bf16 v[188:191], a[252:255], a[36:39], v[188:191]\n\t"
    "v_fma_f32 v20, v20, s55, v3\n\t"
    "v_fma_f32 v21, v21, s55, v3\n\t"
    "v_exp_f32 v20, v20\n\t"
    "v_mfma_f32_16x16x32_bf16 v[192:195], a[248:251], a[56:59], v[192:195]\n\t"
    "v_cvt_pk_bf16_f32 v90, v16, v17\n\t"
    "v_cvt_pk_bf16_f32 v91, v18, v19\n\t"
    "v_mfma_f32_16x16x32_bf16 v[192:195], a[252:255], a[60:63], v[192:195]\n\t"
    "buffer_store_dwordx4 v[220:223], v63, s[48:51], s54 offen offset:3328 sc1\n\t"
    "v_exp_f32 v21, v21\n\t"
    "v_mfma_f32_16x16x32_bf16 v[196:199], a[248:251], a[80:83], v[196:199]\n\t"
    "s_mov_b32 s54, 12288\n\t"
    "v_fma_f32 v22, v22, s55, v3\n\t"
    "v_fma_f32 v23, v23, s55, v3\n\t"
    "v_mfma_f32_16x16x32_bf16 v[196:199], a[252:255], a[84:87], v[196:199]\n\t"
    "v_exp_f32 v22, v22\n\t"
    "v_mfma_f32_16x16x32_bf16 v[200:203], a[248:251], a[104:107], v[200:203]\n\t"
    "v_exp_f32 v23, v23\n\t"
    "v_mfma_f32_16x16x32_bf16 v[200:203], a[252:255], a[108:111], v[200:203]\n\t"
    "v_fma_f32 v24, v24, s55, v4\n\t"
    "v_fma_f32 v25, v25, s55, v4\n\t"
    "v_exp_f32 v24, v24\n\t"
    "v_mfma_f32_16x16x32_bf16 v[204:207], a[248:251], a[128:131], v[204:207]\n\t"
    "v_cvt_pk_bf16_f32 v94, v20, v21\n\t"
    "v_cvt_pk_bf16_f32 v95, v22, v23\n\t"
    "v_mfma_f32_16x16x32_bf16 v[204:207], a[252:255], a[132:135], v[204:207]\n\t"
    "s_waitcnt lgkmcnt(0)\n\t"
    "ds_read_b128 a[248:251], v0 offset:55296\n\t"
    "ds_read_b128 a[252:255], v0 offset:55808\n\t"
    "v_exp_f32 v25, v25\n\t"
    "v_mfma_f32_16x16x32_bf16 v[8:11], v[40:43], v[88:91], v[8:11]\n\t"
    "v_fma_f32 v26, v26, s55, v4\n\t"
    "v_fma_f32 v27, v27, s55, v4\n\t"
    "v_mfma_f32_16x16x32_bf16 v[8:11], v[44:47], v[92:95], v[8:11]\n\t"
    "v_exp_f32 v26, v26\n\t"
    "v_mfma_f32_16x16x32_bf16 v[184:187], a[240:243], a[16:19], v[184:187]\n\t"
    "v_exp_f32 v27, v27\n\t"
    "v_mfma_f32_16x16x32_bf16 v[184:187], a[244:247], a[20:23], v[184:187]\n\t"
    "v_fma_f32 v28, v28, s55, v5\n\t"
    "v_fma_f32 v29, v29, s55, v5\n\t"
    "v_mfma_f32_16x16x32_bf16 v[188:191], a[240:243], a[40:43], v[188:191]\n\t"
    "v_exp_f32 v28, v28\n\t"
    "v_mfma_f32_16x16x32_bf16 v[188:191], a[244:247], a[44:47], v[188:191]\n\t"
    "buffer_load_dwordx4 v1, s[36:39], s53 offen nt lds\n\t"
    "v_cvt_pk_bf16_f32 v98, v24, v25\n\t"
    "v_cvt_pk_bf16_f32 v99, v26, v27\n\t"
    "v_exp_f32 v29, v29\n\t"
    "v_mfma_f32_16x16x32_bf16 v[192:195], a[240:243], a[64:67], v[192:195]\n\t"
    "s_addk_i32 m0, 896\n\t"
    "v_fma_f32 v30, v30, s55, v5\n\t"
    "v_fma_f32 v31, v31, s55, v5\n\t"
    "v_mfma_f32_16x16x32_bf16 v[192:195], a[244:247], a[68:71], v[192:195]\n\t"
    "v_exp_f32 v30, v30\n\t"
    "v_mfma_f32_16x16x32_bf16 v[196:199], a[240:243], a[88:91], v[196:199]\n\t"
    "v_exp_f32 v31, v31\n\t"
    "v_mfma_f32_16x16x32_bf16 v[196:199], a[244:247], a[92:95], v[196:199]\n\t"
    "v_fma_f32 v32, v32, s55, v6\n\t"
    "v_fma_f32 v33, v33, s55, v6\n\t"
    "v_mfma_f32_16x16x32_bf16 v[200:203], a[240:243], a[112:115], v[200:203]\n\t"
    "v_exp_f32 v32, v32\n\t"
    "v_mfma_f32_16x16x32_bf16 v[200:203], a[244:247], a[116:119], v[200:203]\n\t"
    "v_cvt_pk_bf16_f32 v102, v28, v29\n\t"
    "v_cvt_pk_bf16_f32 v103, v30, v31\n\t"
    "v_exp_f32 v33, v33\n\t"
    "v_mfma_f32_16x16x32_bf16 v[10:13], v[40:43], v[96:99], v[10:13]\n\t"
    "v_fma_f32 v34, v34, s55, v6\n\t"
    "v_fma_f32 v35, v35, s55, v6\n\t"
    "v_mfma_f32_16x16x32_bf16 v[10:13], v[44:47], v[100:103], v[10:13]\n\t"
    "v_exp_f32 v34, v34\n\t"
    "v_mfma_f32_16x16x32_bf16 v[204:207], a[240:243], a[136:139], v[204:207]\n\t"
    "v_exp_f32 v35, v35\n\t"
    "v_mfma_f32_16x16x32_bf16 v[204:207], a[244:247], a[140:143], v[204:207]\n\t"
    "s_waitcnt lgkmcnt(0)\n\t"
    "ds_read_b128 a[240:243], v0 offset:56320\n\t"
    "ds_read_b128 a[244:247], v0 offset:56832\n\t"
    "v_fma_f32 v36, v36, s55, v7\n\t"
    "v_fma_f32 v37, v37, s55, v7\n\t"
    "v_mfma_f32_16x16x32_bf16 v[16:19], a[248:251], a[0:3], 0\n\t"
    "v_exp_f32 v36, v36\n\t"
    "v_mfma_f32_16x16x32_bf16 v[16:19], a[252:255], a[4:7], v[16:19]\n\t"
    "v_cvt_pk_bf16_f32 v106, v32, v33\n\t"
    "v_cvt_pk_bf16_f32 v107, v34, v35\n\t"
    "v_mfma_f32_16x16x32_bf16 v[20:23], a[248:251], a[24:27], 0\n\t"
    "v_exp_f32 v37, v37\n\t"
    "v_mfma_f32_16x16x32_bf16 v[20:23], a[252:255], a[28:31], v[20:23]\n\t"
    "buffer_load_dwordx4 v1, s[36:39], s53 offen offset:128 nt lds\n\t"
    "v_fma_f32 v38, v38, s55, v7\n\t"
    "v_fma_f32 v39, v39, s55, v7\n\t"
    "v_exp_f32 v38, v38\n\t"
    "v_mfma_f32_16x16x32_bf16 v[24:27], a[248:251], a[48:51], 0\n\t"
    "s_addk_i32 m0, 896\n\t"
    "v_exp_f32 v39, v39\n\t"
    "v_mfma_f32_16x16x32_bf16 v[24:27], a[252:255], a[52:55], v[24:27]\n\t"
    "v_cvt_pk_bf16_f32 v110, v36, v37\n\t"
    "v_cvt_pk_bf16_f32 v111, v38, v39\n\t"
    "v_mfma_f32_16x16x32_bf16 v[28:31], a[248:251], a[72:75], 0\n\t"
    "v_fma_f32 v184, v184, s55, v2\n\t"
    "v_fma_f32 v185, v185, s55, v2\n\t"
    "v_mfma_f32_16x16x32_bf16 v[28:31], a[252:255], a[76:79], v[28:31]\n\t"
    "v_exp_f32 v184, v184\n\t"
    "v_mfma_f32_16x16x32_bf16 v[12:15], v[40:43], v[104:107], v[12:15]\n\t"
    "v_exp_f32 v185, v185\n\t"
    "v_mfma_f32_16x16x32_bf16 v[12:15], v[44:47], v[108:111], v[12:15]\n\t"
    "v_fma_f32 v186, v186, s55, v2\n\t"
    "v_fma_f32 v187, v187, s55, v2\n\t"
    "v_mfma_f32_16x16x32_bf16 v[32:35], a[248:251], a[96:99], 0\n\t"
    "v_exp_f32 v186, v186\n\t"
    "v_mfma_f32_16x16x32_bf16 v[32:35], a[252:255], a[100:103], v[32:35]\n\t"
    "v_exp_f32 v187, v187\n\t"
    "v_fma_f32 v188, v188, s55, v3\n\t"
    "v_fma_f32 v189, v189, s55, v3\n\t"
    "v_mfma_f32_16x16x32_bf16 v[36:39], a[248:251], a[120:123], 0\n\t"
    "v_exp_f32 v188, v188\n\t"
    "v_mfma_f32_16x16x32_bf16 v[36:39], a[252:255], a[124:127], v[36:39]\n\t"
    "s_waitcnt lgkmcnt(0)\n\t"
    "ds_read_b128 a[248:251], v0 offset:57344\n\t"
    "ds_read_b128 a[252:255], v0 offset:57856\n\t"
    "v_cvt_pk_bf16_f32 v184, v184, v185\n\t"
    "v_cvt_pk_bf16_f32 v186, v186, v187\n\t"
    "v_mfma_f32_16x16x32_bf16 v[16:19], a[240:243], a[8:11], v[16:19]\n\t"
    "v_exp_f32 v189, v189\n\t"
    "v_mfma_f32_16x16x32_bf16 v[16:19], a[244:247], a[12:15], v[16:19]\n\t"
    "buffer_load_dwordx4 v1, s[36:39], s53 offen offset:256 nt lds\n\t"
    "v_fma_f32 v190, v190, s55, v3\n\t"
    "v_fma_f32 v191, v191, s55, v3\n\t"
    "v_mfma_f32_16x16x32_bf16 v[20:23], a[240:243], a[32:35], v[20:23]\n\t"
    "s_mov_b32 s53, s83\n\t"
    "v_accvgpr_write_b32 a168, v184\n\t"
    "v_accvgpr_write_b32 a169, v186\n\t"
    "v_mfma_f32_16x16x32_bf16 v[20:23], a[244:247], a[36:39], v[20:23]\n\t"
    "s_add_u32 m0, s52, 147456\n\t"
    "v_exp_f32 v190, v190\n\t"
    "v_mfma_f32_16x16x32_bf16 v[24:27], a[240:243], a[56:59], v[24:27]\n\t"
    "v_exp_f32 v191, v191\n\t"
    "v_mfma_f32_16x16x32_bf16 v[24:27], a[244:247], a[60:63], v[24:27]\n\t"
    "v_fma_f32 v192, v192, s55, v4\n\t"
    "v_fma_f32 v193, v193, s55, v4\n\t"
    "v_exp_f32 v192, v192\n\t"
    "v_mfma_f32_16x16x32_bf16 v[28:31], a[240:243], a[80:83], v[28:31]\n\t"
    "v_cvt_pk_bf16_f32 v188, v188, v189\n\t"
    "v_cvt_pk_bf16_f32 v190, v190, v191\n\t"
    "v_mfma_f32_16x16x32_bf16 v[28:31], a[244:247], a[84:87], v[28:31]\n\t"
    "v_exp_f32 v193, v193\n\t"
    "v_mfma_f32_16x16x32_bf16 v[32:35], a[240:243], a[104:107], v[32:35]\n\t"
    "v_fma_f32 v194, v194, s55, v4\n\t"
    "v_fma_f32 v195, v195, s55, v4\n\t"
    "v_mfma_f32_16x16x32_bf16 v[32:35], a[244:247], a[108:111], v[32:35]\n\t"
    "v_accvgpr_write_b32 a172, v188\n\t"
    "v_accvgpr_write_b32 a173, v190\n\t"
    "v_mfma_f32_16x16x32_bf16 v[36:39], a[240:243], a[128:131], v[36:39]\n\t"
    "v_exp_f32 v194, v194\n\t"
    "v_mfma_f32_16x16x32_bf16 v[36:39], a[244:247], a[132:135], v[36:39]\n\t"
    "s_waitcnt vmcnt(14) lgkmcnt(0)\n\t"
    "s_barrier\n\t"
    "ds_read_b128 a[240:243], v0 offset:24576\n\t"
    "ds_read_b128 a[244:247], v0 offset:25088\n\t"
    "v_exp_f32 v195, v195\n\t"
    "v_fma_f32 v196, v196, s55, v5\n\t"
    "v_fma_f32 v197, v197, s55, v5\n\t"
    "v_mfma_f32_16x16x32_bf16 v[16:19], a[248:251], a[16:19], v[16:19]\n\t"
    "v_exp_f32 v196, v196\n\t"
    "v_mfma_f32_16x16x32_bf16 v[16:19], a[252:255], a[20:23], v[16:19]\n\t"
    "buffer_load_dwordx4 v1, s[36:39], s53 offen nt lds\n\t"
    "v_cvt_pk_bf16_f32 v192, v192, v193\n\t"
    "v_cvt_pk_bf16_f32 v194, v194, v195\n\t"
    "v_mfma_f32_16x16x32_bf16 v[20:23], a[248:251], a[40:43], v[20:23]\n\t"
    "s_addk_i32 m0, 896\n\t"
    "v_exp_f32 v197, v197\n\t"
    "v_mfma_f32_16x16x32_bf16 v[20:23], a[252:255], a[44:47], v[20:23]\n\t"
    "v_fma_f32 v198, v198, s55, v5\n\t"
    "v_fma_f32 v199, v199, s55, v5\n\t"
    "v_mfma_f32_16x16x32_bf16 v[24:27], a[248:251], a[64:67], v[24:27]\n\t"
    "v_accvgpr_write_b32 a176, v192\n\t"
    "v_accvgpr_write_b32 a177, v194\n\t"
    "v_mfma_f32_16x16x32_bf16 v[24:27], a[252:255], a[68:71], v[24:27]\n\t"
    "v_exp_f32 v198, v198\n\t"
    "v_mfma_f32_16x16x32_bf16 v[28:31], a[248:251], a[88:91], v[28:31]\n\t"
    "v_exp_f32 v199, v199\n\t"
    "v_mfma_f32_16x16x32_bf16 v[28:31], a[252:255], a[92:95], v[28:31]\n\t"
    "v_fma_f32 v200, v200, s55, v6\n\t"
    "v_fma_f32 v201, v201, s55, v6\n\t"
    "v_exp_f32 v200, v200\n\t"
    "v_mfma_f32_16x16x32_bf16 v[32:35], a[248:251], a[112:115], v[32:35]\n\t"
    "v_cvt_pk_bf16_f32 v196, v196, v197\n\t"
    "v_cvt_pk_bf16_f32 v198, v198, v199\n\t"
    "v_mfma_f32_16x16x32_bf16 v[32:35], a[252:255], a[116:119], v[32:35]\n\t"
    "v_exp_f32 v201, v201\n\t"
    "v_mfma_f32_16x16x32_bf16 v[36:39], a[248:251], a[136:139], v[36:39]\n\t"
    "v_fma_f32 v202, v202, s55, v6\n\t"
    "v_fma_f32 v203, v203, s55, v6\n\t"
    "v_mfma_f32_16x16x32_bf16 v[36:39], a[252:255], a[140:143], v[36:39]\n\t"
    "s_waitcnt lgkmcnt(0)\n\t"
    "ds_read_b128 a[248:251], v0 offset:25600\n\t"
    "ds_read_b128 a[252:255], v0 offset:26112\n\t"
    "v_accvgpr_write_b32 a180, v196\n\t"
    "v_accvgpr_write_b32 a181, v198\n\t"
    "v_mfma_f32_16x16x32_bf16 v[112:115], a[240:243], a[0:3], 0\n\t"
    "v_exp_f32 v202, v202\n\t"
    "v_mfma_f32_16x16x32_bf16 v[112:115], a[244:247], a[4:7], v[112:115]\n\t"
    "buffer_store_dwordx4 v[224:227], v63, s[48:51], s54 offen offset:256 sc1\n\t"
    "v_exp_f32 v203, v203\n\t"
    "v_fma_f32 v204, v204, s55, v7\n\t"
    "v_fma_f32 v205, v205, s55, v7\n\t"
    "v_mfma_f32_16x16x32_bf16 v[116:119], a[240:243], a[24:27], 0\n\t"
    "v_exp_f32 v204, v204\n\t"
    "v_mfma_f32_16x16x32_bf16 v[116:119], a[244:247], a[28:31], v[116:119]\n\t"
    "v_cvt_pk_bf16_f32 v200, v200, v201\n\t"
    "v_cvt_pk_bf16_f32 v202, v202, v203\n\t"
    "v_mfma_f32_16x16x32_bf16 v[120:123], a[240:243], a[48:51], 0\n\t"
    "v_exp_f32 v205, v205\n\t"
    "v_mfma_f32_16x16x32_bf16 v[120:123], a[244:247], a[52:55], v[120:123]\n\t"
    "v_fma_f32 v206, v206, s55, v7\n\t"
    "v_fma_f32 v207, v207, s55, v7\n\t"
    "v_mfma_f32_16x16x32_bf16 v[124:127], a[240:243], a[72:75], 0\n\t"
    "v_accvgpr_write_b32 a184, v200\n\t"
    "v_accvgpr_write_b32 a185, v202\n\t"
    "v_mfma_f32_16x16x32_bf16 v[124:127], a[244:247], a[76:79], v[124:127]\n\t"
    "v_exp_f32 v206, v206\n\t"
    "v_exp_f32 v207, v207\n\t"
    "v_mfma_f32_16x16x32_bf16 v[128:131], a[240:243], a[96:99], 0\n\t"
    "v_cvt_pk_bf16_f32 v204, v204, v205\n\t"
    "v_cvt_pk_bf16_f32 v206, v206, v207\n\t"
    "v_mfma_f32_16x16x32_bf16 v[128:131], a[244:247], a[100:103], v[128:131]\n\t"
    "buffer_load_dwordx4 v1, s[36:39], s53 offen offset:128 nt lds\n\t"
    "v_accvgpr_write_b32 a188, v204\n\t"
    "v_accvgpr_write_b32 a189, v206\n\t"
    "v_mfma_f32_16x16x32_bf16 v[132:135], a[240:243], a[120:123], 0\n\t"
    "s_addk_i32 m0, 896\n\t"
    "v_fma_f32 v16, v16, s55, v2\n\t"
    "v_fma_f32 v17, v17, s55, v2\n\t"
    "v_mfma_f32_16x16x32_bf16 v[132:135], a[244:247], a[124:127], v[132:135]\n\t"
    "s_waitcnt lgkmcnt(0)\n\t"
    "ds_read_b128 a[240:243], v0 offset:26624\n\t"
    "ds_read_b128 a[244:247], v0 offset:27136\n\t"
    "v_exp_f32 v16, v16\n\t"
    "v_exp_f32 v17, v17\n\t"
    "v_mfma_f32_16x16x32_bf16 v[112:115], a[248:251], a[8:11], v[112:115]\n\t"
    "v_fma_f32 v18, v18, s55, v2\n\t"
    "v_fma_f32 v19, v19, s55, v2\n\t"
    "v_mfma_f32_16x16x32_bf16 v[112:115], a[252:255], a[12:15], v[112:115]\n\t"
    "v_exp_f32 v18, v18\n\t"
    "v_mfma_f32_16x16x32_bf16 v[116:119], a[248:251], a[32:35], v[116:119]\n\t"
    "v_exp_f32 v19, v19\n\t"
    "v_mfma_f32_16x16x32_bf16 v[116:119], a[252:255], a[36:39], v[116:119]\n\t"
    "v_fma_f32 v20, v20, s55, v3\n\t"
    "v_fma_f32 v21, v21, s55, v3\n\t"
    "v_mfma_f32_16x16x32_bf16 v[120:123], a[248:251], a[56:59], v[120:123]\n\t"
    "v_exp_f32 v20, v20\n\t"
    "v_mfma_f32_16x16x32_bf16 v[120:123], a[252:255], a[60:63], v[120:123]\n\t"
    "v_cvt_pk_bf16_f32 v16, v16, v17\n\t"
    "v_cvt_pk_bf16_f32 v18, v18, v19\n\t"
    "v_exp_f32 v21, v21\n\t"
    "v_mfma_f32_16x16x32_bf16 v[124:127], a[248:251], a[80:83], v[124:127]\n\t"
    "v_fma_f32 v22, v22, s55, v3\n\t"
    "v_fma_f32 v23, v23, s55, v3\n\t"
    "v_mfma_f32_16x16x32_bf16 v[124:127], a[252:255], a[84:87], v[124:127]\n\t"
    "v_accvgpr_write_b32 a170, v16\n\t"
    "v_accvgpr_write_b32 a171, v18\n\t"
    "v_mfma_f32_16x16x32_bf16 v[128:131], a[248:251], a[104:107], v[128:131]\n\t"
    "v_exp_f32 v22, v22\n\t"
    "v_mfma_f32_16x16x32_bf16 v[128:131], a[252:255], a[108:111], v[128:131]\n\t"
    "buffer_load_dwordx4 v1, s[36:39], s53 offen offset:256 nt lds\n\t"
    "v_exp_f32 v23, v23\n\t"
    "v_fma_f32 v24, v24, s55, v4\n\t"
    "v_fma_f32 v25, v25, s55, v4\n\t"
    "v_mfma_f32_16x16x32_bf16 v[132:135], a[248:251], a[128:131], v[132:135]\n\t"
    "s_mov_b32 s53, 24576\n\t"
    "v_exp_f32 v24, v24\n\t"
    "v_mfma_f32_16x16x32_bf16 v[132:135], a[252:255], a[132:135], v[132:135]\n\t"
    "s_waitcnt lgkmcnt(0)\n\t"
    "ds_read_b128 a[248:251], v0 offset:30720\n\t"
    "ds_read_b128 a[252:255], v0 offset:31232\n\t"
    "v_cvt_pk_bf16_f32 v20, v20, v21\n\t"
    "v_cvt_pk_bf16_f32 v22, v22, v23\n\t"
    "v_mfma_f32_16x16x32_bf16 v[112:115], a[240:243], a[16:19], v[112:115]\n\t"
    "s_add_u32 m0, s52, 86016\n\t"
    "v_exp_f32 v25, v25\n\t"
    "v_mfma_f32_16x16x32_bf16 v[112:115], a[244:247], a[20:23], v[112:115]\n\t"
    "v_fma_f32 v26, v26, s55, v4\n\t"
    "v_fma_f32 v27, v27, s55, v4\n\t"
    "v_accvgpr_write_b32 a174, v20\n\t"
    "v_accvgpr_write_b32 a175, v22\n\t"
    "v_mfma_f32_16x16x32_bf16 v[116:119], a[240:243], a[40:43], v[116:119]\n\t"
    "v_exp_f32 v26, v26\n\t"
    "v_mfma_f32_16x16x32_bf16 v[116:119], a[244:247], a[44:47], v[116:119]\n\t"
    "v_exp_f32 v27, v27\n\t"
    "v_mfma_f32_16x16x32_bf16 v[8:11], v[40:43], a[168:171], v[8:11]\n\t"
    "v_fma_f32 v28, v28, s55, v5\n\t"
    "v_fma_f32 v29, v29, s55, v5\n\t"
    "v_mfma_f32_16x16x32_bf16 v[8:11], v[44:47], a[172:175], v[8:11]\n\t"
    "v_exp_f32 v28, v28\n\t"
    "v_cvt_pk_bf16_f32 v24, v24, v25\n\t"
    "v_cvt_pk_bf16_f32 v26, v26, v27\n\t"
    "v_mfma_f32_16x16x32_bf16 v[120:123], a[240:243], a[64:67], v[120:123]\n\t"
    "v_exp_f32 v29, v29\n\t"
    "v_mfma_f32_16x16x32_bf16 v[120:123], a[244:247], a[68:71], v[120:123]\n\t"
    "v_fma_f32 v30, v30, s55, v5\n\t"
    "v_fma_f32 v31, v31, s55, v5\n\t"
    "v_mfma_f32_16x16x32_bf16 v[124:127], a[240:243], a[88:91], v[124:127]\n\t"
    "v_accvgpr_write_b32 a178, v24\n\t"
    "v_accvgpr_write_b32 a179, v26\n\t"
    "v_mfma_f32_16x16x32_bf16 v[124:127], a[244:247], a[92:95], v[124:127]\n\t"
    "buffer_load_dwordx4 v1, s[40:43], s53 offen nt lds\n\t"
    "v_exp_f32 v30, v30\n\t"
    "v_exp_f32 v31, v31\n\t"
    "v_mfma_f32_16x16x32_bf16 v[128:131], a[240:243], a[112:115], v[128:131]\n\t"
    "s_addk_i32 m0, 896\n\t"
    "v_fma_f32 v32, v32, s55, v6\n\t"
    "v_fma_f32 v33, v33, s55, v6\n\t"
    "v_mfma_f32_16x16x32_bf16 v[128:131], a[244:247], a[116:119], v[128:131]\n\t"
    "v_exp_f32 v32, v32\n\t"
    "v_mfma_f32_16x16x32_bf16 v[132:135], a[240:243], a[136:139], v[132:135]\n\t"
    "v_cvt_pk_bf16_f32 v28, v28, v29\n\t"
    "v_cvt_pk_bf16_f32 v30, v30, v31\n\t"
    "v_mfma_f32_16x16x32_bf16 v[132:135], a[244:247], a[140:143], v[132:135]\n\t"
    "s_waitcnt lgkmcnt(0)\n\t"
    "ds_read_b128 a[240:243], v0 offset:31744\n\t"
    "ds_read_b128 a[244:247], v0 offset:32256\n\t"
    "v_exp_f32 v33, v33\n\t"
    "v_mfma_f32_16x16x32_bf16 v[16:19], a[248:251], a[0:3], 0\n\t"
    "v_fma_f32 v34, v34, s55, v6\n\t"
    "v_fma_f32 v35, v35, s55, v6\n\t"
    "v_mfma_f32_16x16x32_bf16 v[16:19], a[252:255], a[4:7], v[16:19]\n\t"
    "v_accvgpr_write_b32 a182, v28\n\t"
    "v_accvgpr_write_b32 a183, v30\n\t"
    "v_exp_f32 v34, v34\n\t"
    "v_mfma_f32_16x16x32_bf16 v[10:13], v[40:43], a[176:179], v[10:13]\n\t"
    "v_exp_f32 v35, v35\n\t"
    "v_mfma_f32_16x16x32_bf16 v[10:13], v[44:47], a[180:183], v[10:13]\n\t"
    "v_fma_f32 v36, v36, s55, v7\n\t"
    "v_fma_f32 v37, v37, s55, v7\n\t"
    "v_mfma_f32_16x16x32_bf16 v[20:23], a[248:251], a[24:27], 0\n\t"
    "v_exp_f32 v36, v36\n\t"
    "v_mfma_f32_16x16x32_bf16 v[20:23], a[252:255], a[28:31], v[20:23]\n\t"
    "v_cvt_pk_bf16_f32 v32, v32, v33\n\t"
    "v_cvt_pk_bf16_f32 v34, v34, v35\n\t"
    "v_mfma_f32_16x16x32_bf16 v[24:27], a[248:251], a[48:51], 0\n\t"
    "v_exp_f32 v37, v37\n\t"
    "v_mfma_f32_16x16x32_bf16 v[24:27], a[252:255], a[52:55], v[24:27]\n\t"
    "v_fma_f32 v38, v38, s55, v7\n\t"
    "v_fma_f32 v39, v39, s55, v7\n\t"
    "v_accvgpr_write_b32 a186, v32\n\t"
    "v_accvgpr_write_b32 a187, v34\n\t"
    "v_mfma_f32_16x16x32_bf16 v[28:31], a[248:251], a[72:75], 0\n\t"
    "v_exp_f32 v38, v38\n\t"
    "v_mfma_f32_16x16x32_bf16 v[28:31], a[252:255], a[76:79], v[28:31]\n\t"
    "buffer_load_dwordx4 v1, s[40:43], s53 offen offset:128 nt lds\n\t"
    "v_exp_f32 v39, v39\n\t"
    "v_mfma_f32_16x16x32_bf16 v[32:35], a[248:251], a[96:99], 0\n\t"
    "s_addk_i32 m0, 896\n\t"
    "v_cvt_pk_bf16_f32 v36, v36, v37\n\t"
    "v_cvt_pk_bf16_f32 v38, v38, v39\n\t"
    "v_mfma_f32_16x16x32_bf16 v[32:35], a[252:255], a[100:103], v[32:35]\n\t"
    "v_accvgpr_write_b32 a190, v36\n\t"
    "v_accvgpr_write_b32 a191, v38\n\t"
    "v_mfma_f32_16x16x32_bf16 v[36:39], a[248:251], a[120:123], 0\n\t"
    "v_fma_f32 v112, v112, s55, v2\n\t"
    "v_fma_f32 v113, v113, s55, v2\n\t"
    "v_mfma_f32_16x16x32_bf16 v[36:39], a[252:255], a[124:127], v[36:39]\n\t"
    "s_waitcnt lgkmcnt(0)\n\t"
    "ds_read_b128 a[248:251], v0 offset:32768\n\t"
    "ds_read_b128 a[252:255], v0 offset:33280\n\t"
    "v_exp_f32 v112, v112\n\t"
    "v_mfma_f32_16x16x32_bf16 v[12:15], v[40:43], a[184:187], v[12:15]\n\t"
    "v_exp_f32 v113, v113\n\t"
    "v_mfma_f32_16x16x32_bf16 v[12:15], v[44:47], a[188:191], v[12:15]\n\t"
    "v_fma_f32 v114, v114, s55, v2\n\t"
    "v_fma_f32 v115, v115, s55, v2\n\t"
    "v_exp_f32 v114, v114\n\t"
    "v_mfma_f32_16x16x32_bf16 v[16:19], a[240:243], a[8:11], v[16:19]\n\t"
    "v_exp_f32 v115, v115\n\t"
    "v_mfma_f32_16x16x32_bf16 v[16:19], a[244:247], a[12:15], v[16:19]\n\t"
    "v_fma_f32 v116, v116, s55, v3\n\t"
    "v_fma_f32 v117, v117, s55, v3\n\t"
    "v_mfma_f32_16x16x32_bf16 v[20:23], a[240:243], a[32:35], v[20:23]\n\t"
    "v_exp_f32 v116, v116\n\t"
    "v_mfma_f32_16x16x32_bf16 v[20:23], a[244:247], a[36:39], v[20:23]\n\t"
    "v_cvt_pk_bf16_f32 v112, v112, v113\n\t"
    "v_cvt_pk_bf16_f32 v113, v114, v115\n\t"
    "v_mfma_f32_16x16x32_bf16 v[24:27], a[240:243], a[56:59], v[24:27]\n\t"
    "v_exp_f32 v117, v117\n\t"
    "v_mfma_f32_16x16x32_bf16 v[24:27], a[244:247], a[60:63], v[24:27]\n\t"
    "buffer_load_dwordx4 v1, s[40:43], s53 offen offset:256 nt lds\n\t"
    "v_fma_f32 v118, v118, s55, v3\n\t"
    "v_fma_f32 v119, v119, s55, v3\n\t"
    "v_mfma_f32_16x16x32_bf16 v[28:31], a[240:243], a[80:83], v[28:31]\n\t"
    "s_mov_b32 s53, 27648\n\t"
    "v_exp_f32 v118, v118\n\t"
    "v_mfma_f32_16x16x32_bf16 v[28:31], a[244:247], a[84:87], v[28:31]\n\t"
    "s_add_u32 m0, s52, 0\n\t"
    "v_exp_f32 v119, v119\n\t"
    "v_mfma_f32_16x16x32_bf16 v[32:35], a[240:243], a[104:107], v[32:35]\n\t"
    "v_fma_f32 v120, v120, s55, v4\n\t"
    "v_fma_f32 v121, v121, s55, v4\n\t"
    "v_mfma_f32_16x16x32_bf16 v[32:35], a[244:247], a[108:111], v[32:35]\n\t"
    "v_exp_f32 v120, v120\n\t"
    "v_cvt_pk_bf16_f32 v116, v116, v117\n\t"
    "v_cvt_pk_bf16_f32 v117, v118, v119\n\t"
    "v_mfma_f32_16x16x32_bf16 v[36:39], a[240:243], a[128:131], v[36:39]\n\t"
    "v_exp_f32 v121, v121\n\t"
    "v_mfma_f32_16x16x32_bf16 v[36:39], a[244:247], a[132:135], v[36:39]\n\t"
    "s_waitcnt lgkmcnt(0)\n\t"
    "v_add_u32 v0, 0xf000, v0\n\t"
    "ds_read_b128 a[240:243], v0 offset:49152\n\t"
    "ds_read_b128 a[244:247], v0 offset:49664\n\t"
    "v_fma_f32 v122, v122, s55, v4\n\t"
    "v_fma_f32 v123, v123, s55, v4\n\t"
    "v_mfma_f32_16x16x32_bf16 v[16:19], a[248:251], a[16:19], v[16:19]\n\t"
    "v_exp_f32 v122, v122\n\t"
    "v_mfma_f32_16x16x32_bf16 v[16:19], a[252:255], a[20:23], v[16:19]\n\t"
    "v_exp_f32 v123, v123\n\t"
    "v_mfma_f32_16x16x32_bf16 v[20:23], a[248:251], a[40:43], v[20:23]\n\t"
    "v_fma_f32 v124, v124, s55, v5\n\t"
    "v_fma_f32 v125, v125, s55, v5\n\t"
    "v_mfma_f32_16x16x32_bf16 v[20:23], a[252:255], a[44:47], v[20:23]\n\t"
    "v_exp_f32 v124, v124\n\t"
    "v_mfma_f32_16x16x32_bf16 v[24:27], a[248:251], a[64:67], v[24:27]\n\t"
    "v_cvt_pk_bf16_f32 v120, v120, v121\n\t"
    "v_cvt_pk_bf16_f32 v121, v122, v123\n\t"
    "v_mfma_f32_16x16x32_bf16 v[24:27], a[252:255], a[68:71], v[24:27]\n\t"
    "buffer_store_dwordx4 v[228:231], v63, s[48:51], s54 offen offset:3328 sc1\n\t"
    "v_exp_f32 v125, v125\n\t"
    "v_fma_f32 v126, v126, s55, v5\n\t"
    "v_fma_f32 v127, v127, s55, v5\n\t"
    "v_mfma_f32_16x16x32_bf16 v[28:31], a[248:251], a[88:91], v[28:31]\n\t"
    "s_mov_b32 s54, 18432\n\t"
    "v_exp_f32 v126, v126\n\t"
    "v_mfma_f32_16x16x32_bf16 v[28:31], a[252:255], a[92:95], v[28:31]\n\t"
    "v_exp_f32 v127, v127\n\t"
    "v_mfma_f32_16x16x32_bf16 v[32:35], a[248:251], a[112:115], v[32:35]\n\t"
    "v_fma_f32 v128, v128, s55, v6\n\t"
    "v_fma_f32 v129, v129, s55, v6\n\t"
    "v_mfma_f32_16x16x32_bf16 v[32:35], a[252:255], a[116:119], v[32:35]\n\t"
    "v_exp_f32 v128, v128\n\t"
    "v_mfma_f32_16x16x32_bf16 v[36:39], a[248:251], a[136:139], v[36:39]\n\t"
    "v_cvt_pk_bf16_f32 v124, v124, v125\n\t"
    "v_cvt_pk_bf16_f32 v125, v126, v127\n\t"
    "v_mfma_f32_16x16x32_bf16 v[36:39], a[252:255], a[140:143], v[36:39]\n\t"
    "s_waitcnt lgkmcnt(0)\n\t"
    "ds_read_b128 a[248:251], v0 offset:50176\n\t"
    "ds_read_b128 a[252:255], v0 offset:50688\n\t"
    "v_exp_f32 v129, v129\n\t"
    "v_mfma_f32_16x16x32_bf16 v[184:187], a[240:243], a[0:3], 0\n\t"
    "v_fma_f32 v130, v130, s55, v6\n\t"
    "v_fma_f32 v131, v131, s55, v6\n\t"
    "v_mfma_f32_16x16x32_bf16 v[184:187], a[244:247], a[4:7], v[184:187]\n\t"
    "v_exp_f32 v130, v130\n\t"
    "v_exp_f32 v131, v131\n\t"
    "v_mfma_f32_16x16x32_bf16 v[188:191], a[240:243], a[24:27], 0\n\t"
    "v_fma_f32 v132, v132, s55, v7\n\t"
    "v_fma_f32 v133, v133, s55, v7\n\t"
    "v_mfma_f32_16x16x32_bf16 v[188:191], a[244:247], a[28:31], v[188:191]\n\t"
    "buffer_load_dwordx4 v1, s[40:43], s53 offen nt lds\n\t"
    "v_exp_f32 v132, v132\n\t"
    "v_mfma_f32_16x16x32_bf16 v[192:195], a[240:243], a[48:51], 0\n\t"
    "s_addk_i32 m0, 896\n\t"
    "v_cvt_pk_bf16_f32 v128, v128, v129\n\t"
    "v_cvt_pk_bf16_f32 v129, v130, v131\n\t"
    "v_mfma_f32_16x16x32_bf16 v[192:195], a[244:247], a[52:55], v[192:195]\n\t"
    "v_exp_f32 v133, v133\n\t"
    "v_mfma_f32_16x16x32_bf16 v[196:199], a[240:243], a[72:75], 0\n\t"
    "v_fma_f32 v134, v134, s55, v7\n\t"
    "v_fma_f32 v135, v135, s55, v7\n\t"
    "v_mfma_f32_16x16x32_bf16 v[196:199], a[244:247], a[76:79], v[196:199]\n\t"
    "v_exp_f32 v134, v134\n\t"
    "v_exp_f32 v135, v135\n\t"
    "v_mfma_f32_16x16x32_bf16 v[200:203], a[240:243], a[96:99], 0\n\t"
    "v_cvt_pk_bf16_f32 v132, v132, v133\n\t"
    "v_cvt_pk_bf16_f32 v133, v134, v135\n\t"
    "v_mfma_f32_16x16x32_bf16 v[200:203], a[244:247], a[100:103], v[200:203]\n\t"
    "v_fma_f32 v16, v16, s55, v2\n\t"
    "v_fma_f32 v17, v17, s55, v2\n\t"
    "v_mfma_f32_16x16x32_bf16 v[204:207], a[240:243], a[120:123], 0\n\t"
    "v_exp_f32 v16, v16\n\t"
    "v_mfma_f32_16x16x32_bf16 v[204:207], a[244:247], a[124:127], v[204:207]\n\t"
    "s_waitcnt lgkmcnt(0)\n\t"
    "ds_read_b128 a[240:243], v0 offset:51200\n\t"
    "ds_read_b128 a[244:247], v0 offset:51712\n\t"
    "v_exp_f32 v17, v17\n\t"
    "v_mfma_f32_16x16x32_bf16 v[184:187], a[248:251], a[8:11], v[184:187]\n\t"
    "v_fma_f32 v18, v18, s55, v2\n\t"
    "v_fma_f32 v19, v19, s55, v2\n\t"
    "v_mfma_f32_16x16x32_bf16 v[184:187], a[252:255], a[12:15], v[184:187]\n\t"
    "v_exp_f32 v18, v18\n\t"
    "v_mfma_f32_16x16x32_bf16 v[188:191], a[248:251], a[32:35], v[188:191]\n\t"
    "v_exp_f32 v19, v19\n\t"
    "v_mfma_f32_16x16x32_bf16 v[188:191], a[252:255], a[36:39], v[188:191]\n\t"
    "buffer_load_dwordx4 v1, s[40:43], s53 offen offset:128 nt lds\n\t"
    "v_fma_f32 v20, v20, s55, v3\n\t"
    "v_fma_f32 v21, v21, s55, v3\n\t"
    "v_exp_f32 v20, v20\n\t"
    "v_mfma_f32_16x16x32_bf16 v[192:195], a[248:251], a[56:59], v[192:195]\n\t"
    "s_addk_i32 m0, 896\n\t"
    "v_cvt_pk_bf16_f32 v114, v16, v17\n\t"
    "v_cvt_pk_bf16_f32 v115, v18, v19\n\t"
    "v_mfma_f32_16x16x32_bf16 v[192:195], a[252:255], a[60:63], v[192:195]\n\t"
    "v_exp_f32 v21, v21\n\t"
    "v_mfma_f32_16x16x32_bf16 v[196:199], a[248:251], a[80:83], v[196:199]\n\t"
    "v_fma_f32 v22, v22, s55, v3\n\t"
    "v_fma_f32 v23, v23, s55, v3\n\t"
    "v_mfma_f32_16x16x32_bf16 v[196:199], a[252:255], a[84:87], v[196:199]\n\t"
    "v_exp_f32 v22, v22\n\t"
    "v_mfma_f32_16x16x32_bf16 v[200:203], a[248:251], a[104:107], v[200:203]\n\t"
    "v_exp_f32 v23, v23\n\t"
    "v_mfma_f32_16x16x32_bf16 v[200:203], a[252:255], a[108:111], v[200:203]\n\t"
    "v_fma_f32 v24, v24, s55, v4\n\t"
    "v_fma_f32 v25, v25, s55, v4\n\t"
    "v_exp_f32 v24, v24\n\t"
    "v_mfma_f32_16x16x32_bf16 v[204:207], a[248:251], a[128:131], v[204:207]\n\t"
    "v_cvt_pk_bf16_f32 v118, v20, v21\n\t"
    "v_cvt_pk_bf16_f32 v119, v22, v23\n\t"
    "v_mfma_f32_16x16x32_bf16 v[204:207], a[252:255], a[132:135], v[204:207]\n\t"
    "s_waitcnt lgkmcnt(0)\n\t"
    "ds_read_b128 a[248:251], v0 offset:55296\n\t"
    "ds_read_b128 a[252:255], v0 offset:55808\n\t"
    "v_exp_f32 v25, v25\n\t"
    "v_mfma_f32_16x16x32_bf16 v[8:11], v[40:43], v[112:115], v[8:11]\n\t"
    "v_fma_f32 v26, v26, s55, v4\n\t"
    "v_fma_f32 v27, v27, s55, v4\n\t"
    "v_mfma_f32_16x16x32_bf16 v[8:11], v[44:47], v[116:119], v[8:11]\n\t"
    "v_exp_f32 v26, v26\n\t"
    "v_mfma_f32_16x16x32_bf16 v[184:187], a[240:243], a[16:19], v[184:187]\n\t"
    "v_exp_f32 v27, v27\n\t"
    "v_mfma_f32_16x16x32_bf16 v[184:187], a[244:247], a[20:23], v[184:187]\n\t"
    "buffer_load_dwordx4 v1, s[40:43], s53 offen offset:256 nt lds\n\t"
    "v_fma_f32 v28, v28, s55, v5\n\t"
    "v_fma_f32 v29, v29, s55, v5\n\t"
    "v_exp_f32 v28, v28\n\t"
    "v_mfma_f32_16x16x32_bf16 v[188:191], a[240:243], a[40:43], v[188:191]\n\t"
    "s_mov_b32 s53, s65\n\t"
    "v_cvt_pk_bf16_f32 v122, v24, v25\n\t"
    "v_cvt_pk_bf16_f32 v123, v26, v27\n\t"
    "v_mfma_f32_16x16x32_bf16 v[188:191], a[244:247], a[44:47], v[188:191]\n\t"
    "s_add_u32 m0, s52, 49152\n\t"
    "v_exp_f32 v29, v29\n\t"
    "v_mfma_f32_16x16x32_bf16 v[192:195], a[240:243], a[64:67], v[192:195]\n\t"
    "v_fma_f32 v30, v30, s55, v5\n\t"
    "v_fma_f32 v31, v31, s55, v5\n\t"
    "v_mfma_f32_16x16x32_bf16 v[192:195], a[244:247], a[68:71], v[192:195]\n\t"
    "v_exp_f32 v30, v30\n\t"
    "v_mfma_f32_16x16x32_bf16 v[196:199], a[240:243], a[88:91], v[196:199]\n\t"
    "v_exp_f32 v31, v31\n\t"
    "v_mfma_f32_16x16x32_bf16 v[196:199], a[244:247], a[92:95], v[196:199]\n\t"
    "v_fma_f32 v32, v32, s55, v6\n\t"
    "v_fma_f32 v33, v33, s55, v6\n\t"
    "v_mfma_f32_16x16x32_bf16 v[200:203], a[240:243], a[112:115], v[200:203]\n\t"
    "v_exp_f32 v32, v32\n\t"
    "v_mfma_f32_16x16x32_bf16 v[200:203], a[244:247], a[116:119], v[200:203]\n\t"
    "v_cvt_pk_bf16_f32 v126, v28, v29\n\t"
    "v_cvt_pk_bf16_f32 v127, v30, v31\n\t"
    "v_exp_f32 v33, v33\n\t"
    "v_mfma_f32_16x16x32_bf16 v[10:13], v[40:43], v[120:123], v[10:13]\n\t"
    "v_fma_f32 v34, v34, s55, v6\n\t"
    "v_fma_f32 v35, v35, s55, v6\n\t"
    "v_mfma_f32_16x16x32_bf16 v[10:13], v[44:47], v[124:127], v[10:13]\n\t"
    "v_exp_f32 v34, v34\n\t"
    "v_mfma_f32_16x16x32_bf16 v[204:207], a[240:243], a[136:139], v[204:207]\n\t"
    "v_exp_f32 v35, v35\n\t"
    "v_mfma_f32_16x16x32_bf16 v[204:207], a[244:247], a[140:143], v[204:207]\n\t"
    "s_waitcnt lgkmcnt(0)\n\t"
    "ds_read_b128 a[240:243], v0 offset:56320\n\t"
    "ds_read_b128 a[244:247], v0 offset:56832\n\t"
    "v_fma_f32 v36, v36, s55, v7\n\t"
    "v_fma_f32 v37, v37, s55, v7\n\t"
    "v_mfma_f32_16x16x32_bf16 v[16:19], a[248:251], a[0:3], 0\n\t"
    "v_exp_f32 v36, v36\n\t"
    "v_mfma_f32_16x16x32_bf16 v[16:19], a[252:255], a[4:7], v[16:19]\n\t"
    "buffer_load_dwordx4 v1, s[40:43], s53 offen nt lds\n\t"
    "v_cvt_pk_bf16_f32 v130, v32, v33\n\t"
    "v_cvt_pk_bf16_f32 v131, v34, v35\n\t"
    "v_mfma_f32_16x16x32_bf16 v[20:23], a[248:251], a[24:27], 0\n\t"
    "s_addk_i32 m0, 896\n\t"
    "v_exp_f32 v37, v37\n\t"
    "v_mfma_f32_16x16x32_bf16 v[20:23], a[252:255], a[28:31], v[20:23]\n\t"
    "v_fma_f32 v38, v38, s55, v7\n\t"
    "v_fma_f32 v39, v39, s55, v7\n\t"
    "v_exp_f32 v38, v38\n\t"
    "v_mfma_f32_16x16x32_bf16 v[24:27], a[248:251], a[48:51], 0\n\t"
    "v_exp_f32 v39, v39\n\t"
    "v_mfma_f32_16x16x32_bf16 v[24:27], a[252:255], a[52:55], v[24:27]\n\t"
    "v_cvt_pk_bf16_f32 v134, v36, v37\n\t"
    "v_cvt_pk_bf16_f32 v135, v38, v39\n\t"
    "v_mfma_f32_16x16x32_bf16 v[28:31], a[248:251], a[72:75], 0\n\t"
    "v_fma_f32 v184, v184, s55, v2\n\t"
    "v_fma_f32 v185, v185, s55, v2\n\t"
    "v_mfma_f32_16x16x32_bf16 v[28:31], a[252:255], a[76:79], v[28:31]\n\t"
    "v_exp_f32 v184, v184\n\t"
    "v_mfma_f32_16x16x32_bf16 v[12:15], v[40:43], v[128:131], v[12:15]\n\t"
    "v_exp_f32 v185, v185\n\t"
    "v_mfma_f32_16x16x32_bf16 v[12:15], v[44:47], v[132:135], v[12:15]\n\t"
    "v_fma_f32 v186, v186, s55, v2\n\t"
    "v_fma_f32 v187, v187, s55, v2\n\t"
    "v_exp_f32 v186, v186\n\t"
    "v_mfma_f32_16x16x32_bf16 v[32:35], a[248:251], a[96:99], 0\n\t"
    "v_exp_f32 v187, v187\n\t"
    "v_mfma_f32_16x16x32_bf16 v[32:35], a[252:255], a[100:103], v[32:35]\n\t"
    "v_fma_f32 v188, v188, s55, v3\n\t"
    "v_fma_f32 v189, v189, s55, v3\n\t"
    "v_mfma_f32_16x16x32_bf16 v[36:39], a[248:251], a[120:123], 0\n\t"
    "v_exp_f32 v188, v188\n\t"
    "v_mfma_f32_16x16x32_bf16 v[36:39], a[252:255], a[124:127], v[36:39]\n\t"
    "s_waitcnt lgkmcnt(0)\n\t"
    "ds_read_b128 a[248:251], v0 offset:57344\n\t"
    "ds_read_b128 a[252:255], v0 offset:57856\n\t"
    "v_cvt_pk_bf16_f32 v184, v184, v185\n\t"
    "v_cvt_pk_bf16_f32 v186, v186, v187\n\t"
    "v_mfma_f32_16x16x32_bf16 v[16:19], a[240:243], a[8:11], v[16:19]\n\t"
    "v_exp_f32 v189, v189\n\t"
    "v_mfma_f32_16x16x32_bf16 v[16:19], a[244:247], a[12:15], v[16:19]\n\t"
    "buffer_load_dwordx4 v1, s[40:43], s53 offen offset:128 nt lds\n\t"
    "v_fma_f32 v190, v190, s55, v3\n\t"
    "v_fma_f32 v191, v191, s55, v3\n\t"
    "v_mfma_f32_16x16x32_bf16 v[20:23], a[240:243], a[32:35], v[20:23]\n\t"
    "s_addk_i32 m0, 896\n\t"
    "v_accvgpr_write_b32 a192, v184\n\t"
    "v_accvgpr_write_b32 a193, v186\n\t"
    "v_mfma_f32_16x16x32_bf16 v[20:23], a[244:247], a[36:39], v[20:23]\n\t"
    "v_exp_f32 v190, v190\n\t"
    "v_mfma_f32_16x16x32_bf16 v[24:27], a[240:243], a[56:59], v[24:27]\n\t"
    "v_exp_f32 v191, v191\n\t"
    "v_mfma_f32_16x16x32_bf16 v[24:27], a[244:247], a[60:63], v[24:27]\n\t"
    "v_fma_f32 v192, v192, s55, v4\n\t"
    "v_fma_f32 v193, v193, s55, v4\n\t"
    "v_exp_f32 v192, v192\n\t"
    "v_mfma_f32_16x16x32_bf16 v[28:31], a[240:243], a[80:83], v[28:31]\n\t"
    "v_cvt_pk_bf16_f32 v188, v188, v189\n\t"
    "v_cvt_pk_bf16_f32 v190, v190, v191\n\t"
    "v_mfma_f32_16x16x32_bf16 v[28:31], a[244:247], a[84:87], v[28:31]\n\t"
    "v_exp_f32 v193, v193\n\t"
    "v_mfma_f32_16x16x32_bf16 v[32:35], a[240:243], a[104:107], v[32:35]\n\t"
    "v_fma_f32 v194, v194, s55, v4\n\t"
    "v_fma_f32 v195, v195, s55, v4\n\t"
    "v_mfma_f32_16x16x32_bf16 v[32:35], a[244:247], a[108:111], v[32:35]\n\t"
    "v_accvgpr_write_b32 a196, v188\n\t"
    "v_accvgpr_write_b32 a197, v190\n\t"
    "v_mfma_f32_16x16x32_bf16 v[36:39], a[240:243], a[128:131], v[36:39]\n\t"
    "v_exp_f32 v194, v194\n\t"
    "v_mfma_f32_16x16x32_bf16 v[36:39], a[244:247], a[132:135], v[36:39]\n\t"
    "s_waitcnt vmcnt(20) lgkmcnt(0)\n\t"
    "s_barrier\n\t"
    "ds_read_b128 a[240:243], v0 offset:12288\n\t"
    "ds_read_b128 a[244:247], v0 offset:12800\n\t"
    "v_exp_f32 v195, v195\n\t"
    "v_fma_f32 v196, v196, s55, v5\n\t"
    "v_fma_f32 v197, v197, s55, v5\n\t"
    "v_mfma_f32_16x16x32_bf16 v[16:19], a[248:251], a[16:19], v[16:19]\n\t"
    "v_exp_f32 v196, v196\n\t"
    "v_mfma_f32_16x16x32_bf16 v[16:19], a[252:255], a[20:23], v[16:19]\n\t"
    "buffer_store_dwordx4 v[232:235], v63, s[48:51], s54 offen offset:256 sc1\n\t"
    "v_cvt_pk_bf16_f32 v192, v192, v193\n\t"
    "v_cvt_pk_bf16_f32 v194, v194, v195\n\t"
    "v_mfma_f32_16x16x32_bf16 v[20:23], a[248:251], a[40:43], v[20:23]\n\t"
    "v_exp_f32 v197, v197\n\t"
    "v_mfma_f32_16x16x32_bf16 v[20:23], a[252:255], a[44:47], v[20:23]\n\t"
    "v_fma_f32 v198, v198, s55, v5\n\t"
    "v_fma_f32 v199, v199, s55, v5\n\t"
    "v_mfma_f32_16x16x32_bf16 v[24:27], a[248:251], a[64:67], v[24:27]\n\t"
    "v_accvgpr_write_b32 a200, v192\n\t"
    "v_accvgpr_write_b32 a201, v194\n\t"
    "v_mfma_f32_16x16x32_bf16 v[24:27], a[252:255], a[68:71], v[24:27]\n\t"
    "v_exp_f32 v198, v198\n\t"
    "v_mfma_f32_16x16x32_bf16 v[28:31], a[248:251], a[88:91], v[28:31]\n\t"
    "v_exp_f32 v199, v199\n\t"
    "v_mfma_f32_16x16x32_bf16 v[28:31], a[252:255], a[92:95], v[28:31]\n\t"
    "v_fma_f32 v200, v200, s55, v6\n\t"
    "v_fma_f32 v201, v201, s55, v6\n\t"
    "v_exp_f32 v200, v200\n\t"
    "v_mfma_f32_16x16x32_bf16 v[32:35], a[248:251], a[112:115], v[32:35]\n\t"
    "v_cvt_pk_bf16_f32 v196, v196, v197\n\t"
    "v_cvt_pk_bf16_f32 v198, v198, v199\n\t"
    "v_mfma_f32_16x16x32_bf16 v[32:35], a[252:255], a[116:119], v[32:35]\n\t"
    "buffer_load_dwordx4 v1, s[40:43], s53 offen offset:256 nt lds\n\t"
    "v_exp_f32 v201, v201\n\t"
    "v_mfma_f32_16x16x32_bf16 v[36:39], a[248:251], a[136:139], v[36:39]\n\t"
    "s_mov_b32 s53, s75\n\t"
    "v_fma_f32 v202, v202, s55, v6\n\t"
    "v_fma_f32 v203, v203, s55, v6\n\t"
    "v_mfma_f32_16x16x32_bf16 v[36:39], a[252:255], a[140:143], v[36:39]\n\t"
    "s_waitcnt lgkmcnt(0)\n\t"
    "ds_read_b128 a[248:251], v0 offset:13312\n\t"
    "ds_read_b128 a[252:255], v0 offset:13824\n\t"
    "v_accvgpr_write_b32 a204, v196\n\t"
    "v_accvgpr_write_b32 a205, v198\n\t"
    "v_mfma_f32_16x16x32_bf16 v[136:139], a[240:243], a[0:3], 0\n\t"
    "s_add_u32 m0, s52, 12288\n\t"
    "v_exp_f32 v202, v202\n\t"
    "v_mfma_f32_16x16x32_bf16 v[136:139], a[244:247], a[4:7], v[136:139]\n\t"
    "v_exp_f32 v203, v203\n\t"
    "v_fma_f32 v204, v204, s55, v7\n\t"
    "v_fma_f32 v205, v205, s55, v7\n\t"
    "v_mfma_f32_16x16x32_bf16 v[140:143], a[240:243], a[24:27], 0\n\t"
    "v_exp_f32 v204, v204\n\t"
    "v_mfma_f32_16x16x32_bf16 v[140:143], a[244:247], a[28:31], v[140:143]\n\t"
    "v_cvt_pk_bf16_f32 v200, v200, v201\n\t"
    "v_cvt_pk_bf16_f32 v202, v202, v203\n\t"
    "v_mfma_f32_16x16x32_bf16 v[144:147], a[240:243], a[48:51], 0\n\t"
    "v_exp_f32 v205, v205\n\t"
    "v_mfma_f32_16x16x32_bf16 v[144:147], a[244:247], a[52:55], v[144:147]\n\t"
    "v_fma_f32 v206, v206, s55, v7\n\t"
    "v_fma_f32 v207, v207, s55, v7\n\t"
    "v_mfma_f32_16x16x32_bf16 v[148:151], a[240:243], a[72:75], 0\n\t"
    "v_accvgpr_write_b32 a208, v200\n\t"
    "v_accvgpr_write_b32 a209, v202\n\t"
    "v_mfma_f32_16x16x32_bf16 v[148:151], a[244:247], a[76:79], v[148:151]\n\t"
    "v_exp_f32 v206, v206\n\t"
    "v_exp_f32 v207, v207\n\t"
    "v_mfma_f32_16x16x32_bf16 v[152:155], a[240:243], a[96:99], 0\n\t"
    "v_cvt_pk_bf16_f32 v204, v204, v205\n\t"
    "v_cvt_pk_bf16_f32 v206, v206, v207\n\t"
    "v_mfma_f32_16x16x32_bf16 v[152:155], a[244:247], a[100:103], v[152:155]\n\t"
    "buffer_load_dwordx4 v1, s[40:43], s53 offen nt lds\n\t"
    "v_accvgpr_write_b32 a212, v204\n\t"
    "v_accvgpr_write_b32 a213, v206\n\t"
    "v_mfma_f32_16x16x32_bf16 v[156:159], a[240:243], a[120:123], 0\n\t"
    "s_addk_i32 m0, 896\n\t"
    "v_fma_f32 v16, v16, s55, v2\n\t"
    "v_fma_f32 v17, v17, s55, v2\n\t"
    "v_mfma_f32_16x16x32_bf16 v[156:159], a[244:247], a[124:127], v[156:159]\n\t"
    "s_waitcnt lgkmcnt(0)\n\t"
    "ds_read_b128 a[240:243], v0 offset:14336\n\t"
    "ds_read_b128 a[244:247], v0 offset:14848\n\t"
    "v_exp_f32 v16, v16\n\t"
    "v_exp_f32 v17, v17\n\t"
    "v_mfma_f32_16x16x32_bf16 v[136:139], a[248:251], a[8:11], v[136:139]\n\t"
    "v_fma_f32 v18, v18, s55, v2\n\t"
    "v_fma_f32 v19, v19, s55, v2\n\t"
    "v_mfma_f32_16x16x32_bf16 v[136:139], a[252:255], a[12:15], v[136:139]\n\t"
    "v_exp_f32 v18, v18\n\t"
    "v_mfma_f32_16x16x32_bf16 v[140:143], a[248:251], a[32:35], v[140:143]\n\t"
    "v_exp_f32 v19, v19\n\t"
    "v_mfma_f32_16x16x32_bf16 v[140:143], a[252:255], a[36:39], v[140:143]\n\t"
    "v_fma_f32 v20, v20, s55, v3\n\t"
    "v_fma_f32 v21, v21, s55, v3\n\t"
    "v_mfma_f32_16x16x32_bf16 v[144:147], a[248:251], a[56:59], v[144:147]\n\t"
    "v_exp_f32 v20, v20\n\t"
    "v_mfma_f32_16x16x32_bf16 v[144:147], a[252:255], a[60:63], v[144:147]\n\t"
    "v_cvt_pk_bf16_f32 v16, v16, v17\n\t"
    "v_cvt_pk_bf16_f32 v18, v18, v19\n\t"
    "v_exp_f32 v21, v21\n\t"
    "v_mfma_f32_16x16x32_bf16 v[148:151], a[248:251], a[80:83], v[148:151]\n\t"
    "v_fma_f32 v22, v22, s55, v3\n\t"
    "v_fma_f32 v23, v23, s55, v3\n\t"
    "v_mfma_f32_16x16x32_bf16 v[148:151], a[252:255], a[84:87], v[148:151]\n\t"
    "buffer_load_dwordx4 v1, s[40:43], s53 offen offset:128 nt lds\n\t"
    "v_accvgpr_write_b32 a194, v16\n\t"
    "v_accvgpr_write_b32 a195, v18\n\t"
    "v_mfma_f32_16x16x32_bf16 v[152:155], a[248:251], a[104:107], v[152:155]\n\t"
    "s_addk_i32 m0, 896\n\t"
    "v_exp_f32 v22, v22\n\t"
    "v_mfma_f32_16x16x32_bf16 v[152:155], a[252:255], a[108:111], v[152:155]\n\t"
    "v_exp_f32 v23, v23\n\t"
    "v_fma_f32 v24, v24, s55, v4\n\t"
    "v_fma_f32 v25, v25, s55, v4\n\t"
    "v_mfma_f32_16x16x32_bf16 v[156:159], a[248:251], a[128:131], v[156:159]\n\t"
    "v_exp_f32 v24, v24\n\t"
    "v_mfma_f32_16x16x32_bf16 v[156:159], a[252:255], a[132:135], v[156:159]\n\t"
    "s_waitcnt lgkmcnt(0)\n\t"
    "ds_read_b128 a[248:251], v0 offset:18432\n\t"
    "ds_read_b128 a[252:255], v0 offset:18944\n\t"
    "v_cvt_pk_bf16_f32 v20, v20, v21\n\t"
    "v_cvt_pk_bf16_f32 v22, v22, v23\n\t"
    "v_mfma_f32_16x16x32_bf16 v[136:139], a[240:243], a[16:19], v[136:139]\n\t"
    "v_exp_f32 v25, v25\n\t"
    "v_mfma_f32_16x16x32_bf16 v[136:139], a[244:247], a[20:23], v[136:139]\n\t"
    "v_fma_f32 v26, v26, s55, v4\n\t"
    "v_fma_f32 v27, v27, s55, v4\n\t"
    "v_accvgpr_write_b32 a198, v20\n\t"
    "v_accvgpr_write_b32 a199, v22\n\t"
    "v_mfma_f32_16x16x32_bf16 v[140:143], a[240:243], a[40:43], v[140:143]\n\t"
    "v_exp_f32 v26, v26\n\t"
    "v_mfma_f32_16x16x32_bf16 v[140:143], a[244:247], a[44:47], v[140:143]\n\t"
    "v_exp_f32 v27, v27\n\t"
    "v_mfma_f32_16x16x32_bf16 v[8:11], v[40:43], a[192:195], v[8:11]\n\t"
    "v_fma_f32 v28, v28, s55, v5\n\t"
    "v_fma_f32 v29, v29, s55, v5\n\t"
    "v_mfma_f32_16x16x32_bf16 v[8:11], v[44:47], a[196:199], v[8:11]\n\t"
    "v_exp_f32 v28, v28\n\t"
    "v_cvt_pk_bf16_f32 v24, v24, v25\n\t"
    "v_cvt_pk_bf16_f32 v26, v26, v27\n\t"
    "v_mfma_f32_16x16x32_bf16 v[144:147], a[240:243], a[64:67], v[144:147]\n\t"
    "v_exp_f32 v29, v29\n\t"
    "v_mfma_f32_16x16x32_bf16 v[144:147], a[244:247], a[68:71], v[144:147]\n\t"
    "v_fma_f32 v30, v30, s55, v5\n\t"
    "v_fma_f32 v31, v31, s55, v5\n\t"
    "v_mfma_f32_16x16x32_bf16 v[148:151], a[240:243], a[88:91], v[148:151]\n\t"
    "v_accvgpr_write_b32 a202, v24\n\t"
    "v_accvgpr_write_b32 a203, v26\n\t"
    "v_mfma_f32_16x16x32_bf16 v[148:151], a[244:247], a[92:95], v[148:151]\n\t"
    "buffer_load_dwordx4 v1, s[40:43], s53 offen offset:256 nt lds\n\t"
    "v_exp_f32 v30, v30\n\t"
    "v_exp_f32 v31, v31\n\t"
    "v_mfma_f32_16x16x32_bf16 v[152:155], a[240:243], a[112:115], v[152:155]\n\t"
    "s_mov_b32 s53, 0\n\t"
    "v_fma_f32 v32, v32, s55, v6\n\t"
    "v_fma_f32 v33, v33, s55, v6\n\t"
    "v_mfma_f32_16x16x32_bf16 v[152:155], a[244:247], a[116:119], v[152:155]\n\t"
    "s_add_u32 m0, s52, 61440\n\t"
    "v_exp_f32 v32, v32\n\t"
    "v_mfma_f32_16x16x32_bf16 v[156:159], a[240:243], a[136:139], v[156:159]\n\t"
    "v_cvt_pk_bf16_f32 v28, v28, v29\n\t"
    "v_cvt_pk_bf16_f32 v30, v30, v31\n\t"
    "v_mfma_f32_16x16x32_bf16 v[156:159], a[244:247], a[140:143], v[156:159]\n\t"
    "s_waitcnt lgkmcnt(0)\n\t"
    "ds_read_b128 a[240:243], v0 offset:19456\n\t"
    "ds_read_b128 a[244:247], v0 offset:19968\n\t"
    "v_exp_f32 v33, v33\n\t"
    "v_mfma_f32_16x16x32_bf16 v[16:19], a[248:251], a[0:3], 0\n\t"
    "v_fma_f32 v34, v34, s55, v6\n\t"
    "v_fma_f32 v35, v35, s55, v6\n\t"
    "v_mfma_f32_16x16x32_bf16 v[16:19], a[252:255], a[4:7], v[16:19]\n\t"
    "v_accvgpr_write_b32 a206, v28\n\t"
    "v_accvgpr_write_b32 a207, v30\n\t"
    "v_exp_f32 v34, v34\n\t"
    "v_mfma_f32_16x16x32_bf16 v[10:13], v[40:43], a[200:203], v[10:13]\n\t"
    "v_exp_f32 v35, v35\n\t"
    "v_mfma_f32_16x16x32_bf16 v[10:13], v[44:47], a[204:207], v[10:13]\n\t"
    "v_fma_f32 v36, v36, s55, v7\n\t"
    "v_fma_f32 v37, v37, s55, v7\n\t"
    "v_mfma_f32_16x16x32_bf16 v[20:23], a[248:251], a[24:27], 0\n\t"
    "v_exp_f32 v36, v36\n\t"
    "v_mfma_f32_16x16x32_bf16 v[20:23], a[252:255], a[28:31], v[20:23]\n\t"
    "v_cvt_pk_bf16_f32 v32, v32, v33\n\t"
    "v_cvt_pk_bf16_f32 v34, v34, v35\n\t"
    "v_mfma_f32_16x16x32_bf16 v[24:27], a[248:251], a[48:51], 0\n\t"
    "v_exp_f32 v37, v37\n\t"
    "v_mfma_f32_16x16x32_bf16 v[24:27], a[252:255], a[52:55], v[24:27]\n\t"
    "buffer_store_dwordx4 v[236:239], v63, s[48:51], s54 offen offset:3328 sc1\n\t"
    "v_fma_f32 v38, v38, s55, v7\n\t"
    "v_fma_f32 v39, v39, s55, v7\n\t"
    "v_accvgpr_write_b32 a210, v32\n\t"
    "v_accvgpr_write_b32 a211, v34\n\t"
    "v_mfma_f32_16x16x32_bf16 v[28:31], a[248:251], a[72:75], 0\n\t"
    "s_mov_b32 s54, 24576\n\t"
    "v_exp_f32 v38, v38\n\t"
    "v_mfma_f32_16x16x32_bf16 v[28:31], a[252:255], a[76:79], v[28:31]\n\t"
    "v_exp_f32 v39, v39\n\t"
    "v_mfma_f32_16x16x32_bf16 v[32:35], a[248:251], a[96:99], 0\n\t"
    "v_cvt_pk_bf16_f32 v36, v36, v37\n\t"
    "v_cvt_pk_bf16_f32 v38, v38, v39\n\t"
    "v_mfma_f32_16x16x32_bf16 v[32:35], a[252:255], a[100:103], v[32:35]\n\t"
    "v_accvgpr_write_b32 a214, v36\n\t"
    "v_accvgpr_write_b32 a215, v38\n\t"
    "v_mfma_f32_16x16x32_bf16 v[36:39], a[248:251], a[120:123], 0\n\t"
    "v_fma_f32 v136, v136, s55, v2\n\t"
    "v_fma_f32 v137, v137, s55, v2\n\t"
    "v_mfma_f32_16x16x32_bf16 v[36:39], a[252:255], a[124:127], v[36:39]\n\t"
    "s_waitcnt lgkmcnt(0)\n\t"
    "ds_read_b128 a[248:251], v0 offset:20480\n\t"
    "ds_read_b128 a[252:255], v0 offset:20992\n\t"
    "v_exp_f32 v136, v136\n\t"
    "v_mfma_f32_16x16x32_bf16 v[12:15], v[40:43], a[208:211], v[12:15]\n\t"
    "v_exp_f32 v137, v137\n\t"
    "v_mfma_f32_16x16x32_bf16 v[12:15], v[44:47], a[212:215], v[12:15]\n\t"
    "v_fma_f32 v138, v138, s55, v2\n\t"
    "v_fma_f32 v139, v139, s55, v2\n\t"
    "v_exp_f32 v138, v138\n\t"
    "v_mfma_f32_16x16x32_bf16 v[16:19], a[240:243], a[8:11], v[16:19]\n\t"
    "v_exp_f32 v139, v139\n\t"
    "v_mfma_f32_16x16x32_bf16 v[16:19], a[244:247], a[12:15], v[16:19]\n\t"
    "v_fma_f32 v140, v140, s55, v3\n\t"
    "v_fma_f32 v141, v141, s55, v3\n\t"
    "v_mfma_f32_16x16x32_bf16 v[20:23], a[240:243], a[32:35], v[20:23]\n\t"
    "v_exp_f32 v140, v140\n\t"
    "v_mfma_f32_16x16x32_bf16 v[20:23], a[244:247], a[36:39], v[20:23]\n\t"
    "v_cvt_pk_bf16_f32 v136, v136, v137\n\t"
    "v_cvt_pk_bf16_f32 v137, v138, v139\n\t"
    "v_mfma_f32_16x16x32_bf16 v[24:27], a[240:243], a[56:59], v[24:27]\n\t"
    "v_exp_f32 v141, v141\n\t"
    "v_mfma_f32_16x16x32_bf16 v[24:27], a[244:247], a[60:63], v[24:27]\n\t"
    "buffer_load_dwordx4 v1, s[44:47], s53 offen nt lds\n\t"
    "v_fma_f32 v142, v142, s55, v3\n\t"
    "v_fma_f32 v143, v143, s55, v3\n\t"
    "v_mfma_f32_16x16x32_bf16 v[28:31], a[240:243], a[80:83], v[28:31]\n\t"
    "s_addk_i32 s53, 3072\n\t"
    "v_exp_f32 v142, v142\n\t"
    "v_mfma_f32_16x16x32_bf16 v[28:31], a[244:247], a[84:87], v[28:31]\n\t"
    "s_addk_i32 m0, 1024\n\t"
    "v_exp_f32 v143, v143\n\t"
    "v_mfma_f32_16x16x32_bf16 v[32:35], a[240:243], a[104:107], v[32:35]\n\t"
    "v_fma_f32 v144, v144, s55, v4\n\t"
    "v_fma_f32 v145, v145, s55, v4\n\t"
    "v_mfma_f32_16x16x32_bf16 v[32:35], a[244:247], a[108:111], v[32:35]\n\t"
    "v_exp_f32 v144, v144\n\t"
    "v_cvt_pk_bf16_f32 v140, v140, v141\n\t"
    "v_cvt_pk_bf16_f32 v141, v142, v143\n\t"
    "v_mfma_f32_16x16x32_bf16 v[36:39], a[240:243], a[128:131], v[36:39]\n\t"
    "v_exp_f32 v145, v145\n\t"
    "v_mfma_f32_16x16x32_bf16 v[36:39], a[244:247], a[132:135], v[36:39]\n\t"
    "s_waitcnt lgkmcnt(0)\n\t"
    "ds_read_b128 a[240:243], v0 offset:36864\n\t"
    "ds_read_b128 a[244:247], v0 offset:37376\n\t"
    "v_fma_f32 v146, v146, s55, v4\n\t"
    "v_fma_f32 v147, v147, s55, v4\n\t"
    "v_mfma_f32_16x16x32_bf16 v[16:19], a[248:251], a[16:19], v[16:19]\n\t"
    "v_exp_f32 v146, v146\n\t"
    "v_mfma_f32_16x16x32_bf16 v[16:19], a[252:255], a[20:23], v[16:19]\n\t"
    "v_exp_f32 v147, v147\n\t"
    "v_mfma_f32_16x16x32_bf16 v[20:23], a[248:251], a[40:43], v[20:23]\n\t"
    "v_fma_f32 v148, v148, s55, v5\n\t"
    "v_fma_f32 v149, v149, s55, v5\n\t"
    "v_mfma_f32_16x16x32_bf16 v[20:23], a[252:255], a[44:47], v[20:23]\n\t"
    "buffer_load_dwordx4 v1, s[44:47], s53 offen nt lds\n\t"
    "v_exp_f32 v148, v148\n\t"
    "v_mfma_f32_16x16x32_bf16 v[24:27], a[248:251], a[64:67], v[24:27]\n\t"
    "s_addk_i32 s53, 3072\n\t"
    "v_cvt_pk_bf16_f32 v144, v144, v145\n\t"
    "v_cvt_pk_bf16_f32 v145, v146, v147\n\t"
    "v_mfma_f32_16x16x32_bf16 v[24:27], a[252:255], a[68:71], v[24:27]\n\t"
    "s_addk_i32 m0, 1024\n\t"
    "v_exp_f32 v149, v149\n\t"
    "v_fma_f32 v150, v150, s55, v5\n\t"
    "v_fma_f32 v151, v151, s55, v5\n\t"
    "v_mfma_f32_16x16x32_bf16 v[28:31], a[248:251], a[88:91], v[28:31]\n\t"
    "v_exp_f32 v150, v150\n\t"
    "v_mfma_f32_16x16x32_bf16 v[28:31], a[252:255], a[92:95], v[28:31]\n\t"
    "v_exp_f32 v151, v151\n\t"
    "v_mfma_f32_16x16x32_bf16 v[32:35], a[248:251], a[112:115], v[32:35]\n\t"
    "v_fma_f32 v152, v152, s55, v6\n\t"
    "v_fma_f32 v153, v153, s55, v6\n\t"
    "v_mfma_f32_16x16x32_bf16 v[32:35], a[252:255], a[116:119], v[32:35]\n\t"
    "v_exp_f32 v152, v152\n\t"
    "v_mfma_f32_16x16x32_bf16 v[36:39], a[248:251], a[136:139], v[36:39]\n\t"
    "v_cvt_pk_bf16_f32 v148, v148, v149\n\t"
    "v_cvt_pk_bf16_f32 v149, v150, v151\n\t"
    "v_mfma_f32_16x16x32_bf16 v[36:39], a[252:255], a[140:143], v[36:39]\n\t"
    "s_waitcnt lgkmcnt(0)\n\t"
    "ds_read_b128 a[248:251], v0 offset:37888\n\t"
    "ds_read_b128 a[252:255], v0 offset:38400\n\t"
    "v_exp_f32 v153, v153\n\t"
    "v_mfma_f32_16x16x32_bf16 v[184:187], a[240:243], a[0:3], 0\n\t"
    "v_fma_f32 v154, v154, s55, v6\n\t"
    "v_fma_f32 v155, v155, s55, v6\n\t"
    "v_mfma_f32_16x16x32_bf16 v[184:187], a[244:247], a[4:7], v[184:187]\n\t"
    "v_exp_f32 v154, v154\n\t"
    "v_exp_f32 v155, v155\n\t"
    "v_mfma_f32_16x16x32_bf16 v[188:191], a[240:243], a[24:27], 0\n\t"
    "v_fma_f32 v156, v156, s55, v7\n\t"
    "v_fma_f32 v157, v157, s55, v7\n\t"
    "v_mfma_f32_16x16x32_bf16 v[188:191], a[244:247], a[28:31], v[188:191]\n\t"
    "buffer_load_dwordx4 v1, s[44:47], s53 offen nt lds\n\t"
    "v_exp_f32 v156, v156\n\t"
    "v_mfma_f32_16x16x32_bf16 v[192:195], a[240:243], a[48:51], 0\n\t"
    "s_mov_b32 s53, 9216\n\t"
    "v_cvt_pk_bf16_f32 v152, v152, v153\n\t"
    "v_cvt_pk_bf16_f32 v153, v154, v155\n\t"
    "v_mfma_f32_16x16x32_bf16 v[192:195], a[244:247], a[52:55], v[192:195]\n\t"
    "s_add_u32 m0, s52, 110592\n\t"
    "v_exp_f32 v157, v157\n\t"
    "v_mfma_f32_16x16x32_bf16 v[196:199], a[240:243], a[72:75], 0\n\t"
    "v_fma_f32 v158, v158, s55, v7\n\t"
    "v_fma_f32 v159, v159, s55, v7\n\t"
    "v_mfma_f32_16x16x32_bf16 v[196:199], a[244:247], a[76:79], v[196:199]\n\t"
    "v_exp_f32 v158, v158\n\t"
    "v_exp_f32 v159, v159\n\t"
    "v_mfma_f32_16x16x32_bf16 v[200:203], a[240:243], a[96:99], 0\n\t"
    "v_cvt_pk_bf16_f32 v156, v156, v157\n\t"
    "v_cvt_pk_bf16_f32 v157, v158, v159\n\t"
    "v_mfma_f32_16x16x32_bf16 v[200:203], a[244:247], a[100:103], v[200:203]\n\t"
    "v_fma_f32 v16, v16, s55, v2\n\t"
    "v_fma_f32 v17, v17, s55, v2\n\t"
    "v_mfma_f32_16x16x32_bf16 v[204:207], a[240:243], a[120:123], 0\n\t"
    "v_exp_f32 v16, v16\n\t"
    "v_mfma_f32_16x16x32_bf16 v[204:207], a[244:247], a[124:127], v[204:207]\n\t"
    "s_waitcnt lgkmcnt(0)\n\t"
    "ds_read_b128 a[240:243], v0 offset:38912\n\t"
    "ds_read_b128 a[244:247], v0 offset:39424\n\t"
    "v_exp_f32 v17, v17\n\t"
    "v_mfma_f32_16x16x32_bf16 v[184:187], a[248:251], a[8:11], v[184:187]\n\t"
    "v_fma_f32 v18, v18, s55, v2\n\t"
    "v_fma_f32 v19, v19, s55, v2\n\t"
    "v_mfma_f32_16x16x32_bf16 v[184:187], a[252:255], a[12:15], v[184:187]\n\t"
    "buffer_load_dwordx4 v1, s[44:47], s53 offen nt lds\n\t"
    "v_exp_f32 v18, v18\n\t"
    "v_mfma_f32_16x16x32_bf16 v[188:191], a[248:251], a[32:35], v[188:191]\n\t"
    "s_addk_i32 s53, 3072\n\t"
    "v_exp_f32 v19, v19\n\t"
    "v_mfma_f32_16x16x32_bf16 v[188:191], a[252:255], a[36:39], v[188:191]\n\t"
    "s_addk_i32 m0, 1024\n\t"
    "v_fma_f32 v20, v20, s55, v3\n\t"
    "v_fma_f32 v21, v21, s55, v3\n\t"
    "v_exp_f32 v20, v20\n\t"
    "v_mfma_f32_16x16x32_bf16 v[192:195], a[248:251], a[56:59], v[192:195]\n\t"
    "v_cvt_pk_bf16_f32 v138, v16, v17\n\t"
    "v_cvt_pk_bf16_f32 v139, v18, v19\n\t"
    "v_mfma_f32_16x16x32_bf16 v[192:195], a[252:255], a[60:63], v[192:195]\n\t"
    "v_exp_f32 v21, v21\n\t"
    "v_mfma_f32_16x16x32_bf16 v[196:199], a[248:251], a[80:83], v[196:199]\n\t"
    "v_fma_f32 v22, v22, s55, v3\n\t"
    "v_fma_f32 v23, v23, s55, v3\n\t"
    "v_mfma_f32_16x16x32_bf16 v[196:199], a[252:255], a[84:87], v[196:199]\n\t"
    "v_exp_f32 v22, v22\n\t"
    "v_mfma_f32_16x16x32_bf16 v[200:203], a[248:251], a[104:107], v[200:203]\n\t"
    "v_exp_f32 v23, v23\n\t"
    "v_mfma_f32_16x16x32_bf16 v[200:203], a[252:255], a[108:111], v[200:203]\n\t"
    "v_fma_f32 v24, v24, s55, v4\n\t"
    "v_fma_f32 v25, v25, s55, v4\n\t"
    "v_exp_f32 v24, v24\n\t"
    "v_mfma_f32_16x16x32_bf16 v[204:207], a[248:251], a[128:131], v[204:207]\n\t"
    "v_cvt_pk_bf16_f32 v142, v20, v21\n\t"
    "v_cvt_pk_bf16_f32 v143, v22, v23\n\t"
    "v_mfma_f32_16x16x32_bf16 v[204:207], a[252:255], a[132:135], v[204:207]\n\t"
    "s_waitcnt lgkmcnt(0)\n\t"
    "ds_read_b128 a[248:251], v0 offset:43008\n\t"
    "ds_read_b128 a[252:255], v0 offset:43520\n\t"
    "v_exp_f32 v25, v25\n\t"
    "v_mfma_f32_16x16x32_bf16 v[8:11], v[40:43], v[136:139], v[8:11]\n\t"
    "v_fma_f32 v26, v26, s55, v4\n\t"
    "v_fma_f32 v27, v27, s55, v4\n\t"
    "v_mfma_f32_16x16x32_bf16 v[8:11], v[44:47], v[140:143], v[8:11]\n\t"
    "v_exp_f32 v26, v26\n\t"
    "v_mfma_f32_16x16x32_bf16 v[184:187], a[240:243], a[16:19], v[184:187]\n\t"
    "v_exp_f32 v27, v27\n\t"
    "v_mfma_f32_16x16x32_bf16 v[184:187], a[244:247], a[20:23], v[184:187]\n\t"
    "buffer_load_dwordx4 v1, s[44:47], s53 offen nt lds\n\t"
    "v_fma_f32 v28, v28, s55, v5\n\t"
    "v_fma_f32 v29, v29, s55, v5\n\t"
    "v_exp_f32 v28, v28\n\t"
    "v_mfma_f32_16x16x32_bf16 v[188:191], a[240:243], a[40:43], v[188:191]\n\t"
    "s_addk_i32 s53, 3072\n\t"
    "v_cvt_pk_bf16_f32 v146, v24, v25\n\t"
    "v_cvt_pk_bf16_f32 v147, v26, v27\n\t"
    "v_mfma_f32_16x16x32_bf16 v[188:191], a[244:247], a[44:47], v[188:191]\n\t"
    "s_addk_i32 m0, 1024\n\t"
    "v_exp_f32 v29, v29\n\t"
    "v_mfma_f32_16x16x32_bf16 v[192:195], a[240:243], a[64:67], v[192:195]\n\t"
    "v_fma_f32 v30, v30, s55, v5\n\t"
    "v_fma_f32 v31, v31, s55, v5\n\t"
    "v_mfma_f32_16x16x32_bf16 v[192:195], a[244:247], a[68:71], v[192:195]\n\t"
    "v_exp_f32 v30, v30\n\t"
    "v_mfma_f32_16x16x32_bf16 v[196:199], a[240:243], a[88:91], v[196:199]\n\t"
    "v_exp_f32 v31, v31\n\t"
    "v_mfma_f32_16x16x32_bf16 v[196:199], a[244:247], a[92:95], v[196:199]\n\t"
    "v_fma_f32 v32, v32, s55, v6\n\t"
    "v_fma_f32 v33, v33, s55, v6\n\t"
    "v_mfma_f32_16x16x32_bf16 v[200:203], a[240:243], a[112:115], v[200:203]\n\t"
    "v_exp_f32 v32, v32\n\t"
    "v_mfma_f32_16x16x32_bf16 v[200:203], a[244:247], a[116:119], v[200:203]\n\t"
    "v_cvt_pk_bf16_f32 v150, v28, v29\n\t"
    "v_cvt_pk_bf16_f32 v151, v30, v31\n\t"
    "v_exp_f32 v33, v33\n\t"
    "v_mfma_f32_16x16x32_bf16 v[10:13], v[40:43], v[144:147], v[10:13]\n\t"
    "v_fma_f32 v34, v34, s55, v6\n\t"
    "v_fma_f32 v35, v35, s55, v6\n\t"
    "v_mfma_f32_16x16x32_bf16 v[10:13], v[44:47], v[148:151], v[10:13]\n\t"
    "v_exp_f32 v34, v34\n\t"
    "v_mfma_f32_16x16x32_bf16 v[204:207], a[240:243], a[136:139], v[204:207]\n\t"
    "v_exp_f32 v35, v35\n\t"
    "v_mfma_f32_16x16x32_bf16 v[204:207], a[244:247], a[140:143], v[204:207]\n\t"
    "s_waitcnt lgkmcnt(0)\n\t"
    "ds_read_b128 a[240:243], v0 offset:44032\n\t"
    "ds_read_b128 a[244:247], v0 offset:44544\n\t"
    "v_fma_f32 v36, v36, s55, v7\n\t"
    "v_fma_f32 v37, v37, s55, v7\n\t"
    "v_mfma_f32_16x16x32_bf16 v[16:19], a[248:251], a[0:3], 0\n\t"
    "v_exp_f32 v36, v36\n\t"
    "v_mfma_f32_16x16x32_bf16 v[16:19], a[252:255], a[4:7], v[16:19]\n\t"
    "buffer_store_dwordx4 v[240:243], v63, s[48:51], s54 offen offset:256 sc1\n\t"
    "v_cvt_pk_bf16_f32 v154, v32, v33\n\t"
    "v_cvt_pk_bf16_f32 v155, v34, v35\n\t"
    "v_mfma_f32_16x16x32_bf16 v[20:23], a[248:251], a[24:27], 0\n\t"
    "v_exp_f32 v37, v37\n\t"
    "v_mfma_f32_16x16x32_bf16 v[20:23], a[252:255], a[28:31], v[20:23]\n\t"
    "v_fma_f32 v38, v38, s55, v7\n\t"
    "v_fma_f32 v39, v39, s55, v7\n\t"
    "v_exp_f32 v38, v38\n\t"
    "v_mfma_f32_16x16x32_bf16 v[24:27], a[248:251], a[48:51], 0\n\t"
    "v_exp_f32 v39, v39\n\t"
    "v_mfma_f32_16x16x32_bf16 v[24:27], a[252:255], a[52:55], v[24:27]\n\t"
    "v_cvt_pk_bf16_f32 v158, v36, v37\n\t"
    "v_cvt_pk_bf16_f32 v159, v38, v39\n\t"
    "v_mfma_f32_16x16x32_bf16 v[28:31], a[248:251], a[72:75], 0\n\t"
    "v_fma_f32 v184, v184, s55, v2\n\t"
    "v_fma_f32 v185, v185, s55, v2\n\t"
    "v_mfma_f32_16x16x32_bf16 v[28:31], a[252:255], a[76:79], v[28:31]\n\t"
    "v_exp_f32 v184, v184\n\t"
    "v_mfma_f32_16x16x32_bf16 v[12:15], v[40:43], v[152:155], v[12:15]\n\t"
    "v_exp_f32 v185, v185\n\t"
    "v_mfma_f32_16x16x32_bf16 v[12:15], v[44:47], v[156:159], v[12:15]\n\t"
    "v_fma_f32 v186, v186, s55, v2\n\t"
    "v_fma_f32 v187, v187, s55, v2\n\t"
    "v_exp_f32 v186, v186\n\t"
    "v_mfma_f32_16x16x32_bf16 v[32:35], a[248:251], a[96:99], 0\n\t"
    "v_exp_f32 v187, v187\n\t"
    "v_mfma_f32_16x16x32_bf16 v[32:35], a[252:255], a[100:103], v[32:35]\n\t"
    "buffer_load_dwordx4 v1, s[44:47], s53 offen nt lds\n\t"
    "v_fma_f32 v188, v188, s55, v3\n\t"
    "v_fma_f32 v189, v189, s55, v3\n\t"
    "v_mfma_f32_16x16x32_bf16 v[36:39], a[248:251], a[120:123], 0\n\t"
    "s_mov_b32 s53, s89\n\t"
    "v_exp_f32 v188, v188\n\t"
    "v_mfma_f32_16x16x32_bf16 v[36:39], a[252:255], a[124:127], v[36:39]\n\t"
    "s_waitcnt lgkmcnt(0)\n\t"
    "ds_read_b128 a[248:251], v0 offset:45056\n\t"
    "ds_read_b128 a[252:255], v0 offset:45568\n\t"
    "v_cvt_pk_bf16_f32 v184, v184, v185\n\t"
    "v_cvt_pk_bf16_f32 v186, v186, v187\n\t"
    "v_mfma_f32_16x16x32_bf16 v[16:19], a[240:243], a[8:11], v[16:19]\n\t"
    "s_add_u32 m0, s52, 24576\n\t"
    "v_exp_f32 v189, v189\n\t"
    "v_mfma_f32_16x16x32_bf16 v[16:19], a[244:247], a[12:15], v[16:19]\n\t"
    "v_fma_f32 v190, v190, s55, v3\n\t"
    "v_fma_f32 v191, v191, s55, v3\n\t"
    "v_mfma_f32_16x16x32_bf16 v[20:23], a[240:243], a[32:35], v[20:23]\n\t"
    "v_accvgpr_write_b32 a216, v184\n\t"
    "v_accvgpr_write_b32 a217, v186\n\t"
    "v_mfma_f32_16x16x32_bf16 v[20:23], a[244:247], a[36:39], v[20:23]\n\t"
    "v_exp_f32 v190, v190\n\t"
    "v_mfma_f32_16x16x32_bf16 v[24:27], a[240:243], a[56:59], v[24:27]\n\t"
    "v_exp_f32 v191, v191\n\t"
    "v_mfma_f32_16x16x32_bf16 v[24:27], a[244:247], a[60:63], v[24:27]\n\t"
    "v_fma_f32 v192, v192, s55, v4\n\t"
    "v_fma_f32 v193, v193, s55, v4\n\t"
    "v_exp_f32 v192, v192\n\t"
    "v_mfma_f32_16x16x32_bf16 v[28:31], a[240:243], a[80:83], v[28:31]\n\t"
    "v_cvt_pk_bf16_f32 v188, v188, v189\n\t"
    "v_cvt_pk_bf16_f32 v190, v190, v191\n\t"
    "v_mfma_f32_16x16x32_bf16 v[28:31], a[244:247], a[84:87], v[28:31]\n\t"
    "v_exp_f32 v193, v193\n\t"
    "v_mfma_f32_16x16x32_bf16 v[32:35], a[240:243], a[104:107], v[32:35]\n\t"
    "v_fma_f32 v194, v194, s55, v4\n\t"
    "v_fma_f32 v195, v195, s55, v4\n\t"
    "v_mfma_f32_16x16x32_bf16 v[32:35], a[244:247], a[108:111], v[32:35]\n\t"
    "buffer_load_dwordx4 v1, s[36:39], s53 offen nt lds\n\t"
    "v_accvgpr_write_b32 a220, v188\n\t"
    "v_accvgpr_write_b32 a221, v190\n\t"
    "v_mfma_f32_16x16x32_bf16 v[36:39], a[240:243], a[128:131], v[36:39]\n\t"
    "s_addk_i32 m0, 896\n\t"
    "v_exp_f32 v194, v194\n\t"
    "v_mfma_f32_16x16x32_bf16 v[36:39], a[244:247], a[132:135], v[36:39]\n\t"
    "s_waitcnt vmcnt(16) lgkmcnt(0)\n\t"
    "s_barrier\n\t"
    "ds_read_b128 a[240:243], v0 offset:24576\n\t"
    "ds_read_b128 a[244:247], v0 offset:25088\n\t"
    "v_exp_f32 v195, v195\n\t"
    "v_fma_f32 v196, v196, s55, v5\n\t"
    "v_fma_f32 v197, v197, s55, v5\n\t"
    "v_mfma_f32_16x16x32_bf16 v[16:19], a[248:251], a[16:19], v[16:19]\n\t"
    "v_exp_f32 v196, v196\n\t"
    "v_mfma_f32_16x16x32_bf16 v[16:19], a[252:255], a[20:23], v[16:19]\n\t"
    "v_cvt_pk_bf16_f32 v192, v192, v193\n\t"
    "v_cvt_pk_bf16_f32 v194, v194, v195\n\t"
    "v_mfma_f32_16x16x32_bf16 v[20:23], a[248:251], a[40:43], v[20:23]\n\t"
    "v_exp_f32 v197, v197\n\t"
    "v_mfma_f32_16x16x32_bf16 v[20:23], a[252:255], a[44:47], v[20:23]\n\t"
    "v_fma_f32 v198, v198, s55, v5\n\t"
    "v_fma_f32 v199, v199, s55, v5\n\t"
    "v_mfma_f32_16x16x32_bf16 v[24:27], a[248:251], a[64:67], v[24:27]\n\t"
    "v_accvgpr_write_b32 a224, v192\n\t"
    "v_accvgpr_write_b32 a225, v194\n\t"
    "v_mfma_f32_16x16x32_bf16 v[24:27], a[252:255], a[68:71], v[24:27]\n\t"
    "v_exp_f32 v198, v198\n\t"
    "v_mfma_f32_16x16x32_bf16 v[28:31], a[248:251], a[88:91], v[28:31]\n\t"
    "v_exp_f32 v199, v199\n\t"
    "v_mfma_f32_16x16x32_bf16 v[28:31], a[252:255], a[92:95], v[28:31]\n\t"
    "buffer_load_dwordx4 v1, s[36:39], s53 offen offset:128 nt lds\n\t"
    "v_fma_f32 v200, v200, s55, v6\n\t"
    "v_fma_f32 v201, v201, s55, v6\n\t"
    "v_exp_f32 v200, v200\n\t"
    "v_mfma_f32_16x16x32_bf16 v[32:35], a[248:251], a[112:115], v[32:35]\n\t"
    "s_addk_i32 m0, 896\n\t"
    "v_cvt_pk_bf16_f32 v196, v196, v197\n\t"
    "v_cvt_pk_bf16_f32 v198, v198, v199\n\t"
    "v_mfma_f32_16x16x32_bf16 v[32:35], a[252:255], a[116:119], v[32:35]\n\t"
    "v_exp_f32 v201, v201\n\t"
    "v_mfma_f32_16x16x32_bf16 v[36:39], a[248:251], a[136:139], v[36:39]\n\t"
    "v_fma_f32 v202, v202, s55, v6\n\t"
    "v_fma_f32 v203, v203, s55, v6\n\t"
    "v_mfma_f32_16x16x32_bf16 v[36:39], a[252:255], a[140:143], v[36:39]\n\t"
    "s_waitcnt lgkmcnt(0)\n\t"
    "ds_read_b128 a[248:251], v0 offset:25600\n\t"
    "ds_read_b128 a[252:255], v0 offset:26112\n\t"
    "v_accvgpr_write_b32 a228, v196\n\t"
    "v_accvgpr_write_b32 a229, v198\n\t"
    "v_mfma_f32_16x16x32_bf16 v[160:163], a[240:243], a[0:3], 0\n\t"
    "v_exp_f32 v202, v202\n\t"
    "v_mfma_f32_16x16x32_bf16 v[160:163], a[244:247], a[4:7], v[160:163]\n\t"
    "v_exp_f32 v203, v203\n\t"
    "v_fma_f32 v204, v204, s55, v7\n\t"
    "v_fma_f32 v205, v205, s55, v7\n\t"
    "v_mfma_f32_16x16x32_bf16 v[164:167], a[240:243], a[24:27], 0\n\t"
    "v_exp_f32 v204, v204\n\t"
    "v_mfma_f32_16x16x32_bf16 v[164:167], a[244:247], a[28:31], v[164:167]\n\t"
    "v_cvt_pk_bf16_f32 v200, v200, v201\n\t"
    "v_cvt_pk_bf16_f32 v202, v202, v203\n\t"
    "v_mfma_f32_16x16x32_bf16 v[168:171], a[240:243], a[48:51], 0\n\t"
    "v_exp_f32 v205, v205\n\t"
    "v_mfma_f32_16x16x32_bf16 v[168:171], a[244:247], a[52:55], v[168:171]\n\t"
    "v_fma_f32 v206, v206, s55, v7\n\t"
    "v_fma_f32 v207, v207, s55, v7\n\t"
    "v_mfma_f32_16x16x32_bf16 v[172:175], a[240:243], a[72:75], 0\n\t"
    "v_accvgpr_write_b32 a232, v200\n\t"
    "v_accvgpr_write_b32 a233, v202\n\t"
    "v_mfma_f32_16x16x32_bf16 v[172:175], a[244:247], a[76:79], v[172:175]\n\t"
    "buffer_load_dwordx4 v1, s[36:39], s53 offen offset:256 nt lds\n\t"
    "v_exp_f32 v206, v206\n\t"
    "v_exp_f32 v207, v207\n\t"
    "v_mfma_f32_16x16x32_bf16 v[176:179], a[240:243], a[96:99], 0\n\t"
    "s_mov_b32 s53, 18432\n\t"
    "v_cvt_pk_bf16_f32 v204, v204, v205\n\t"
    "v_cvt_pk_bf16_f32 v206, v206, v207\n\t"
    "v_mfma_f32_16x16x32_bf16 v[176:179], a[244:247], a[100:103], v[176:179]\n\t"
    "s_add_u32 m0, s52, 73728\n\t"
    "v_accvgpr_write_b32 a236, v204\n\t"
    "v_accvgpr_write_b32 a237, v206\n\t"
    "v_mfma_f32_16x16x32_bf16 v[180:183], a[240:243], a[120:123], 0\n\t"
    "v_fma_f32 v16, v16, s55, v2\n\t"
    "v_fma_f32 v17, v17, s55, v2\n\t"
    "v_mfma_f32_16x16x32_bf16 v[180:183], a[244:247], a[124:127], v[180:183]\n\t"
    "s_waitcnt lgkmcnt(0)\n\t"
    "ds_read_b128 a[240:243], v0 offset:26624\n\t"
    "ds_read_b128 a[244:247], v0 offset:27136\n\t"
    "v_exp_f32 v16, v16\n\t"
    "v_exp_f32 v17, v17\n\t"
    "v_mfma_f32_16x16x32_bf16 v[160:163], a[248:251], a[8:11], v[160:163]\n\t"
    "v_fma_f32 v18, v18, s55, v2\n\t"
    "v_fma_f32 v19, v19, s55, v2\n\t"
    "v_mfma_f32_16x16x32_bf16 v[160:163], a[252:255], a[12:15], v[160:163]\n\t"
    "v_exp_f32 v18, v18\n\t"
    "v_mfma_f32_16x16x32_bf16 v[164:167], a[248:251], a[32:35], v[164:167]\n\t"
    "v_exp_f32 v19, v19\n\t"
    "v_mfma_f32_16x16x32_bf16 v[164:167], a[252:255], a[36:39], v[164:167]\n\t"
    "v_fma_f32 v20, v20, s55, v3\n\t"
    "v_fma_f32 v21, v21, s55, v3\n\t"
    "v_mfma_f32_16x16x32_bf16 v[168:171], a[248:251], a[56:59], v[168:171]\n\t"
    "v_exp_f32 v20, v20\n\t"
    "v_mfma_f32_16x16x32_bf16 v[168:171], a[252:255], a[60:63], v[168:171]\n\t"
    "buffer_load_dwordx4 v1, s[44:47], s53 offen nt lds\n\t"
    "v_cvt_pk_bf16_f32 v16, v16, v17\n\t"
    "v_cvt_pk_bf16_f32 v18, v18, v19\n\t"
    "v_exp_f32 v21, v21\n\t"
    "v_mfma_f32_16x16x32_bf16 v[172:175], a[248:251], a[80:83], v[172:175]\n\t"
    "s_addk_i32 s53, 3072\n\t"
    "v_fma_f32 v22, v22, s55, v3\n\t"
    "v_fma_f32 v23, v23, s55, v3\n\t"
    "v_mfma_f32_16x16x32_bf16 v[172:175], a[252:255], a[84:87], v[172:175]\n\t"
    "s_addk_i32 m0, 1024\n\t"
    "v_accvgpr_write_b32 a218, v16\n\t"
    "v_accvgpr_write_b32 a219, v18\n\t"
    "v_mfma_f32_16x16x32_bf16 v[176:179], a[248:251], a[104:107], v[176:179]\n\t"
    "v_exp_f32 v22, v22\n\t"
    "v_mfma_f32_16x16x32_bf16 v[176:179], a[252:255], a[108:111], v[176:179]\n\t"
    "v_exp_f32 v23, v23\n\t"
    "v_fma_f32 v24, v24, s55, v4\n\t"
    "v_fma_f32 v25, v25, s55, v4\n\t"
    "v_mfma_f32_16x16x32_bf16 v[180:183], a[248:251], a[128:131], v[180:183]\n\t"
    "v_exp_f32 v24, v24\n\t"
    "v_mfma_f32_16x16x32_bf16 v[180:183], a[252:255], a[132:135], v[180:183]\n\t"
    "s_waitcnt lgkmcnt(0)\n\t"
    "ds_read_b128 a[248:251], v0 offset:30720\n\t"
    "ds_read_b128 a[252:255], v0 offset:31232\n\t"
    "v_cvt_pk_bf16_f32 v20, v20, v21\n\t"
    "v_cvt_pk_bf16_f32 v22, v22, v23\n\t"
    "v_mfma_f32_16x16x32_bf16 v[160:163], a[240:243], a[16:19], v[160:163]\n\t"
    "v_exp_f32 v25, v25\n\t"
    "v_mfma_f32_16x16x32_bf16 v[160:163], a[244:247], a[20:23], v[160:163]\n\t"
    "v_fma_f32 v26, v26, s55, v4\n\t"
    "v_fma_f32 v27, v27, s55, v4\n\t"
    "v_accvgpr_write_b32 a222, v20\n\t"
    "v_accvgpr_write_b32 a223, v22\n\t"
    "v_mfma_f32_16x16x32_bf16 v[164:167], a[240:243], a[40:43], v[164:167]\n\t"
    "v_exp_f32 v26, v26\n\t"
    "v_mfma_f32_16x16x32_bf16 v[164:167], a[244:247], a[44:47], v[164:167]\n\t"
    "v_exp_f32 v27, v27\n\t"
    "v_mfma_f32_16x16x32_bf16 v[8:11], v[40:43], a[216:219], v[8:11]\n\t"
    "v_fma_f32 v28, v28, s55, v5\n\t"
    "v_fma_f32 v29, v29, s55, v5\n\t"
    "v_mfma_f32_16x16x32_bf16 v[8:11], v[44:47], a[220:223], v[8:11]\n\t"
    "v_exp_f32 v28, v28\n\t"
    "v_cvt_pk_bf16_f32 v24, v24, v25\n\t"
    "v_cvt_pk_bf16_f32 v26, v26, v27\n\t"
    "v_mfma_f32_16x16x32_bf16 v[168:171], a[240:243], a[64:67], v[168:171]\n\t"
    "v_exp_f32 v29, v29\n\t"
    "v_mfma_f32_16x16x32_bf16 v[168:171], a[244:247], a[68:71], v[168:171]\n\t"
    "buffer_store_dwordx4 v[244:247], v63, s[48:51], s54 offen offset:3328 sc1\n\t"
    "v_fma_f32 v30, v30, s55, v5\n\t"
    "v_fma_f32 v31, v31, s55, v5\n\t"
    "v_mfma_f32_16x16x32_bf16 v[172:175], a[240:243], a[88:91], v[172:175]\n\t"
    "v_accvgpr_write_b32 a226, v24\n\t"
    "v_accvgpr_write_b32 a227, v26\n\t"
    "v_mfma_f32_16x16x32_bf16 v[172:175], a[244:247], a[92:95], v[172:175]\n\t"
    "v_exp_f32 v30, v30\n\t"
    "v_exp_f32 v31, v31\n\t"
    "v_mfma_f32_16x16x32_bf16 v[176:179], a[240:243], a[112:115], v[176:179]\n\t"
    "v_fma_f32 v32, v32, s55, v6\n\t"
    "v_fma_f32 v33, v33, s55, v6\n\t"
    "v_mfma_f32_16x16x32_bf16 v[176:179], a[244:247], a[116:119], v[176:179]\n\t"
    "v_exp_f32 v32, v32\n\t"
    "v_mfma_f32_16x16x32_bf16 v[180:183], a[240:243], a[136:139], v[180:183]\n\t"
    "v_cvt_pk_bf16_f32 v28, v28, v29\n\t"
    "v_cvt_pk_bf16_f32 v30, v30, v31\n\t"
    "v_mfma_f32_16x16x32_bf16 v[180:183], a[244:247], a[140:143], v[180:183]\n\t"
    "s_waitcnt lgkmcnt(0)\n\t"
    "ds_read_b128 a[240:243], v0 offset:31744\n\t"
    "ds_read_b128 a[244:247], v0 offset:32256\n\t"
    "v_exp_f32 v33, v33\n\t"
    "v_mfma_f32_16x16x32_bf16 v[16:19], a[248:251], a[0:3], 0\n\t"
    "v_fma_f32 v34, v34, s55, v6\n\t"
    "v_fma_f32 v35, v35, s55, v6\n\t"
    "v_mfma_f32_16x16x32_bf16 v[16:19], a[252:255], a[4:7], v[16:19]\n\t"
    "v_accvgpr_write_b32 a230, v28\n\t"
    "v_accvgpr_write_b32 a231, v30\n\t"
    "v_exp_f32 v34, v34\n\t"
    "v_mfma_f32_16x16x32_bf16 v[10:13], v[40:43], a[224:227], v[10:13]\n\t"
    "v_exp_f32 v35, v35\n\t"
    "v_mfma_f32_16x16x32_bf16 v[10:13], v[44:47], a[228:231], v[10:13]\n\t"
    "v_fma_f32 v36, v36, s55, v7\n\t"
    "v_fma_f32 v37, v37, s55, v7\n\t"
    "v_mfma_f32_16x16x32_bf16 v[20:23], a[248:251], a[24:27], 0\n\t"
    "v_exp_f32 v36, v36\n\t"
    "v_mfma_f32_16x16x32_bf16 v[20:23], a[252:255], a[28:31], v[20:23]\n\t"
    "buffer_load_dwordx4 v1, s[44:47], s53 offen nt lds\n\t"
    "v_cvt_pk_bf16_f32 v32, v32, v33\n\t"
    "v_cvt_pk_bf16_f32 v34, v34, v35\n\t"
    "v_mfma_f32_16x16x32_bf16 v[24:27], a[248:251], a[48:51], 0\n\t"
    "s_addk_i32 s53, 3072\n\t"
    "v_exp_f32 v37, v37\n\t"
    "v_mfma_f32_16x16x32_bf16 v[24:27], a[252:255], a[52:55], v[24:27]\n\t"
    "s_addk_i32 m0, 1024\n\t"
    "v_fma_f32 v38, v38, s55, v7\n\t"
    "v_fma_f32 v39, v39, s55, v7\n\t"
    "v_mfma_f32_16x16x32_bf16 v[28:31], a[248:251], a[72:75], 0\n\t"
    "v_accvgpr_write_b32 a234, v32\n\t"
    "v_accvgpr_write_b32 a235, v34\n\t"
    "v_mfma_f32_16x16x32_bf16 v[28:31], a[252:255], a[76:79], v[28:31]\n\t"
    "v_exp_f32 v38, v38\n\t"
    "v_exp_f32 v39, v39\n\t"
    "v_mfma_f32_16x16x32_bf16 v[32:35], a[248:251], a[96:99], 0\n\t"
    "v_cvt_pk_bf16_f32 v36, v36, v37\n\t"
    "v_cvt_pk_bf16_f32 v38, v38, v39\n\t"
    "v_mfma_f32_16x16x32_bf16 v[32:35], a[252:255], a[100:103], v[32:35]\n\t"
    "v_accvgpr_write_b32 a238, v36\n\t"
    "v_accvgpr_write_b32 a239, v38\n\t"
    "v_mfma_f32_16x16x32_bf16 v[36:39], a[248:251], a[120:123], 0\n\t"
    "v_fma_f32 v160, v160, s55, v2\n\t"
    "v_fma_f32 v161, v161, s55, v2\n\t"
    "v_mfma_f32_16x16x32_bf16 v[36:39], a[252:255], a[124:127], v[36:39]\n\t"
    "s_waitcnt lgkmcnt(0)\n\t"
    "ds_read_b128 a[248:251], v0 offset:32768\n\t"
    "ds_read_b128 a[252:255], v0 offset:33280\n\t"
    "v_exp_f32 v160, v160\n\t"
    "v_mfma_f32_16x16x32_bf16 v[12:15], v[40:43], a[232:235], v[12:15]\n\t"
    "v_exp_f32 v161, v161\n\t"
    "v_mfma_f32_16x16x32_bf16 v[12:15], v[44:47], a[236:239], v[12:15]\n\t"
    "v_fma_f32 v162, v162, s55, v2\n\t"
    "v_fma_f32 v163, v163, s55, v2\n\t"
    "v_mfma_f32_16x16x32_bf16 v[16:19], a[240:243], a[8:11], v[16:19]\n\t"
    "v_exp_f32 v162, v162\n\t"
    "v_mfma_f32_16x16x32_bf16 v[16:19], a[244:247], a[12:15], v[16:19]\n\t"
    "v_exp_f32 v163, v163\n\t"
    "v_mfma_f32_16x16x32_bf16 v[20:23], a[240:243], a[32:35], v[20:23]\n\t"
    "v_fma_f32 v164, v164, s55, v3\n\t"
    "v_fma_f32 v165, v165, s55, v3\n\t"
    "v_mfma_f32_16x16x32_bf16 v[20:23], a[244:247], a[36:39], v[20:23]\n\t"
    "buffer_load_dwordx4 v1, s[44:47], s53 offen nt lds\n\t"
    "v_exp_f32 v164, v164\n\t"
    "v_mfma_f32_16x16x32_bf16 v[24:27], a[240:243], a[56:59], v[24:27]\n\t"
    "s_mov_b32 s53, 27648\n\t"
    "v_cvt_pk_bf16_f32 v160, v160, v161\n\t"
    "v_cvt_pk_bf16_f32 v161, v162, v163\n\t"
    "v_mfma_f32_16x16x32_bf16 v[24:27], a[244:247], a[60:63], v[24:27]\n\t"
    "s_add_u32 m0, s52, 98304\n\t"
    "v_exp_f32 v165, v165\n\t"
    "v_mfma_f32_16x16x32_bf16 v[28:31], a[240:243], a[80:83], v[28:31]\n\t"
    "v_fma_f32 v166, v166, s55, v3\n\t"
    "v_fma_f32 v167, v167, s55, v3\n\t"
    "v_mfma_f32_16x16x32_bf16 v[28:31], a[244:247], a[84:87], v[28:31]\n\t"
    "v_exp_f32 v166, v166\n\t"
    "v_mfma_f32_16x16x32_bf16 v[32:35], a[240:243], a[104:107], v[32:35]\n\t"
    "v_exp_f32 v167, v167\n\t"
    "v_mfma_f32_16x16x32_bf16 v[32:35], a[244:247], a[108:111], v[32:35]\n\t"
    "v_fma_f32 v168, v168, s55, v4\n\t"
    "v_fma_f32 v169, v169, s55, v4\n\t"
    "v_mfma_f32_16x16x32_bf16 v[36:39], a[240:243], a[128:131], v[36:39]\n\t"
    "v_exp_f32 v168, v168\n\t"
    "v_mfma_f32_16x16x32_bf16 v[36:39], a[244:247], a[132:135], v[36:39]\n\t"
    "s_waitcnt lgkmcnt(0)\n\t"
    "v_add_u32 v0, 0xffff1000, v0\n\t"
    "ds_read_b128 a[240:243], v0 offset:0\n\t"
    "ds_read_b128 a[244:247], v0 offset:512\n\t"
    "v_cvt_pk_bf16_f32 v164, v164, v165\n\t"
    "v_cvt_pk_bf16_f32 v165, v166, v167\n\t"
    "v_mfma_f32_16x16x32_bf16 v[16:19], a[248:251], a[16:19], v[16:19]\n\t"
    "v_exp_f32 v169, v169\n\t"
    "v_mfma_f32_16x16x32_bf16 v[16:19], a[252:255], a[20:23], v[16:19]\n\t"
    "buffer_load_dwordx4 v1, s[44:47], s53 offen nt lds\n\t"
    "v_fma_f32 v170, v170, s55, v4\n\t"
    "v_fma_f32 v171, v171, s55, v4\n\t"
    "v_exp_f32 v170, v170\n\t"
    "v_mfma_f32_16x16x32_bf16 v[20:23], a[248:251], a[40:43], v[20:23]\n\t"
    "s_mov_b32 s53, s65\n\t"
    "v_exp_f32 v171, v171\n\t"
    "v_mfma_f32_16x16x32_bf16 v[20:23], a[252:255], a[44:47], v[20:23]\n\t"
    "s_addk_i32 m0, 1024\n\t"
    "v_fma_f32 v172, v172, s55, v5\n\t"
    "v_fma_f32 v173, v173, s55, v5\n\t"
    "v_mfma_f32_16x16x32_bf16 v[24:27], a[248:251], a[64:67], v[24:27]\n\t"
    "v_exp_f32 v172, v172\n\t"
    "v_mfma_f32_16x16x32_bf16 v[24:27], a[252:255], a[68:71], v[24:27]\n\t"
    "v_cvt_pk_bf16_f32 v168, v168, v169\n\t"
    "v_cvt_pk_bf16_f32 v169, v170, v171\n\t"
    "v_mfma_f32_16x16x32_bf16 v[28:31], a[248:251], a[88:91], v[28:31]\n\t"
    "v_exp_f32 v173, v173\n\t"
    "v_mfma_f32_16x16x32_bf16 v[28:31], a[252:255], a[92:95], v[28:31]\n\t"
    "v_fma_f32 v174, v174, s55, v5\n\t"
    "v_fma_f32 v175, v175, s55, v5\n\t"
    "v_mfma_f32_16x16x32_bf16 v[32:35], a[248:251], a[112:115], v[32:35]\n\t"
    "v_exp_f32 v174, v174\n\t"
    "v_mfma_f32_16x16x32_bf16 v[32:35], a[252:255], a[116:119], v[32:35]\n\t"
    "v_exp_f32 v175, v175\n\t"
    "v_mfma_f32_16x16x32_bf16 v[36:39], a[248:251], a[136:139], v[36:39]\n\t"
    "v_fma_f32 v176, v176, s55, v6\n\t"
    "v_fma_f32 v177, v177, s55, v6\n\t"
    "v_mfma_f32_16x16x32_bf16 v[36:39], a[252:255], a[140:143], v[36:39]\n\t"
    "s_waitcnt lgkmcnt(0)\n\t"
    "ds_read_b128 a[248:251], v0 offset:1024\n\t"
    "ds_read_b128 a[252:255], v0 offset:1536\n\t"
    "v_exp_f32 v176, v176\n\t"
    "v_mfma_f32_16x16x32_bf16 v[184:187], a[240:243], a[0:3], 0\n\t"
    "v_cvt_pk_bf16_f32 v172, v172, v173\n\t"
    "v_cvt_pk_bf16_f32 v173, v174, v175\n\t"
    "v_mfma_f32_16x16x32_bf16 v[184:187], a[244:247], a[4:7], v[184:187]\n\t"
    "buffer_load_dwordx4 v1, s[44:47], s53 offen nt lds\n\t"
    "v_exp_f32 v177, v177\n\t"
    "v_mfma_f32_16x16x32_bf16 v[188:191], a[240:243], a[24:27], 0\n\t"
    "s_mov_b32 s53, s75\n\t"
    "v_fma_f32 v178, v178, s55, v6\n\t"
    "v_fma_f32 v179, v179, s55, v6\n\t"
    "v_mfma_f32_16x16x32_bf16 v[188:191], a[244:247], a[28:31], v[188:191]\n\t"
    "s_addk_i32 m0, 1024\n\t"
    "v_exp_f32 v178, v178\n\t"
    "v_mfma_f32_16x16x32_bf16 v[192:195], a[240:243], a[48:51], 0\n\t"
    "v_exp_f32 v179, v179\n\t"
    "v_mfma_f32_16x16x32_bf16 v[192:195], a[244:247], a[52:55], v[192:195]\n\t"
    "v_fma_f32 v180, v180, s55, v7\n\t"
    "v_fma_f32 v181, v181, s55, v7\n\t"
    "v_exp_f32 v180, v180\n\t"
    "v_mfma_f32_16x16x32_bf16 v[196:199], a[240:243], a[72:75], 0\n\t"
    "v_cvt_pk_bf16_f32 v176, v176, v177\n\t"
    "v_cvt_pk_bf16_f32 v177, v178, v179\n\t"
    "v_mfma_f32_16x16x32_bf16 v[196:199], a[244:247], a[76:79], v[196:199]\n\t"
    "v_exp_f32 v181, v181\n\t"
    "v_mfma_f32_16x16x32_bf16 v[200:203], a[240:243], a[96:99], 0\n\t"
    "v_fma_f32 v182, v182, s55, v7\n\t"
    "v_fma_f32 v183, v183, s55, v7\n\t"
    "v_mfma_f32_16x16x32_bf16 v[200:203], a[244:247], a[100:103], v[200:203]\n\t"
    "v_exp_f32 v182, v182\n\t"
    "v_mfma_f32_16x16x32_bf16 v[204:207], a[240:243], a[120:123], 0\n\t"
    "v_exp_f32 v183, v183\n\t"
    "v_mfma_f32_16x16x32_bf16 v[204:207], a[244:247], a[124:127], v[204:207]\n\t"
    "s_waitcnt lgkmcnt(0)\n\t"
    "ds_read_b128 a[240:243], v0 offset:2048\n\t"
    "ds_read_b128 a[244:247], v0 offset:2560\n\t"
    "v_cvt_pk_bf16_f32 v180, v180, v181\n\t"
    "v_cvt_pk_bf16_f32 v181, v182, v183\n\t"
    "v_mfma_f32_16x16x32_bf16 v[184:187], a[248:251], a[8:11], v[184:187]\n\t"
    "v_fma_f32 v16, v16, s55, v2\n\t"
    "v_fma_f32 v17, v17, s55, v2\n\t"
    "v_mfma_f32_16x16x32_bf16 v[184:187], a[252:255], a[12:15], v[184:187]\n\t"
    "buffer_store_dwordx4 v[248:251], v63, s[48:51], s65 offen offset:256 sc1\n\t"
    "v_exp_f32 v16, v16\n\t"
    "v_mfma_f32_16x16x32_bf16 v[188:191], a[248:251], a[32:35], v[188:191]\n\t"
    "v_exp_f32 v17, v17\n\t"
    "v_mfma_f32_16x16x32_bf16 v[188:191], a[252:255], a[36:39], v[188:191]\n\t"
    "v_fma_f32 v18, v18, s55, v2\n\t"
    "v_fma_f32 v19, v19, s55, v2\n\t"
    "v_exp_f32 v18, v18\n\t"
    "v_mfma_f32_16x16x32_bf16 v[192:195], a[248:251], a[56:59], v[192:195]\n\t"
    "v_exp_f32 v19, v19\n\t"
    "v_mfma_f32_16x16x32_bf16 v[192:195], a[252:255], a[60:63], v[192:195]\n\t"
    "v_fma_f32 v20, v20, s55, v3\n\t"
    "v_fma_f32 v21, v21, s55, v3\n\t"
    "v_mfma_f32_16x16x32_bf16 v[196:199], a[248:251], a[80:83], v[196:199]\n\t"
    "v_exp_f32 v20, v20\n\t"
    "v_mfma_f32_16x16x32_bf16 v[196:199], a[252:255], a[84:87], v[196:199]\n\t"
    "v_cvt_pk_bf16_f32 v162, v16, v17\n\t"
    "v_cvt_pk_bf16_f32 v163, v18, v19\n\t"
    "v_mfma_f32_16x16x32_bf16 v[200:203], a[248:251], a[104:107], v[200:203]\n\t"
    "v_exp_f32 v21, v21\n\t"
    "v_mfma_f32_16x16x32_bf16 v[200:203], a[252:255], a[108:111], v[200:203]\n\t"
    "v_fma_f32 v22, v22, s55, v3\n\t"
    "v_fma_f32 v23, v23, s55, v3\n\t"
    "v_mfma_f32_16x16x32_bf16 v[204:207], a[248:251], a[128:131], v[204:207]\n\t"
    "v_exp_f32 v22, v22\n\t"
    "v_mfma_f32_16x16x32_bf16 v[204:207], a[252:255], a[132:135], v[204:207]\n\t"
    "s_waitcnt lgkmcnt(0)\n\t"
    "ds_read_b128 a[248:251], v0 offset:6144\n\t"
    "ds_read_b128 a[252:255], v0 offset:6656\n\t"
    "v_exp_f32 v23, v23\n\t"
    "v_fma_f32 v24, v24, s55, v4\n\t"
    "v_fma_f32 v25, v25, s55, v4\n\t"
    "v_mfma_f32_16x16x32_bf16 v[184:187], a[240:243], a[16:19], v[184:187]\n\t"
    "v_exp_f32 v24, v24\n\t"
    "v_mfma_f32_16x16x32_bf16 v[184:187], a[244:247], a[20:23], v[184:187]\n\t"
    "buffer_load_dwordx4 v1, s[44:47], s53 offen nt lds\n\t"
    "v_cvt_pk_bf16_f32 v166, v20, v21\n\t"
    "v_cvt_pk_bf16_f32 v167, v22, v23\n\t"
    "v_mfma_f32_16x16x32_bf16 v[188:191], a[240:243], a[40:43], v[188:191]\n\t"
    "s_mov_b32 s53, s68\n\t"
    "v_exp_f32 v25, v25\n\t"
    "v_mfma_f32_16x16x32_bf16 v[188:191], a[244:247], a[44:47], v[188:191]\n\t"
    "s_add_u32 m0, s52, 36864\n\t"
    "v_fma_f32 v26, v26, s55, v4\n\t"
    "v_fma_f32 v27, v27, s55, v4\n\t"
    "v_mfma_f32_16x16x32_bf16 v[8:11], v[40:43], v[160:163], v[8:11]\n\t"
    "v_exp_f32 v26, v26\n\t"
    "v_mfma_f32_16x16x32_bf16 v[8:11], v[44:47], v[164:167], v[8:11]\n\t"
    "v_exp_f32 v27, v27\n\t"
    "v_fma_f32 v28, v28, s55, v5\n\t"
    "v_fma_f32 v29, v29, s55, v5\n\t"
    "v_mfma_f32_16x16x32_bf16 v[192:195], a[240:243], a[64:67], v[192:195]\n\t"
    "v_exp_f32 v28, v28\n\t"
    "v_mfma_f32_16x16x32_bf16 v[192:195], a[244:247], a[68:71], v[192:195]\n\t"
    "v_cvt_pk_bf16_f32 v170, v24, v25\n\t"
    "v_cvt_pk_bf16_f32 v171, v26, v27\n\t"
    "v_mfma_f32_16x16x32_bf16 v[196:199], a[240:243], a[88:91], v[196:199]\n\t"
    "v_exp_f32 v29, v29\n\t"
    "v_mfma_f32_16x16x32_bf16 v[196:199], a[244:247], a[92:95], v[196:199]\n\t"
    "v_fma_f32 v30, v30, s55, v5\n\t"
    "v_fma_f32 v31, v31, s55, v5\n\t"
    "v_mfma_f32_16x16x32_bf16 v[200:203], a[240:243], a[112:115], v[200:203]\n\t"
    "v_exp_f32 v30, v30\n\t"
    "v_mfma_f32_16x16x32_bf16 v[200:203], a[244:247], a[116:119], v[200:203]\n\t"
    "buffer_load_dwordx4 v1, s[36:39], s53 offen nt lds\n\t"
    "v_exp_f32 v31, v31\n\t"
    "v_mfma_f32_16x16x32_bf16 v[204:207], a[240:243], a[136:139], v[204:207]\n\t"
    "s_addk_i32 m0, 896\n\t"
    "v_fma_f32 v32, v32, s55, v6\n\t"
    "v_fma_f32 v33, v33, s55, v6\n\t"
    "v_mfma_f32_16x16x32_bf16 v[204:207], a[244:247], a[140:143], v[204:207]\n\t"
    "s_waitcnt lgkmcnt(0)\n\t"
    "ds_read_b128 a[240:243], v0 offset:7168\n\t"
    "ds_read_b128 a[244:247], v0 offset:7680\n\t"
    "v_exp_f32 v32, v32\n\t"
    "v_mfma_f32_16x16x32_bf16 v[16:19], a[248:251], a[0:3], 0\n\t"
    "v_cvt_pk_bf16_f32 v174, v28, v29\n\t"
    "v_cvt_pk_bf16_f32 v175, v30, v31\n\t"
    "v_mfma_f32_16x16x32_bf16 v[16:19], a[252:255], a[4:7], v[16:19]\n\t"
    "v_exp_f32 v33, v33\n\t"
    "v_fma_f32 v34, v34, s55, v6\n\t"
    "v_fma_f32 v35, v35, s55, v6\n\t"
    "v_mfma_f32_16x16x32_bf16 v[10:13], v[40:43], v[168:171], v[10:13]\n\t"
    "v_exp_f32 v34, v34\n\t"
    "v_mfma_f32_16x16x32_bf16 v[10:13], v[44:47], v[172:175], v[10:13]\n\t"
    "v_exp_f32 v35, v35\n\t"
    "v_mfma_f32_16x16x32_bf16 v[20:23], a[248:251], a[24:27], 0\n\t"
    "v_fma_f32 v36, v36, s55, v7\n\t"
    "v_fma_f32 v37, v37, s55, v7\n\t"
    "v_mfma_f32_16x16x32_bf16 v[20:23], a[252:255], a[28:31], v[20:23]\n\t"
    "v_exp_f32 v36, v36\n\t"
    "v_mfma_f32_16x16x32_bf16 v[24:27], a[248:251], a[48:51], 0\n\t"
    "v_cvt_pk_bf16_f32 v178, v32, v33\n\t"
    "v_cvt_pk_bf16_f32 v179, v34, v35\n\t"
    "v_mfma_f32_16x16x32_bf16 v[24:27], a[252:255], a[52:55], v[24:27]\n\t"
    "v_exp_f32 v37, v37\n\t"
    "v_mfma_f32_16x16x32_bf16 v[28:31], a[248:251], a[72:75], 0\n\t"
    "v_fma_f32 v38, v38, s55, v7\n\t"
    "v_fma_f32 v39, v39, s55, v7\n\t"
    "v_mfma_f32_16x16x32_bf16 v[28:31], a[252:255], a[76:79], v[28:31]\n\t"
    "v_exp_f32 v38, v38\n\t"
    "v_mfma_f32_16x16x32_bf16 v[32:35], a[248:251], a[96:99], 0\n\t"
    "v_exp_f32 v39, v39\n\t"
    "v_mfma_f32_16x16x32_bf16 v[32:35], a[252:255], a[100:103], v[32:35]\n\t"
    "buffer_load_dwordx4 v1, s[36:39], s53 offen offset:128 nt lds\n\t"
    "v_cvt_pk_bf16_f32 v182, v36, v37\n\t"
    "v_cvt_pk_bf16_f32 v183, v38, v39\n\t"
    "v_mfma_f32_16x16x32_bf16 v[36:39], a[248:251], a[120:123], 0\n\t"
    "s_addk_i32 m0, 896\n\t"
    "v_fma_f32 v184, v184, s55, v2\n\t"
    "v_fma_f32 v185, v185, s55, v2\n\t"
    "v_mfma_f32_16x16x32_bf16 v[36:39], a[252:255], a[124:127], v[36:39]\n\t"
    "s_waitcnt lgkmcnt(0)\n\t"
    "ds_read_b128 a[248:251], v0 offset:8192\n\t"
    "ds_read_b128 a[252:255], v0 offset:8704\n\t"
    "v_exp_f32 v184, v184\n\t"
    "v_mfma_f32_16x16x32_bf16 v[12:15], v[40:43], v[176:179], v[12:15]\n\t"
    "v_exp_f32 v185, v185\n\t"
    "v_mfma_f32_16x16x32_bf16 v[12:15], v[44:47], v[180:183], v[12:15]\n\t"
    "v_fma_f32 v186, v186, s55, v2\n\t"
    "v_fma_f32 v187, v187, s55, v2\n\t"
    "v_mfma_f32_16x16x32_bf16 v[16:19], a[240:243], a[8:11], v[16:19]\n\t"
    "v_exp_f32 v186, v186\n\t"
    "v_mfma_f32_16x16x32_bf16 v[16:19], a[244:247], a[12:15], v[16:19]\n\t"
    "v_exp_f32 v187, v187\n\t"
    "v_mfma_f32_16x16x32_bf16 v[20:23], a[240:243], a[32:35], v[20:23]\n\t"
    "v_fma_f32 v188, v188, s55, v3\n\t"
    "v_fma_f32 v189, v189, s55, v3\n\t"
    "v_mfma_f32_16x16x32_bf16 v[20:23], a[244:247], a[36:39], v[20:23]\n\t"
    "v_exp_f32 v188, v188\n\t"
    "v_cvt_pk_bf16_f32 v184, v184, v185\n\t"
    "v_cvt_pk_bf16_f32 v185, v186, v187\n\t"
    "v_mfma_f32_16x16x32_bf16 v[24:27], a[240:243], a[56:59], v[24:27]\n\t"
    "v_exp_f32 v189, v189\n\t"
    "v_mfma_f32_16x16x32_bf16 v[24:27], a[244:247], a[60:63], v[24:27]\n\t"
    "v_fma_f32 v190, v190, s55, v3\n\t"
    "v_fma_f32 v191, v191, s55, v3\n\t"
    "v_mfma_f32_16x16x32_bf16 v[28:31], a[240:243], a[80:83], v[28:31]\n\t"
    "v_exp_f32 v190, v190\n\t"
    "v_mfma_f32_16x16x32_bf16 v[28:31], a[244:247], a[84:87], v[28:31]\n\t"
    "buffer_load_dwordx4 v1, s[36:39], s53 offen offset:256 nt lds\n\t"
    "v_exp_f32 v191, v191\n\t"
    "v_mfma_f32_16x16x32_bf16 v[32:35], a[240:243], a[104:107], v[32:35]\n\t"
    "s_mov_b32 s53, s69\n\t"
    "v_fma_f32 v192, v192, s55, v4\n\t"
    "v_fma_f32 v193, v193, s55, v4\n\t"
    "v_mfma_f32_16x16x32_bf16 v[32:35], a[244:247], a[108:111], v[32:35]\n\t"
    "s_add_u32 m0, s52, 0\n\t"
    "v_exp_f32 v192, v192\n\t"
    "v_mfma_f32_16x16x32_bf16 v[36:39], a[240:243], a[128:131], v[36:39]\n\t"
    "v_cvt_pk_bf16_f32 v188, v188, v189\n\t"
    "v_cvt_pk_bf16_f32 v189, v190, v191\n\t"
    "v_mfma_f32_16x16x32_bf16 v[36:39], a[244:247], a[132:135], v[36:39]\n\t"
    "s_waitcnt vmcnt(22) lgkmcnt(0)\n\t"
    "s_barrier\n\t"
    "ds_read_b128 a[240:243], v0 offset:49152\n\t"
    "ds_read_b128 a[244:247], v0 offset:49664\n\t"
    "v_exp_f32 v193, v193\n\t"
    "v_mfma_f32_16x16x32_bf16 v[16:19], a[248:251], a[16:19], v[16:19]\n\t"
    "v_fma_f32 v194, v194, s55, v4\n\t"
    "v_fma_f32 v195, v195, s55, v4\n\t"
    "v_mfma_f32_16x16x32_bf16 v[16:19], a[252:255], a[20:23], v[16:19]\n\t"
    "v_exp_f32 v194, v194\n\t"
    "v_mfma_f32_16x16x32_bf16 v[20:23], a[248:251], a[40:43], v[20:23]\n\t"
    "v_exp_f32 v195, v195\n\t"
    "v_mfma_f32_16x16x32_bf16 v[20:23], a[252:255], a[44:47], v[20:23]\n\t"
    "v_fma_f32 v196, v196, s55, v5\n\t"
    "v_fma_f32 v197, v197, s55, v5\n\t"
    "v_mfma_f32_16x16x32_bf16 v[24:27], a[248:251], a[64:67], v[24:27]\n\t"
    "v_exp_f32 v196, v196\n\t"
    "v_mfma_f32_16x16x32_bf16 v[24:27], a[252:255], a[68:71], v[24:27]\n\t"
    "v_cvt_pk_bf16_f32 v192, v192, v193\n\t"
    "v_cvt_pk_bf16_f32 v193, v194, v195\n\t"
    "v_mfma_f32_16x16x32_bf16 v[28:31], a[248:251], a[88:91], v[28:31]\n\t"
    "v_exp_f32 v197, v197\n\t"
    "v_mfma_f32_16x16x32_bf16 v[28:31], a[252:255], a[92:95], v[28:31]\n\t"
    "buffer_load_dwordx4 v1, s[36:39], s53 offen nt lds\n\t"
    "v_fma_f32 v198, v198, s55, v5\n\t"
    "v_fma_f32 v199, v199, s55, v5\n\t"
    "v_mfma_f32_16x16x32_bf16 v[32:35], a[248:251], a[112:115], v[32:35]\n\t"
    "s_addk_i32 m0, 896\n\t"
    "v_exp_f32 v198, v198\n\t"
    "v_mfma_f32_16x16x32_bf16 v[32:35], a[252:255], a[116:119], v[32:35]\n\t"
    "v_exp_f32 v199, v199\n\t"
    "v_mfma_f32_16x16x32_bf16 v[36:39], a[248:251], a[136:139], v[36:39]\n\t"
    "v_fma_f32 v200, v200, s55, v6\n\t"
    "v_fma_f32 v201, v201, s55, v6\n\t"
    "v_mfma_f32_16x16x32_bf16 v[36:39], a[252:255], a[140:143], v[36:39]\n\t"
    "s_waitcnt lgkmcnt(0)\n\t"
    "ds_read_b128 a[248:251], v0 offset:50176\n\t"
    "ds_read_b128 a[252:255], v0 offset:50688\n\t"
    "v_exp_f32 v200, v200\n\t"
    "v_cvt_pk_bf16_f32 v196, v196, v197\n\t"
    "v_cvt_pk_bf16_f32 v197, v198, v199\n\t"
    "v_mfma_f32_16x16x32_bf16 v[208:211], a[240:243], a[0:3], 0\n\t"
    "v_exp_f32 v201, v201\n\t"
    "v_mfma_f32_16x16x32_bf16 v[208:211], a[244:247], a[4:7], v[208:211]\n\t"
    "v_fma_f32 v202, v202, s55, v6\n\t"
    "v_fma_f32 v203, v203, s55, v6\n\t"
    "v_mfma_f32_16x16x32_bf16 v[212:215], a[240:243], a[24:27], 0\n\t"
    "v_exp_f32 v202, v202\n\t"
    "v_mfma_f32_16x16x32_bf16 v[212:215], a[244:247], a[28:31], v[212:215]\n\t"
    "v_exp_f32 v203, v203\n\t"
    "v_mfma_f32_16x16x32_bf16 v[216:219], a[240:243], a[48:51], 0\n\t"
    "v_fma_f32 v204, v204, s55, v7\n\t"
    "v_fma_f32 v205, v205, s55, v7\n\t"
    "v_mfma_f32_16x16x32_bf16 v[216:219], a[244:247], a[52:55], v[216:219]\n\t"
    "buffer_store_dwordx4 v[252:255], v63, s[48:51], s75 offen offset:256 sc1\n\t"
    "v_exp_f32 v204, v204\n\t"
    "v_mfma_f32_16x16x32_bf16 v[220:223], a[240:243], a[72:75], 0\n\t"
    "v_cvt_pk_bf16_f32 v200, v200, v201\n\t"
    "v_cvt_pk_bf16_f32 v201, v202, v203\n\t"
    "v_mfma_f32_16x16x32_bf16 v[220:223], a[244:247], a[76:79], v[220:223]\n\t"
    "v_exp_f32 v205, v205\n\t"
    "v_mfma_f32_16x16x32_bf16 v[224:227], a[240:243], a[96:99], 0\n\t"
    "v_fma_f32 v206, v206, s55, v7\n\t"
    "v_fma_f32 v207, v207, s55, v7\n\t"
    "v_mfma_f32_16x16x32_bf16 v[224:227], a[244:247], a[100:103], v[224:227]\n\t"
    "v_exp_f32 v206, v206\n\t"
    "v_mfma_f32_16x16x32_bf16 v[228:231], a[240:243], a[120:123], 0\n\t"
    "v_exp_f32 v207, v207\n\t"
    "v_mfma_f32_16x16x32_bf16 v[228:231], a[244:247], a[124:127], v[228:231]\n\t"
    "s_waitcnt lgkmcnt(0)\n\t"
    "ds_read_b128 a[240:243], v0 offset:51200\n\t"
    "ds_read_b128 a[244:247], v0 offset:51712\n\t"
    "v_cvt_pk_bf16_f32 v204, v204, v205\n\t"
    "v_cvt_pk_bf16_f32 v205, v206, v207\n\t"
    "v_fma_f32 v16, v16, s55, v2\n\t"
    "v_fma_f32 v17, v17, s55, v2\n\t"
    "v_mfma_f32_16x16x32_bf16 v[208:211], a[248:251], a[8:11], v[208:211]\n\t"
    "v_exp_f32 v16, v16\n\t"
    "v_mfma_f32_16x16x32_bf16 v[208:211], a[252:255], a[12:15], v[208:211]\n\t"
    "v_exp_f32 v17, v17\n\t"
    "v_mfma_f32_16x16x32_bf16 v[212:215], a[248:251], a[32:35], v[212:215]\n\t"
    "v_fma_f32 v18, v18, s55, v2\n\t"
    "v_fma_f32 v19, v19, s55, v2\n\t"
    "v_mfma_f32_16x16x32_bf16 v[212:215], a[252:255], a[36:39], v[212:215]\n\t"
    "v_exp_f32 v18, v18\n\t"
    "v_mfma_f32_16x16x32_bf16 v[216:219], a[248:251], a[56:59], v[216:219]\n\t"
    "v_exp_f32 v19, v19\n\t"
    "v_mfma_f32_16x16x32_bf16 v[216:219], a[252:255], a[60:63], v[216:219]\n\t"
    "buffer_load_dwordx4 v1, s[36:39], s53 offen offset:128 nt lds\n\t"
    "v_fma_f32 v20, v20, s55, v3\n\t"
    "v_fma_f32 v21, v21, s55, v3\n\t"
    "v_mfma_f32_16x16x32_bf16 v[220:223], a[248:251], a[80:83], v[220:223]\n\t"
    "s_addk_i32 m0, 896\n\t"
    "v_exp_f32 v20, v20\n\t"
    "v_mfma_f32_16x16x32_bf16 v[220:223], a[252:255], a[84:87], v[220:223]\n\t"
    "v_cvt_pk_bf16_f32 v186, v16, v17\n\t"
    "v_cvt_pk_bf16_f32 v187, v18, v19\n\t"
    "v_mfma_f32_16x16x32_bf16 v[224:227], a[248:251], a[104:107], v[224:227]\n\t"
    "v_exp_f32 v21, v21\n\t"
    "v_mfma_f32_16x16x32_bf16 v[224:227], a[252:255], a[108:111], v[224:227]\n\t"
    "v_fma_f32 v22, v22, s55, v3\n\t"
    "v_fma_f32 v23, v23, s55, v3\n\t"
    "v_exp_f32 v22, v22\n\t"
    "v_mfma_f32_16x16x32_bf16 v[228:231], a[248:251], a[128:131], v[228:231]\n\t"
    "v_exp_f32 v23, v23\n\t"
    "v_mfma_f32_16x16x32_bf16 v[228:231], a[252:255], a[132:135], v[228:231]\n\t"
    "s_waitcnt lgkmcnt(0)\n\t"
    "ds_read_b128 a[248:251], v0 offset:55296\n\t"
    "ds_read_b128 a[252:255], v0 offset:55808\n\t"
    "v_fma_f32 v24, v24, s55, v4\n\t"
    "v_fma_f32 v25, v25, s55, v4\n\t"
    "v_mfma_f32_16x16x32_bf16 v[208:211], a[240:243], a[16:19], v[208:211]\n\t"
    "v_exp_f32 v24, v24\n\t"
    "v_mfma_f32_16x16x32_bf16 v[208:211], a[244:247], a[20:23], v[208:211]\n\t"
    "v_cvt_pk_bf16_f32 v190, v20, v21\n\t"
    "v_cvt_pk_bf16_f32 v191, v22, v23\n\t"
    "v_mfma_f32_16x16x32_bf16 v[212:215], a[240:243], a[40:43], v[212:215]\n\t"
    "v_exp_f32 v25, v25\n\t"
    "v_mfma_f32_16x16x32_bf16 v[212:215], a[244:247], a[44:47], v[212:215]\n\t"
    "buffer_load_dwordx4 v1, s[36:39], s53 offen offset:256 nt lds\n\t"
    "v_fma_f32 v26, v26, s55, v4\n\t"
    "v_fma_f32 v27, v27, s55, v4\n\t"
    "v_exp_f32 v26, v26\n\t"
    "v_mfma_f32_16x16x32_bf16 v[8:11], v[40:43], v[184:187], v[8:11]\n\t"
    "s_mov_b32 s53, s70\n\t"
    "v_exp_f32 v27, v27\n\t"
    "v_mfma_f32_16x16x32_bf16 v[8:11], v[44:47], v[188:191], v[8:11]\n\t"
    "s_add_u32 m0, s52, 86016\n\t"
    "v_fma_f32 v28, v28, s55, v5\n\t"
    "v_fma_f32 v29, v29, s55, v5\n\t"
    "v_mfma_f32_16x16x32_bf16 v[216:219], a[240:243], a[64:67], v[216:219]\n\t"
    "v_exp_f32 v28, v28\n\t"
    "v_mfma_f32_16x16x32_bf16 v[216:219], a[244:247], a[68:71], v[216:219]\n\t"
    "v_cvt_pk_bf16_f32 v194, v24, v25\n\t"
    "v_cvt_pk_bf16_f32 v195, v26, v27\n\t"
    "v_mfma_f32_16x16x32_bf16 v[220:223], a[240:243], a[88:91], v[220:223]\n\t"
    "v_exp_f32 v29, v29\n\t"
    "v_mfma_f32_16x16x32_bf16 v[220:223], a[244:247], a[92:95], v[220:223]\n\t"
    "v_fma_f32 v30, v30, s55, v5\n\t"
    "v_fma_f32 v31, v31, s55, v5\n\t"
    "v_mfma_f32_16x16x32_bf16 v[224:227], a[240:243], a[112:115], v[224:227]\n\t"
    "v_exp_f32 v30, v30\n\t"
    "v_mfma_f32_16x16x32_bf16 v[224:227], a[244:247], a[116:119], v[224:227]\n\t"
    "v_exp_f32 v31, v31\n\t"
    "v_mfma_f32_16x16x32_bf16 v[228:231], a[240:243], a[136:139], v[228:231]\n\t"
    "v_fma_f32 v32, v32, s55, v6\n\t"
    "v_fma_f32 v33, v33, s55, v6\n\t"
    "v_mfma_f32_16x16x32_bf16 v[228:231], a[244:247], a[140:143], v[228:231]\n\t"
    "s_waitcnt lgkmcnt(0)\n\t"
    "ds_read_b128 a[240:243], v0 offset:56320\n\t"
    "ds_read_b128 a[244:247], v0 offset:56832\n\t"
    "v_exp_f32 v32, v32\n\t"
    "v_mfma_f32_16x16x32_bf16 v[16:19], a[248:251], a[0:3], 0\n\t"
    "v_cvt_pk_bf16_f32 v198, v28, v29\n\t"
    "v_cvt_pk_bf16_f32 v199, v30, v31\n\t"
    "v_mfma_f32_16x16x32_bf16 v[16:19], a[252:255], a[4:7], v[16:19]\n\t"
    "v_exp_f32 v33, v33\n\t"
    "v_fma_f32 v34, v34, s55, v6\n\t"
    "v_fma_f32 v35, v35, s55, v6\n\t"
    "v_mfma_f32_16x16x32_bf16 v[10:13], v[40:43], v[192:195], v[10:13]\n\t"
    "v_exp_f32 v34, v34\n\t"
    "v_mfma_f32_16x16x32_bf16 v[10:13], v[44:47], v[196:199], v[10:13]\n\t"
    "v_exp_f32 v35, v35\n\t"
    "v_mfma_f32_16x16x32_bf16 v[20:23], a[248:251], a[24:27], 0\n\t"
    "v_fma_f32 v36, v36, s55, v7\n\t"
    "v_fma_f32 v37, v37, s55, v7\n\t"
    "v_mfma_f32_16x16x32_bf16 v[20:23], a[252:255], a[28:31], v[20:23]\n\t"
    "buffer_load_dwordx4 v1, s[36:39], s53 offen nt lds\n\t"
    "v_exp_f32 v36, v36\n\t"
    "v_mfma_f32_16x16x32_bf16 v[24:27], a[248:251], a[48:51], 0\n\t"
    "s_addk_i32 m0, 896\n\t"
    "v_cvt_pk_bf16_f32 v202, v32, v33\n\t"
    "v_cvt_pk_bf16_f32 v203, v34, v35\n\t"
    "v_mfma_f32_16x16x32_bf16 v[24:27], a[252:255], a[52:55], v[24:27]\n\t"
    "v_exp_f32 v37, v37\n\t"
    "v_mfma_f32_16x16x32_bf16 v[28:31], a[248:251], a[72:75], 0\n\t"
    "v_fma_f32 v38, v38, s55, v7\n\t"
    "v_fma_f32 v39, v39, s55, v7\n\t"
    "v_mfma_f32_16x16x32_bf16 v[28:31], a[252:255], a[76:79], v[28:31]\n\t"
    "v_exp_f32 v38, v38\n\t"
    "v_mfma_f32_16x16x32_bf16 v[32:35], a[248:251], a[96:99], 0\n\t"
    "v_exp_f32 v39, v39\n\t"
    "v_mfma_f32_16x16x32_bf16 v[32:35], a[252:255], a[100:103], v[32:35]\n\t"
    "v_cvt_pk_bf16_f32 v206, v36, v37\n\t"
    "v_cvt_pk_bf16_f32 v207, v38, v39\n\t"
    "v_mfma_f32_16x16x32_bf16 v[36:39], a[248:251], a[120:123], 0\n\t"
    "v_fma_f32 v208, v208, s55, v2\n\t"
    "v_fma_f32 v209, v209, s55, v2\n\t"
    "v_mfma_f32_16x16x32_bf16 v[36:39], a[252:255], a[124:127], v[36:39]\n\t"
    "s_waitcnt lgkmcnt(0)\n\t"
    "ds_read_b128 a[248:251], v0 offset:57344\n\t"
    "ds_read_b128 a[252:255], v0 offset:57856\n\t"
    "v_exp_f32 v208, v208\n\t"
    "v_mfma_f32_16x16x32_bf16 v[12:15], v[40:43], v[200:203], v[12:15]\n\t"
    "v_exp_f32 v209, v209\n\t"
    "v_mfma_f32_16x16x32_bf16 v[12:15], v[44:47], v[204:207], v[12:15]\n\t"
    "v_fma_f32 v210, v210, s55, v2\n\t"
    "v_fma_f32 v211, v211, s55, v2\n\t"
    "v_mfma_f32_16x16x32_bf16 v[16:19], a[240:243], a[8:11], v[16:19]\n\t"
    "v_exp_f32 v210, v210\n\t"
    "v_mfma_f32_16x16x32_bf16 v[16:19], a[244:247], a[12:15], v[16:19]\n\t"
    "buffer_load_dwordx4 v1, s[36:39], s53 offen offset:128 nt lds\n\t"
    "v_exp_f32 v211, v211\n\t"
    "v_mfma_f32_16x16x32_bf16 v[20:23], a[240:243], a[32:35], v[20:23]\n\t"
    "s_addk_i32 m0, 896\n\t"
    "v_fma_f32 v212, v212, s55, v3\n\t"
    "v_fma_f32 v213, v213, s55, v3\n\t"
    "v_mfma_f32_16x16x32_bf16 v[20:23], a[244:247], a[36:39], v[20:23]\n\t"
    "v_exp_f32 v212, v212\n\t"
    "v_cvt_pk_bf16_f32 v208, v208, v209\n\t"
    "v_cvt_pk_bf16_f32 v209, v210, v211\n\t"
    "v_mfma_f32_16x16x32_bf16 v[24:27], a[240:243], a[56:59], v[24:27]\n\t"
    "v_exp_f32 v213, v213\n\t"
    "v_mfma_f32_16x16x32_bf16 v[24:27], a[244:247], a[60:63], v[24:27]\n\t"
    "v_fma_f32 v214, v214, s55, v3\n\t"
    "v_fma_f32 v215, v215, s55, v3\n\t"
    "v_mfma_f32_16x16x32_bf16 v[28:31], a[240:243], a[80:83], v[28:31]\n\t"
    "v_exp_f32 v214, v214\n\t"
    "v_mfma_f32_16x16x32_bf16 v[28:31], a[244:247], a[84:87], v[28:31]\n\t"
    "v_exp_f32 v215, v215\n\t"
    "v_mfma_f32_16x16x32_bf16 v[32:35], a[240:243], a[104:107], v[32:35]\n\t"
    "v_fma_f32 v216, v216, s55, v4\n\t"
    "v_fma_f32 v217, v217, s55, v4\n\t"
    "v_mfma_f32_16x16x32_bf16 v[32:35], a[244:247], a[108:111], v[32:35]\n\t"
    "v_exp_f32 v216, v216\n\t"
    "v_mfma_f32_16x16x32_bf16 v[36:39], a[240:243], a[128:131], v[36:39]\n\t"
    "v_cvt_pk_bf16_f32 v212, v212, v213\n\t"
    "v_cvt_pk_bf16_f32 v213, v214, v215\n\t"
    "v_mfma_f32_16x16x32_bf16 v[36:39], a[244:247], a[132:135], v[36:39]\n\t"
    "s_waitcnt lgkmcnt(0)\n\t"
    "s_barrier\n\t"
    "ds_read_b128 a[240:243], v0 offset:12288\n\t"
    "ds_read_b128 a[244:247], v0 offset:12800\n\t"
    "v_exp_f32 v217, v217\n\t"
    "v_mfma_f32_16x16x32_bf16 v[16:19], a[248:251], a[16:19], v[16:19]\n\t"
    "v_fma_f32 v218, v218, s55, v4\n\t"
    "v_fma_f32 v219, v219, s55, v4\n\t"
    "v_mfma_f32_16x16x32_bf16 v[16:19], a[252:255], a[20:23], v[16:19]\n\t"
    "buffer_load_dwordx4 v1, s[36:39], s53 offen offset:256 nt lds\n\t"
    "v_exp_f32 v218, v218\n\t"
    "v_mfma_f32_16x16x32_bf16 v[20:23], a[248:251], a[40:43], v[20:23]\n\t"
    "s_mov_b32 s53, s71\n\t"
    "v_exp_f32 v219, v219\n\t"
    "v_mfma_f32_16x16x32_bf16 v[20:23], a[252:255], a[44:47], v[20:23]\n\t"
    "s_add_u32 m0, s52, 49152\n\t"
    "v_fma_f32 v220, v220, s55, v5\n\t"
    "v_fma_f32 v221, v221, s55, v5\n\t"
    "v_mfma_f32_16x16x32_bf16 v[24:27], a[248:251], a[64:67], v[24:27]\n\t"
    "v_exp_f32 v220, v220\n\t"
    "v_mfma_f32_16x16x32_bf16 v[24:27], a[252:255], a[68:71], v[24:27]\n\t"
    "v_cvt_pk_bf16_f32 v216, v216, v217\n\t"
    "v_cvt_pk_bf16_f32 v217, v218, v219\n\t"
    "v_mfma_f32_16x16x32_bf16 v[28:31], a[248:251], a[88:91], v[28:31]\n\t"
    "v_exp_f32 v221, v221\n\t"
    "v_mfma_f32_16x16x32_bf16 v[28:31], a[252:255], a[92:95], v[28:31]\n\t"
    "v_fma_f32 v222, v222, s55, v5\n\t"
    "v_fma_f32 v223, v223, s55, v5\n\t"
    "v_mfma_f32_16x16x32_bf16 v[32:35], a[248:251], a[112:115], v[32:35]\n\t"
    "v_exp_f32 v222, v222\n\t"
    "v_mfma_f32_16x16x32_bf16 v[32:35], a[252:255], a[116:119], v[32:35]\n\t"
    "v_exp_f32 v223, v223\n\t"
    "v_mfma_f32_16x16x32_bf16 v[36:39], a[248:251], a[136:139], v[36:39]\n\t"
    "v_fma_f32 v224, v224, s55, v6\n\t"
    "v_fma_f32 v225, v225, s55, v6\n\t"
    "v_mfma_f32_16x16x32_bf16 v[36:39], a[252:255], a[140:143], v[36:39]\n\t"
    "s_waitcnt lgkmcnt(0)\n\t"
    "ds_read_b128 a[248:251], v0 offset:13312\n\t"
    "ds_read_b128 a[252:255], v0 offset:13824\n\t"
    "v_exp_f32 v224, v224\n\t"
    "v_mfma_f32_16x16x32_bf16 v[232:235], a[240:243], a[0:3], 0\n\t"
    "v_cvt_pk_bf16_f32 v220, v220, v221\n\t"
    "v_cvt_pk_bf16_f32 v221, v222, v223\n\t"
    "v_mfma_f32_16x16x32_bf16 v[232:235], a[244:247], a[4:7], v[232:235]\n\t"
    "v_exp_f32 v225, v225\n\t"
    "v_fma_f32 v226, v226, s55, v6\n\t"
    "v_fma_f32 v227, v227, s55, v6\n\t"
    "v_mfma_f32_16x16x32_bf16 v[236:239], a[240:243], a[24:27], 0\n\t"
    "v_exp_f32 v226, v226\n\t"
    "v_mfma_f32_16x16x32_bf16 v[236:239], a[244:247], a[28:31], v[236:239]\n\t"
    "v_exp_f32 v227, v227\n\t"
    "v_mfma_f32_16x16x32_bf16 v[240:243], a[240:243], a[48:51], 0\n\t"
    "v_fma_f32 v228, v228, s55, v7\n\t"
    "v_fma_f32 v229, v229, s55, v7\n\t"
    "v_mfma_f32_16x16x32_bf16 v[240:243], a[244:247], a[52:55], v[240:243]\n\t"
    "v_exp_f32 v228, v228\n\t"
    "v_mfma_f32_16x16x32_bf16 v[244:247], a[240:243], a[72:75], 0\n\t"
    "v_cvt_pk_bf16_f32 v224, v224, v225\n\t"
    "v_cvt_pk_bf16_f32 v225, v226, v227\n\t"
    "v_mfma_f32_16x16x32_bf16 v[244:247], a[244:247], a[76:79], v[244:247]\n\t"
    "v_exp_f32 v229, v229\n\t"
    "v_mfma_f32_16x16x32_bf16 v[248:251], a[240:243], a[96:99], 0\n\t"
    "v_fma_f32 v230, v230, s55, v7\n\t"
    "v_fma_f32 v231, v231, s55, v7\n\t"
    "v_mfma_f32_16x16x32_bf16 v[248:251], a[244:247], a[100:103], v[248:251]\n\t"
    "buffer_load_dwordx4 v1, s[36:39], s53 offen nt lds\n\t"
    "v_exp_f32 v230, v230\n\t"
    "v_mfma_f32_16x16x32_bf16 v[252:255], a[240:243], a[120:123], 0\n\t"
    "s_addk_i32 m0, 896\n\t"
    "v_exp_f32 v231, v231\n\t"
    "v_mfma_f32_16x16x32_bf16 v[252:255], a[244:247], a[124:127], v[252:255]\n\t"
    "s_waitcnt lgkmcnt(0)\n\t"
    "ds_read_b128 a[240:243], v0 offset:14336\n\t"
    "ds_read_b128 a[244:247], v0 offset:14848\n\t"
    "v_cvt_pk_bf16_f32 v228, v228, v229\n\t"
    "v_cvt_pk_bf16_f32 v229, v230, v231\n\t"
    "v_mfma_f32_16x16x32_bf16 v[232:235], a[248:251], a[8:11], v[232:235]\n\t"
    "v_fma_f32 v16, v16, s55, v2\n\t"
    "v_fma_f32 v17, v17, s55, v2\n\t"
    "v_mfma_f32_16x16x32_bf16 v[232:235], a[252:255], a[12:15], v[232:235]\n\t"
    "v_exp_f32 v16, v16\n\t"
    "v_exp_f32 v17, v17\n\t"
    "v_mfma_f32_16x16x32_bf16 v[236:239], a[248:251], a[32:35], v[236:239]\n\t"
    "v_fma_f32 v18, v18, s55, v2\n\t"
    "v_fma_f32 v19, v19, s55, v2\n\t"
    "v_mfma_f32_16x16x32_bf16 v[236:239], a[252:255], a[36:39], v[236:239]\n\t"
    "v_exp_f32 v18, v18\n\t"
    "v_mfma_f32_16x16x32_bf16 v[240:243], a[248:251], a[56:59], v[240:243]\n\t"
    "v_exp_f32 v19, v19\n\t"
    "v_mfma_f32_16x16x32_bf16 v[240:243], a[252:255], a[60:63], v[240:243]\n\t"
    "v_fma_f32 v20, v20, s55, v3\n\t"
    "v_fma_f32 v21, v21, s55, v3\n\t"
    "v_mfma_f32_16x16x32_bf16 v[244:247], a[248:251], a[80:83], v[244:247]\n\t"
    "v_exp_f32 v20, v20\n\t"
    "v_mfma_f32_16x16x32_bf16 v[244:247], a[252:255], a[84:87], v[244:247]\n\t"
    "v_cvt_pk_bf16_f32 v210, v16, v17\n\t"
    "v_cvt_pk_bf16_f32 v211, v18, v19\n\t"
    "v_mfma_f32_16x16x32_bf16 v[248:251], a[248:251], a[104:107], v[248:251]\n\t"
    "v_exp_f32 v21, v21\n\t"
    "v_mfma_f32_16x16x32_bf16 v[248:251], a[252:255], a[108:111], v[248:251]\n\t"
    "v_fma_f32 v22, v22, s55, v3\n\t"
    "v_fma_f32 v23, v23, s55, v3\n\t"
    "v_exp_f32 v22, v22\n\t"
    "v_mfma_f32_16x16x32_bf16 v[252:255], a[248:251], a[128:131], v[252:255]\n\t"
    "v_exp_f32 v23, v23\n\t"
    "v_mfma_f32_16x16x32_bf16 v[252:255], a[252:255], a[132:135], v[252:255]\n\t"
    "s_waitcnt lgkmcnt(0)\n\t"
    "ds_read_b128 a[248:251], v0 offset:18432\n\t"
    "ds_read_b128 a[252:255], v0 offset:18944\n\t"
    "v_fma_f32 v24, v24, s55, v4\n\t"
    "v_fma_f32 v25, v25, s55, v4\n\t"
    "v_mfma_f32_16x16x32_bf16 v[232:235], a[240:243], a[16:19], v[232:235]\n\t"
    "v_exp_f32 v24, v24\n\t"
    "v_mfma_f32_16x16x32_bf16 v[232:235], a[244:247], a[20:23], v[232:235]\n\t"
    "buffer_load_dwordx4 v1, s[36:39], s53 offen offset:128 nt lds\n\t"
    "v_cvt_pk_bf16_f32 v214, v20, v21\n\t"
    "v_cvt_pk_bf16_f32 v215, v22, v23\n\t"
    "v_mfma_f32_16x16x32_bf16 v[236:239], a[240:243], a[40:43], v[236:239]\n\t"
    "s_addk_i32 m0, 896\n\t"
    "v_exp_f32 v25, v25\n\t"
    "v_mfma_f32_16x16x32_bf16 v[236:239], a[244:247], a[44:47], v[236:239]\n\t"
    "v_fma_f32 v26, v26, s55, v4\n\t"
    "v_fma_f32 v27, v27, s55, v4\n\t"
    "v_exp_f32 v26, v26\n\t"
    "v_mfma_f32_16x16x32_bf16 v[8:11], v[40:43], v[208:211], v[8:11]\n\t"
    "v_exp_f32 v27, v27\n\t"
    "v_mfma_f32_16x16x32_bf16 v[8:11], v[44:47], v[212:215], v[8:11]\n\t"
    "v_fma_f32 v28, v28, s55, v5\n\t"
    "v_fma_f32 v29, v29, s55, v5\n\t"
    "v_mfma_f32_16x16x32_bf16 v[240:243], a[240:243], a[64:67], v[240:243]\n\t"
    "v_exp_f32 v28, v28\n\t"
    "v_mfma_f32_16x16x32_bf16 v[240:243], a[244:247], a[68:71], v[240:243]\n\t"
    "v_cvt_pk_bf16_f32 v218, v24, v25\n\t"
    "v_cvt_pk_bf16_f32 v219, v26, v27\n\t"
    "v_mfma_f32_16x16x32_bf16 v[244:247], a[240:243], a[88:91], v[244:247]\n\t"
    "v_exp_f32 v29, v29\n\t"
    "v_mfma_f32_16x16x32_bf16 v[244:247], a[244:247], a[92:95], v[244:247]\n\t"
    "v_fma_f32 v30, v30, s55, v5\n\t"
    "v_fma_f32 v31, v31, s55, v5\n\t"
    "v_mfma_f32_16x16x32_bf16 v[248:251], a[240:243], a[112:115], v[248:251]\n\t"
    "v_exp_f32 v30, v30\n\t"
    "v_mfma_f32_16x16x32_bf16 v[248:251], a[244:247], a[116:119], v[248:251]\n\t"
    "v_exp_f32 v31, v31\n\t"
    "v_mfma_f32_16x16x32_bf16 v[252:255], a[240:243], a[136:139], v[252:255]\n\t"
    "v_fma_f32 v32, v32, s55, v6\n\t"
    "v_fma_f32 v33, v33, s55, v6\n\t"
    "v_mfma_f32_16x16x32_bf16 v[252:255], a[244:247], a[140:143], v[252:255]\n\t"
    "s_waitcnt lgkmcnt(0)\n\t"
    "ds_read_b128 a[240:243], v0 offset:19456\n\t"
    "ds_read_b128 a[244:247], v0 offset:19968\n\t"
    "v_exp_f32 v32, v32\n\t"
    "v_mfma_f32_16x16x32_bf16 v[16:19], a[248:251], a[0:3], 0\n\t"
    "v_cvt_pk_bf16_f32 v222, v28, v29\n\t"
    "v_cvt_pk_bf16_f32 v223, v30, v31\n\t"
    "v_mfma_f32_16x16x32_bf16 v[16:19], a[252:255], a[4:7], v[16:19]\n\t"
    "buffer_load_dwordx4 v1, s[36:39], s53 offen offset:256 nt lds\n\t"
    "v_exp_f32 v33, v33\n\t"
    "v_fma_f32 v34, v34, s55, v6\n\t"
    "v_fma_f32 v35, v35, s55, v6\n\t"
    "v_mfma_f32_16x16x32_bf16 v[10:13], v[40:43], v[216:219], v[10:13]\n\t"
    "s_mov_b32 s53, 128\n\t"
    "v_exp_f32 v34, v34\n\t"
    "v_mfma_f32_16x16x32_bf16 v[10:13], v[44:47], v[220:223], v[10:13]\n\t"
    "s_add_u32 m0, s52, 12288\n\t"
    "v_exp_f32 v35, v35\n\t"
    "v_mfma_f32_16x16x32_bf16 v[20:23], a[248:251], a[24:27], 0\n\t"
    "v_fma_f32 v36, v36, s55, v7\n\t"
    "v_fma_f32 v37, v37, s55, v7\n\t"
    "v_mfma_f32_16x16x32_bf16 v[20:23], a[252:255], a[28:31], v[20:23]\n\t"
    "v_exp_f32 v36, v36\n\t"
    "v_mfma_f32_16x16x32_bf16 v[24:27], a[248:251], a[48:51], 0\n\t"
    "v_cvt_pk_bf16_f32 v226, v32, v33\n\t"
    "v_cvt_pk_bf16_f32 v227, v34, v35\n\t"
    "v_mfma_f32_16x16x32_bf16 v[24:27], a[252:255], a[52:55], v[24:27]\n\t"
    "v_exp_f32 v37, v37\n\t"
    "v_mfma_f32_16x16x32_bf16 v[28:31], a[248:251], a[72:75], 0\n\t"
    "v_fma_f32 v38, v38, s55, v7\n\t"
    "v_fma_f32 v39, v39, s55, v7\n\t"
    "v_mfma_f32_16x16x32_bf16 v[28:31], a[252:255], a[76:79], v[28:31]\n\t"
    "v_exp_f32 v38, v38\n\t"
    "v_mfma_f32_16x16x32_bf16 v[32:35], a[248:251], a[96:99], 0\n\t"
    "v_exp_f32 v39, v39\n\t"
    "v_mfma_f32_16x16x32_bf16 v[32:35], a[252:255], a[100:103], v[32:35]\n\t"
    "v_cvt_pk_bf16_f32 v230, v36, v37\n\t"
    "v_cvt_pk_bf16_f32 v231, v38, v39\n\t"
    "v_mfma_f32_16x16x32_bf16 v[36:39], a[248:251], a[120:123], 0\n\t"
    "v_fma_f32 v232, v232, s55, v2\n\t"
    "v_fma_f32 v233, v233, s55, v2\n\t"
    "v_mfma_f32_16x16x32_bf16 v[36:39], a[252:255], a[124:127], v[36:39]\n\t"
    "s_waitcnt lgkmcnt(0)\n\t"
    "ds_read_b128 a[248:251], v0 offset:20480\n\t"
    "ds_read_b128 a[252:255], v0 offset:20992\n\t"
    "v_mfma_f32_16x16x32_bf16 v[12:15], v[40:43], v[224:227], v[12:15]\n\t"
    "v_exp_f32 v232, v232\n\t"
    "v_mfma_f32_16x16x32_bf16 v[12:15], v[44:47], v[228:231], v[12:15]\n\t"
    "v_exp_f32 v233, v233\n\t"
    "v_mfma_f32_16x16x32_bf16 v[16:19], a[240:243], a[8:11], v[16:19]\n\t"
    "v_fma_f32 v234, v234, s55, v2\n\t"
    "v_fma_f32 v235, v235, s55, v2\n\t"
    "v_mfma_f32_16x16x32_bf16 v[16:19], a[244:247], a[12:15], v[16:19]\n\t"
    "v_exp_f32 v234, v234\n\t"
    "v_mfma_f32_16x16x32_bf16 v[20:23], a[240:243], a[32:35], v[20:23]\n\t"
    "v_exp_f32 v235, v235\n\t"
    "v_mfma_f32_16x16x32_bf16 v[20:23], a[244:247], a[36:39], v[20:23]\n\t"
    "v_fma_f32 v236, v236, s55, v3\n\t"
    "v_fma_f32 v237, v237, s55, v3\n\t"
    "v_mfma_f32_16x16x32_bf16 v[24:27], a[240:243], a[56:59], v[24:27]\n\t"
    "v_exp_f32 v236, v236\n\t"
    "v_mfma_f32_16x16x32_bf16 v[24:27], a[244:247], a[60:63], v[24:27]\n\t"
    "v_cvt_pk_bf16_f32 v232, v232, v233\n\t"
    "v_cvt_pk_bf16_f32 v233, v234, v235\n\t"
    "v_mfma_f32_16x16x32_bf16 v[28:31], a[240:243], a[80:83], v[28:31]\n\t"
    "v_exp_f32 v237, v237\n\t"
    "v_mfma_f32_16x16x32_bf16 v[28:31], a[244:247], a[84:87], v[28:31]\n\t"
    "v_fma_f32 v238, v238, s55, v3\n\t"
    "v_fma_f32 v239, v239, s55, v3\n\t"
    "v_mfma_f32_16x16x32_bf16 v[32:35], a[240:243], a[104:107], v[32:35]\n\t"
    "v_exp_f32 v238, v238\n\t"
    "v_mfma_f32_16x16x32_bf16 v[32:35], a[244:247], a[108:111], v[32:35]\n\t"
    "v_exp_f32 v239, v239\n\t"
    "v_mfma_f32_16x16x32_bf16 v[36:39], a[240:243], a[128:131], v[36:39]\n\t"
    "v_fma_f32 v240, v240, s55, v4\n\t"
    "v_fma_f32 v241, v241, s55, v4\n\t"
    "v_mfma_f32_16x16x32_bf16 v[36:39], a[244:247], a[132:135], v[36:39]\n\t"
    "s_waitcnt vmcnt(21) lgkmcnt(0)\n\t"
    "s_barrier\n\t"
    "v_mov_b32 v61, v0\n\t"
    "v_mov_b32 v0, v59\n\t"
    "v_add_u32 v0, 0x6000, v0\n\t"
    "v_xor_b32 v0, 32, v0\n\t"
    "ds_read_b64_tr_b16 a[240:241], v0 offset:0\n\t"
    "v_mfma_f32_16x16x32_bf16 v[16:19], a[248:251], a[16:19], v[16:19]\n\t"
    "ds_read_b64_tr_b16 a[242:243], v0 offset:6144\n\t"
    "v_add_u32 v60, s62, v61\n\t"
    "v_mfma_f32_16x16x32_bf16 v[16:19], a[252:255], a[20:23], v[16:19]\n\t"
    "ds_read_b64_tr_b16 a[244:245], v0 offset:1024\n\t"
    "ds_read_b128 a[48:51], v60 offset:0\n\t"
    "v_exp_f32 v240, v240\n\t"
    "v_mfma_f32_16x16x32_bf16 v[20:23], a[248:251], a[40:43], v[20:23]\n\t"
    "ds_read_b64_tr_b16 a[246:247], v0 offset:7168\n\t"
    "ds_read_b128 a[72:75], v60 offset:6144\n\t"
    "v_mfma_f32_16x16x32_bf16 v[20:23], a[252:255], a[44:47], v[20:23]\n\t"
    "buffer_load_dwordx4 v1, s[44:47], s53 offen nt lds\n\t"
    "v_cvt_pk_bf16_f32 v236, v236, v237\n\t"
    "v_cvt_pk_bf16_f32 v237, v238, v239\n\t"
    "v_exp_f32 v241, v241\n\t"
    "v_mfma_f32_16x16x32_bf16 v[24:27], a[248:251], a[64:67], v[24:27]\n\t"
    "s_addk_i32 s53, 3072\n\t"
    "ds_read_b128 a[52:55], v60 offset:512\n\t"
    "v_mfma_f32_16x16x32_bf16 v[24:27], a[252:255], a[68:71], v[24:27]\n\t"
    "s_addk_i32 m0, 1024\n\t"
    "v_fma_f32 v242, v242, s55, v4\n\t"
    "v_fma_f32 v243, v243, s55, v4\n\t"
    "v_mfma_f32_16x16x32_bf16 v[28:31], a[248:251], a[88:91], v[28:31]\n\t"
    "ds_read_b128 a[76:79], v60 offset:6656\n\t"
    "v_mfma_f32_16x16x32_bf16 v[28:31], a[252:255], a[92:95], v[28:31]\n\t"
    "v_exp_f32 v242, v242\n\t"
    "v_exp_f32 v243, v243\n\t"
    "v_mfma_f32_16x16x32_bf16 v[32:35], a[248:251], a[112:115], v[32:35]\n\t"
    "ds_read_b128 a[56:59], v60 offset:1024\n\t"
    "v_mfma_f32_16x16x32_bf16 v[32:35], a[252:255], a[116:119], v[32:35]\n\t"
    "v_fma_f32 v244, v244, s55, v5\n\t"
    "v_fma_f32 v245, v245, s55, v5\n\t"
    "v_exp_f32 v244, v244\n\t"
    "v_mfma_f32_16x16x32_bf16 v[36:39], a[248:251], a[136:139], v[36:39]\n\t"
    "ds_read_b128 a[80:83], v60 offset:7168\n\t"
    "v_mfma_f32_16x16x32_bf16 v[36:39], a[252:255], a[140:143], v[36:39]\n\t"
    "s_waitcnt lgkmcnt(5)\n\t"
    "ds_read_b64_tr_b16 a[248:249], v0 offset:2048\n\t"
    "v_cvt_pk_bf16_f32 v240, v240, v241\n\t"
    "v_cvt_pk_bf16_f32 v241, v242, v243\n\t"
    "v_mfma_f32_16x16x32_bf16 v[48:51], a[240:243], v[64:67], 0\n\t"
    "ds_read_b64_tr_b16 a[250:251], v0 offset:8192\n\t"
    "ds_read_b128 a[60:63], v60 offset:1536\n\t"
    "v_mfma_f32_16x16x32_bf16 v[48:51], a[244:247], a[144:147], v[48:51]\n\t"
    "ds_read_b64_tr_b16 a[252:253], v0 offset:49152\n\t"
    "v_exp_f32 v245, v245\n\t"
    "v_fma_f32 v246, v246, s55, v5\n\t"
    "v_fma_f32 v247, v247, s55, v5\n\t"
    "v_mfma_f32_16x16x32_bf16 v[52:55], a[240:243], v[68:71], 0\n\t"
    "ds_read_b64_tr_b16 a[254:255], v0 offset:55296\n\t"
    "ds_read_b128 a[84:87], v60 offset:7680\n\t"
    "v_mfma_f32_16x16x32_bf16 v[52:55], a[244:247], a[148:151], v[52:55]\n\t"
    "buffer_load_dwordx4 v1, s[44:47], s53 offen nt lds\n\t"
    "v_exp_f32 v246, v246\n\t"
    "v_exp_f32 v247, v247\n\t"
    "v_mfma_f32_16x16x32_bf16 v[56:59], a[240:243], v[72:75], 0\n\t"
    "s_addk_i32 s53, 3072\n\t"
    "ds_read_b128 a[64:67], v60 offset:2048\n\t"
    "v_mfma_f32_16x16x32_bf16 v[56:59], a[244:247], a[152:155], v[56:59]\n\t"
    "s_addk_i32 m0, 1024\n\t"
    "v_fma_f32 v248, v248, s55, v6\n\t"
    "v_fma_f32 v249, v249, s55, v6\n\t"
    "v_mfma_f32_16x16x32_bf16 a[120:123], a[240:243], v[76:79], 0\n\t"
    "ds_read_b128 a[88:91], v60 offset:8192\n\t"
    "v_mfma_f32_16x16x32_bf16 a[120:123], a[244:247], a[156:159], a[120:123]\n\t"
    "v_exp_f32 v248, v248\n\t"
    "v_cvt_pk_bf16_f32 v244, v244, v245\n\t"
    "v_cvt_pk_bf16_f32 v245, v246, v247\n\t"
    "v_mfma_f32_16x16x32_bf16 a[124:127], a[240:243], v[80:83], 0\n\t"
    "ds_read_b128 a[68:71], v60 offset:2560\n\t"
    "v_mfma_f32_16x16x32_bf16 a[124:127], a[244:247], a[160:163], a[124:127]\n\t"
    "v_exp_f32 v249, v249\n\t"
    "v_fma_f32 v250, v250, s55, v6\n\t"
    "v_fma_f32 v251, v251, s55, v6\n\t"
    "v_mfma_f32_16x16x32_bf16 a[128:131], a[240:243], v[84:87], 0\n\t"
    "ds_read_b128 a[92:95], v60 offset:8704\n\t"
    "v_mfma_f32_16x16x32_bf16 a[128:131], a[244:247], a[164:167], a[128:131]\n\t"
    "s_waitcnt lgkmcnt(5)\n\t"
    "ds_read_b64_tr_b16 a[240:241], v0 offset:50176\n\t"
    "v_exp_f32 v250, v250\n\t"
    "v_exp_f32 v251, v251\n\t"
    "v_mfma_f32_16x16x32_bf16 v[48:51], a[248:251], v[88:91], v[48:51]\n\t"
    "ds_read_b64_tr_b16 a[242:243], v0 offset:56320\n\t"
    "v_mfma_f32_16x16x32_bf16 v[48:51], a[252:255], a[168:171], v[48:51]\n\t"
    "ds_read_b64_tr_b16 a[244:245], v0 offset:51200\n\t"
    "v_fma_f32 v252, v252, s55, v7\n\t"
    "v_fma_f32 v253, v253, s55, v7\n\t"
    "v_exp_f32 v252, v252\n\t"
    "v_mfma_f32_16x16x32_bf16 v[52:55], a[248:251], v[92:95], v[52:55]\n\t"
    "ds_read_b64_tr_b16 a[246:247], v0 offset:57344\n\t"
    "v_mfma_f32_16x16x32_bf16 v[52:55], a[252:255], a[172:175], v[52:55]\n\t"
    "buffer_load_dwordx4 v1, s[44:47], s53 offen nt lds\n\t"
    "v_cvt_pk_bf16_f32 v248, v248, v249\n\t"
    "v_cvt_pk_bf16_f32 v249, v250, v251\n\t"
    "v_exp_f32 v253, v253\n\t"
    "v_mfma_f32_16x16x32_bf16 v[56:59], a[248:251], v[96:99], v[56:59]\n\t"
    "s_mov_b32 s53, 9344\n\t"
    "v_fma_f32 v254, v254, s55, v7\n\t"
    "v_fma_f32 v255, v255, s55, v7\n\t"
    "v_mfma_f32_16x16x32_bf16 v[56:59], a[252:255], a[176:179], v[56:59]\n\t"
    "s_add_u32 m0, s52, 24576\n\t"
    "v_exp_f32 v254, v254\n\t"
    "v_mfma_f32_16x16x32_bf16 a[120:123], a[248:251], v[100:103], a[120:123]\n\t"
    "v_exp_f32 v255, v255\n\t"
    "v_mfma_f32_16x16x32_bf16 a[120:123], a[252:255], a[180:183], a[120:123]\n\t"
    "v_cvt_pk_bf16_f32 v252, v252, v253\n\t"
    "v_cvt_pk_bf16_f32 v253, v254, v255\n\t"
    "v_mfma_f32_16x16x32_bf16 a[124:127], a[248:251], v[104:107], a[124:127]\n\t"
    "v_fma_f32 v16, v16, s55, v2\n\t"
    "v_fma_f32 v17, v17, s55, v2\n\t"
    "v_mfma_f32_16x16x32_bf16 a[124:127], a[252:255], a[184:187], a[124:127]\n\t"
    "v_exp_f32 v16, v16\n\t"
    "v_mfma_f32_16x16x32_bf16 a[128:131], a[248:251], v[108:111], a[128:131]\n\t"
    "v_exp_f32 v17, v17\n\t"
    "v_mfma_f32_16x16x32_bf16 a[128:131], a[252:255], a[188:191], a[128:131]\n\t"
    "s_waitcnt vmcnt(16) lgkmcnt(0)\n\t"
    "s_barrier\n\t"
    "ds_read_b64_tr_b16 a[248:249], v0 offset:12288\n\t"
    "v_mfma_f32_16x16x32_bf16 v[48:51], a[240:243], v[112:115], v[48:51]\n\t"
    "ds_read_b64_tr_b16 a[250:251], v0 offset:18432\n\t"
    "v_mfma_f32_16x16x32_bf16 v[48:51], a[244:247], a[192:195], v[48:51]\n\t"
    "ds_read_b64_tr_b16 a[252:253], v0 offset:13312\n\t"
    "v_fma_f32 v18, v18, s55, v2\n\t"
    "v_fma_f32 v19, v19, s55, v2\n\t"
    "v_mfma_f32_16x16x32_bf16 v[52:55], a[240:243], v[116:119], v[52:55]\n\t"
    "ds_read_b64_tr_b16 a[254:255], v0 offset:19456\n\t"
    "v_mfma_f32_16x16x32_bf16 v[52:55], a[244:247], a[196:199], v[52:55]\n\t"
    "buffer_load_dwordx4 v1, s[44:47], s53 offen nt lds\n\t"
    "v_exp_f32 v18, v18\n\t"
    "v_exp_f32 v19, v19\n\t"
    "v_mfma_f32_16x16x32_bf16 v[56:59], a[240:243], v[120:123], v[56:59]\n\t"
    "s_addk_i32 s53, 3072\n\t"
    "v_fma_f32 v20, v20, s55, v3\n\t"
    "v_fma_f32 v21, v21, s55, v3\n\t"
    "v_mfma_f32_16x16x32_bf16 v[56:59], a[244:247], a[200:203], v[56:59]\n\t"
    "s_addk_i32 m0, 1024\n\t"
    "v_exp_f32 v20, v20\n\t"
    "v_mfma_f32_16x16x32_bf16 a[120:123], a[240:243], v[124:127], a[120:123]\n\t"
    "v_cvt_pk_bf16_f32 v234, v16, v17\n\t"
    "v_cvt_pk_bf16_f32 v235, v18, v19\n\t"
    "v_mfma_f32_16x16x32_bf16 a[120:123], a[244:247], a[204:207], a[120:123]\n\t"
    "v_exp_f32 v21, v21\n\t"
    "v_mfma_f32_16x16x32_bf16 a[124:127], a[240:243], v[128:131], a[124:127]\n\t"
    "v_fma_f32 v22, v22, s55, v3\n\t"
    "v_fma_f32 v23, v23, s55, v3\n\t"
    "v_mfma_f32_16x16x32_bf16 a[124:127], a[244:247], a[208:211], a[124:127]\n\t"
    "v_exp_f32 v22, v22\n\t"
    "v_mfma_f32_16x16x32_bf16 a[128:131], a[240:243], v[132:135], a[128:131]\n\t"
    "v_exp_f32 v23, v23\n\t"
    "v_mfma_f32_16x16x32_bf16 a[128:131], a[244:247], a[212:215], a[128:131]\n\t"
    "s_waitcnt lgkmcnt(0)\n\t"
    "ds_read_b64_tr_b16 a[240:241], v0 offset:14336\n\t"
    "v_mfma_f32_16x16x32_bf16 v[48:51], a[248:251], v[136:139], v[48:51]\n\t"
    "ds_read_b64_tr_b16 a[242:243], v0 offset:20480\n\t"
    "v_mfma_f32_16x16x32_bf16 v[48:51], a[252:255], a[216:219], v[48:51]\n\t"
    "ds_read_b64_tr_b16 a[244:245], v0 offset:36864\n\t"
    "v_fma_f32 v24, v24, s55, v4\n\t"
    "v_fma_f32 v25, v25, s55, v4\n\t"
    "v_mfma_f32_16x16x32_bf16 v[52:55], a[248:251], v[140:143], v[52:55]\n\t"
    "ds_read_b64_tr_b16 a[246:247], v0 offset:43008\n\t"
    "v_mfma_f32_16x16x32_bf16 v[52:55], a[252:255], a[220:223], v[52:55]\n\t"
    "v_exp_f32 v24, v24\n\t"
    "v_cvt_pk_bf16_f32 v238, v20, v21\n\t"
    "v_cvt_pk_bf16_f32 v239, v22, v23\n\t"
    "v_mfma_f32_16x16x32_bf16 v[56:59], a[248:251], v[144:147], v[56:59]\n\t"
    "v_exp_f32 v25, v25\n\t"
    "v_mfma_f32_16x16x32_bf16 v[56:59], a[252:255], a[224:227], v[56:59]\n\t"
    "v_fma_f32 v26, v26, s55, v4\n\t"
    "v_fma_f32 v27, v27, s55, v4\n\t"
    "v_mfma_f32_16x16x32_bf16 v[8:11], v[40:43], v[232:235], v[8:11]\n\t"
    "v_exp_f32 v26, v26\n\t"
    "v_mfma_f32_16x16x32_bf16 v[8:11], v[44:47], v[236:239], v[8:11]\n\t"
    "v_exp_f32 v27, v27\n\t"
    "v_mfma_f32_16x16x32_bf16 a[120:123], a[248:251], v[148:151], a[120:123]\n\t"
    "v_fma_f32 v28, v28, s55, v5\n\t"
    "v_fma_f32 v29, v29, s55, v5\n\t"
    "v_mfma_f32_16x16x32_bf16 a[120:123], a[252:255], a[228:231], a[120:123]\n\t"
    "v_exp_f32 v28, v28\n\t"
    "v_mfma_f32_16x16x32_bf16 a[124:127], a[248:251], v[152:155], a[124:127]\n\t"
    "v_cvt_pk_bf16_f32 v242, v24, v25\n\t"
    "v_cvt_pk_bf16_f32 v243, v26, v27\n\t"
    "v_mfma_f32_16x16x32_bf16 a[124:127], a[252:255], a[232:235], a[124:127]\n\t"
    "v_exp_f32 v29, v29\n\t"
    "v_mfma_f32_16x16x32_bf16 a[128:131], a[248:251], v[156:159], a[128:131]\n\t"
    "v_fma_f32 v30, v30, s55, v5\n\t"
    "v_fma_f32 v31, v31, s55, v5\n\t"
    "v_mfma_f32_16x16x32_bf16 a[128:131], a[252:255], a[236:239], a[128:131]\n\t"
    "s_waitcnt lgkmcnt(0)\n\t"
    "ds_read_b64_tr_b16 a[248:249], v0 offset:37888\n\t"
    "v_mfma_f32_16x16x32_bf16 v[48:51], a[240:243], v[160:163], v[48:51]\n\t"
    "ds_read_b64_tr_b16 a[250:251], v0 offset:44032\n\t"
    "v_mfma_f32_16x16x32_bf16 v[48:51], a[244:247], v[184:187], v[48:51]\n\t"
    "ds_read_b64_tr_b16 a[252:253], v0 offset:38912\n\t"
    "v_exp_f32 v30, v30\n\t"
    "v_mfma_f32_16x16x32_bf16 v[52:55], a[240:243], v[164:167], v[52:55]\n\t"
    "ds_read_b64_tr_b16 a[254:255], v0 offset:45056\n\t"
    "v_mfma_f32_16x16x32_bf16 v[52:55], a[244:247], v[188:191], v[52:55]\n\t"
    "buffer_load_dwordx4 v1, s[44:47], s53 offen nt lds\n\t"
    "v_exp_f32 v31, v31\n\t"
    "v_mfma_f32_16x16x32_bf16 v[56:59], a[240:243], v[168:171], v[56:59]\n\t"
    "s_addk_i32 s53, 3072\n\t"
    "v_fma_f32 v32, v32, s55, v6\n\t"
    "v_fma_f32 v33, v33, s55, v6\n\t"
    "v_mfma_f32_16x16x32_bf16 v[56:59], a[244:247], v[192:195], v[56:59]\n\t"
    "s_addk_i32 m0, 1024\n\t"
    "v_exp_f32 v32, v32\n\t"
    "v_mfma_f32_16x16x32_bf16 a[120:123], a[240:243], v[172:175], a[120:123]\n\t"
    "v_cvt_pk_bf16_f32 v246, v28, v29\n\t"
    "v_cvt_pk_bf16_f32 v247, v30, v31\n\t"
    "v_mfma_f32_16x16x32_bf16 a[120:123], a[244:247], v[196:199], a[120:123]\n\t"
    "v_exp_f32 v33, v33\n\t"
    "v_mfma_f32_16x16x32_bf16 v[10:13], v[40:43], v[240:243], v[10:13]\n\t"
    "v_fma_f32 v34, v34, s55, v6\n\t"
    "v_fma_f32 v35, v35, s55, v6\n\t"
    "v_mfma_f32_16x16x32_bf16 v[10:13], v[44:47], v[244:247], v[10:13]\n\t"
    "v_exp_f32 v34, v34\n\t"
    "v_mfma_f32_16x16x32_bf16 a[124:127], a[240:243], v[176:179], a[124:127]\n\t"
    "v_exp_f32 v35, v35\n\t"
    "v_mfma_f32_16x16x32_bf16 a[124:127], a[244:247], v[200:203], a[124:127]\n\t"
    "v_fma_f32 v36, v36, s55, v7\n\t"
    "v_fma_f32 v37, v37, s55, v7\n\t"
    "v_mfma_f32_16x16x32_bf16 a[128:131], a[240:243], v[180:183], a[128:131]\n\t"
    "v_exp_f32 v36, v36\n\t"
    "v_mfma_f32_16x16x32_bf16 a[128:131], a[244:247], v[204:207], a[128:131]\n\t"
    "s_waitcnt vmcnt(5) lgkmcnt(0)\n\t"
    "s_barrier\n\t"
    "v_xor_b32 v0, 32, v0\n\t"
    "ds_read_b64_tr_b16 a[240:241], v0 offset:0\n\t"
    "v_mfma_f32_16x16x32_bf16 v[48:51], a[248:251], v[208:211], v[48:51]\n\t"
    "ds_read_b64_tr_b16 a[242:243], v0 offset:6144\n\t"
    "v_add_u32 v60, s61, v61\n\t"
    "v_mfma_f32_16x16x32_bf16 v[48:51], a[252:255], v[232:235], v[48:51]\n\t"
    "ds_read_b64_tr_b16 a[244:245], v0 offset:1024\n\t"
    "ds_read_b128 a[0:3], v60 offset:0\n\t"
    "v_mfma_f32_16x16x32_bf16 v[52:55], a[248:251], v[212:215], v[52:55]\n\t"
    "ds_read_b64_tr_b16 a[246:247], v0 offset:7168\n\t"
    "ds_read_b128 a[24:27], v60 offset:6144\n\t"
    "v_mfma_f32_16x16x32_bf16 v[52:55], a[252:255], v[236:239], v[52:55]\n\t"
    "v_cvt_pk_bf16_f32 v250, v32, v33\n\t"
    "v_cvt_pk_bf16_f32 v251, v34, v35\n\t"
    "v_exp_f32 v37, v37\n\t"
    "v_mfma_f32_16x16x32_bf16 v[56:59], a[248:251], v[216:219], v[56:59]\n\t"
    "ds_read_b128 a[4:7], v60 offset:512\n\t"
    "v_mfma_f32_16x16x32_bf16 v[56:59], a[252:255], v[240:243], v[56:59]\n\t"
    "v_fma_f32 v38, v38, s55, v7\n\t"
    "v_fma_f32 v39, v39, s55, v7\n\t"
    "v_exp_f32 v38, v38\n\t"
    "v_mfma_f32_16x16x32_bf16 a[120:123], a[248:251], v[220:223], a[120:123]\n\t"
    "ds_read_b128 a[28:31], v60 offset:6656\n\t"
    "v_mfma_f32_16x16x32_bf16 a[120:123], a[252:255], v[244:247], a[120:123]\n\t"
    "buffer_load_dwordx4 v1, s[44:47], s53 offen nt lds\n\t"
    "v_exp_f32 v39, v39\n\t"
    "v_cvt_pk_bf16_f32 v254, v36, v37\n\t"
    "v_cvt_pk_bf16_f32 v255, v38, v39\n\t"
    "v_mfma_f32_16x16x32_bf16 a[124:127], a[248:251], v[224:227], a[124:127]\n\t"
    "s_mov_b32 s53, 18560\n\t"
    "ds_read_b128 a[8:11], v60 offset:1024\n\t"
    "v_mfma_f32_16x16x32_bf16 a[124:127], a[252:255], v[248:251], a[124:127]\n\t"
    "s_add_u32 m0, s52, 49152\n\t"
    "v_mfma_f32_16x16x32_bf16 v[12:15], v[40:43], v[248:251], v[12:15]\n\t"
    "v_mfma_f32_16x16x32_bf16 v[12:15], v[44:47], v[252:255], v[12:15]\n\t"
    "v_add_f32 v4, v8, v9\n\t"
    "v_add_f32 v4, v4, v10\n\t"
    "v_add_f32 v4, v4, v11\n\t"
    "v_mfma_f32_16x16x32_bf16 a[128:131], a[248:251], v[228:231], a[128:131]\n\t"
    "ds_read_b128 a[32:35], v60 offset:7168\n\t"
    "v_mfma_f32_16x16x32_bf16 a[128:131], a[252:255], v[252:255], a[128:131]\n\t"
    "s_waitcnt lgkmcnt(5)\n\t"
    "ds_read_b64_tr_b16 a[248:249], v0 offset:2048\n\t"
    "v_add_f32 v4, v4, v12\n\t"
    "v_add_f32 v4, v4, v13\n\t"
    "v_cmp_nlt_f32_e64 s[66:67], v4, s57\n\t"
    "v_rcp_f32 v8, v8\n\t"
    "v_mfma_f32_16x16x32_bf16 v[16:19], a[240:243], v[64:67], 0\n\t"
    "ds_read_b64_tr_b16 a[250:251], v0 offset:8192\n\t"
    "ds_read_b128 a[12:15], v60 offset:1536\n\t"
    "v_mfma_f32_16x16x32_bf16 v[16:19], a[244:247], a[144:147], v[16:19]\n\t"
    "ds_read_b64_tr_b16 a[252:253], v0 offset:49152\n\t"
    "v_rcp_f32 v9, v9\n\t"
    "v_mfma_f32_16x16x32_bf16 v[20:23], a[240:243], v[68:71], 0\n\t"
    "ds_read_b64_tr_b16 a[254:255], v0 offset:55296\n\t"
    "ds_read_b128 a[36:39], v60 offset:7680\n\t"
    "v_mfma_f32_16x16x32_bf16 v[20:23], a[244:247], a[148:151], v[20:23]\n\t"
    "v_rcp_f32 v10, v10\n\t"
    "v_mfma_f32_16x16x32_bf16 v[24:27], a[240:243], v[72:75], 0\n\t"
    "ds_read_b128 a[16:19], v60 offset:2048\n\t"
    "v_mfma_f32_16x16x32_bf16 v[24:27], a[244:247], a[152:155], v[24:27]\n\t"
    "v_rcp_f32 v11, v11\n\t"
    "v_mfma_f32_16x16x32_bf16 v[28:31], a[240:243], v[76:79], 0\n\t"
    "ds_read_b128 a[40:43], v60 offset:8192\n\t"
    "v_mfma_f32_16x16x32_bf16 v[28:31], a[244:247], a[156:159], v[28:31]\n\t"
    "v_rcp_f32 v12, v12\n\t"
    "v_rcp_f32 v13, v13\n\t"
    "v_mfma_f32_16x16x32_bf16 v[32:35], a[240:243], v[80:83], 0\n\t"
    "ds_read_b128 a[20:23], v60 offset:2560\n\t"
    "v_and_b32 v14, 0xffff, v62\n\t"
    "v_mfma_f32_16x16x32_bf16 v[32:35], a[244:247], a[160:163], v[32:35]\n\t"
    "v_lshrrev_b32 v15, 16, v62\n\t"
    "v_add_u32 v14, s64, v14\n\t"
    "v_mfma_f32_16x16x32_bf16 v[36:39], a[240:243], v[84:87], 0\n\t"
    "ds_read_b128 a[44:47], v60 offset:8704\n\t"
    "v_add_u32 v15, s64, v15\n\t"
    "v_mfma_f32_16x16x32_bf16 v[36:39], a[244:247], a[164:167], v[36:39]\n\t"
    "s_waitcnt lgkmcnt(5)\n\t"
    "ds_read_b64_tr_b16 a[240:241], v0 offset:50176\n\t"
    "v_mov_b32 v3, v63\n\t"
    "v_mfma_f32_16x16x32_bf16 v[16:19], a[248:251], v[88:91], v[16:19]\n\t"
    "ds_read_b64_tr_b16 a[242:243], v0 offset:56320\n\t"
    "v_mov_b32 v5, v62\n\t"
    "v_mfma_f32_16x16x32_bf16 v[16:19], a[252:255], a[168:171], v[16:19]\n\t"
    "ds_read_b64_tr_b16 a[244:245], v0 offset:51200\n\t"
    "v_mov_b32 v2, v61\n\t"
    "v_mfma_f32_16x16x32_bf16 v[20:23], a[248:251], v[92:95], v[20:23]\n\t"
    "ds_read_b64_tr_b16 a[246:247], v0 offset:57344\n\t"
    "v_mfma_f32_16x16x32_bf16 v[20:23], a[252:255], a[172:175], v[20:23]\n\t"
    "v_mfma_f32_16x16x32_bf16 v[24:27], a[248:251], v[96:99], v[24:27]\n\t"
    "v_mfma_f32_16x16x32_bf16 v[24:27], a[252:255], a[176:179], v[24:27]\n\t"
    "s_cmp_lg_u64 s[66:67], 0\n\t"
    "s_cselect_b32 s72, s59, 0\n\t"
    "s_or_b32 s60, s60, s72\n\t"
    "s_lshl_b32 s59, s59, 1\n\t"
    "v_mul_f32 v48, v48, v8\n\t"
    "v_mfma_f32_16x16x32_bf16 v[28:31], a[248:251], v[100:103], v[28:31]\n\t"
    "v_mul_f32 v49, v49, v8\n\t"
    "v_mul_f32 v50, v50, v8\n\t"
    "v_mfma_f32_16x16x32_bf16 v[28:31], a[252:255], a[180:183], v[28:31]\n\t"
    "v_mul_f32 v51, v51, v8\n\t"
    "v_cvt_pk_bf16_f32 v48, v48, v49\n\t"
    "v_mfma_f32_16x16x32_bf16 v[32:35], a[248:251], v[104:107], v[32:35]\n\t"
    "v_cvt_pk_bf16_f32 v49, v50, v51\n\t"
    "v_mfma_f32_16x16x32_bf16 v[32:35], a[252:255], a[184:187], v[32:35]\n\t"
    "ds_write_b64 v14, v[48:49] offset:2304\n\t"
    "v_mfma_f32_16x16x32_bf16 v[36:39], a[248:251], v[108:111], v[36:39]\n\t"
    "v_mul_f32 v52, v52, v9\n\t"
    "v_mul_f32 v53, v53, v9\n\t"
    "v_mfma_f32_16x16x32_bf16 v[36:39], a[252:255], a[188:191], v[36:39]\n\t"
    "s_waitcnt lgkmcnt(1)\n\t"
    "s_barrier\n\t"
    "ds_read_b64_tr_b16 a[248:249], v0 offset:12288\n\t"
    "v_mfma_f32_16x16x32_bf16 v[16:19], a[240:243], v[112:115], v[16:19]\n\t"
    "ds_read_b64_tr_b16 a[250:251], v0 offset:18432\n\t"
    "v_mul_f32 v54, v54, v9\n\t"
    "v_mfma_f32_16x16x32_bf16 v[16:19], a[244:247], a[192:195], v[16:19]\n\t"
    "ds_read_b64_tr_b16 a[252:253], v0 offset:13312\n\t"
    "v_mul_f32 v55, v55, v9\n\t"
    "v_mfma_f32_16x16x32_bf16 v[20:23], a[240:243], v[116:119], v[20:23]\n\t"
    "ds_read_b64_tr_b16 a[254:255], v0 offset:19456\n\t"
    "v_cvt_pk_bf16_f32 v52, v52, v53\n\t"
    "v_mfma_f32_16x16x32_bf16 v[20:23], a[244:247], a[196:199], v[20:23]\n\t"
    "buffer_load_dwordx4 v1, s[44:47], s53 offen nt lds\n\t"
    "v_mfma_f32_16x16x32_bf16 v[24:27], a[240:243], v[120:123], v[24:27]\n\t"
    "s_addk_i32 s53, 3072\n\t"
    "v_cvt_pk_bf16_f32 v53, v54, v55\n\t"
    "v_mfma_f32_16x16x32_bf16 v[24:27], a[244:247], a[200:203], v[24:27]\n\t"
    "s_addk_i32 m0, 1024\n\t"
    "ds_write_b64 v14, v[52:53] offset:2560\n\t"
    "v_mfma_f32_16x16x32_bf16 v[28:31], a[240:243], v[124:127], v[28:31]\n\t"
    "v_mul_f32 v56, v56, v10\n\t"
    "v_mul_f32 v57, v57, v10\n\t"
    "v_mfma_f32_16x16x32_bf16 v[28:31], a[244:247], a[204:207], v[28:31]\n\t"
    "v_mul_f32 v58, v58, v10\n\t"
    "v_mul_f32 v59, v59, v10\n\t"
    "v_mfma_f32_16x16x32_bf16 v[32:35], a[240:243], v[128:131], v[32:35]\n\t"
    "v_cvt_pk_bf16_f32 v56, v56, v57\n\t"
    "v_cvt_pk_bf16_f32 v57, v58, v59\n\t"
    "v_mfma_f32_16x16x32_bf16 v[32:35], a[244:247], a[208:211], v[32:35]\n\t"
    "ds_write_b64 v14, v[56:57] offset:4608\n\t"
    "v_mfma_f32_16x16x32_bf16 v[36:39], a[240:243], v[132:135], v[36:39]\n\t"
    "v_accvgpr_read_b32 v6, a120\n\t"
    "v_accvgpr_read_b32 v4, a121\n\t"
    "v_mfma_f32_16x16x32_bf16 v[36:39], a[244:247], a[212:215], v[36:39]\n\t"
    "s_waitcnt lgkmcnt(2)\n\t"
    "ds_read_b64_tr_b16 a[240:241], v0 offset:14336\n\t"
    "v_mul_f32 v6, v6, v11\n\t"
    "v_mfma_f32_16x16x32_bf16 v[16:19], a[248:251], v[136:139], v[16:19]\n\t"
    "ds_read_b64_tr_b16 a[242:243], v0 offset:20480\n\t"
    "v_mul_f32 v4, v4, v11\n\t"
    "v_mfma_f32_16x16x32_bf16 v[16:19], a[252:255], a[216:219], v[16:19]\n\t"
    "ds_read_b64_tr_b16 a[244:245], v0 offset:36864\n\t"
    "v_cvt_pk_bf16_f32 v6, v6, v4\n\t"
    "v_mfma_f32_16x16x32_bf16 v[20:23], a[248:251], v[140:143], v[20:23]\n\t"
    "ds_read_b64_tr_b16 a[246:247], v0 offset:43008\n\t"
    "v_accvgpr_read_b32 v7, a122\n\t"
    "v_mfma_f32_16x16x32_bf16 v[20:23], a[252:255], a[220:223], v[20:23]\n\t"
    "buffer_load_dwordx4 v1, s[44:47], s53 offen nt lds\n\t"
    "v_mfma_f32_16x16x32_bf16 v[24:27], a[248:251], v[144:147], v[24:27]\n\t"
    "s_addk_i32 s53, 3072\n\t"
    "v_accvgpr_read_b32 v4, a123\n\t"
    "v_mul_f32 v7, v7, v11\n\t"
    "v_mfma_f32_16x16x32_bf16 v[24:27], a[252:255], a[224:227], v[24:27]\n\t"
    "s_addk_i32 m0, 1024\n\t"
    "v_mul_f32 v4, v4, v11\n\t"
    "v_cvt_pk_bf16_f32 v7, v7, v4\n\t"
    "v_mfma_f32_16x16x32_bf16 v[28:31], a[248:251], v[148:151], v[28:31]\n\t"
    "v_mfma_f32_16x16x32_bf16 v[28:31], a[252:255], a[228:231], v[28:31]\n\t"
    "ds_write_b64 v14, v[6:7] offset:4864\n\t"
    "v_mfma_f32_16x16x32_bf16 v[32:35], a[248:251], v[152:155], v[32:35]\n\t"
    "v_accvgpr_read_b32 v6, a124\n\t"
    "v_accvgpr_read_b32 v4, a125\n\t"
    "v_mfma_f32_16x16x32_bf16 v[32:35], a[252:255], a[232:235], v[32:35]\n\t"
    "v_mul_f32 v6, v6, v12\n\t"
    "v_mul_f32 v4, v4, v12\n\t"
    "v_mfma_f32_16x16x32_bf16 v[36:39], a[248:251], v[156:159], v[36:39]\n\t"
    "v_cvt_pk_bf16_f32 v6, v6, v4\n\t"
    "v_accvgpr_read_b32 v7, a126\n\t"
    "v_mfma_f32_16x16x32_bf16 v[36:39], a[252:255], a[236:239], v[36:39]\n\t"
    "s_waitcnt lgkmcnt(1)\n\t"
    "ds_read_b64_tr_b16 a[248:249], v0 offset:37888\n\t"
    "v_accvgpr_read_b32 v4, a127\n\t"
    "v_mfma_f32_16x16x32_bf16 v[16:19], a[240:243], v[160:163], v[16:19]\n\t"
    "ds_read_b64_tr_b16 a[250:251], v0 offset:44032\n\t"
    "v_mul_f32 v7, v7, v12\n\t"
    "v_mfma_f32_16x16x32_bf16 v[16:19], a[244:247], v[184:187], v[16:19]\n\t"
    "ds_read_b64_tr_b16 a[252:253], v0 offset:38912\n\t"
    "v_mul_f32 v4, v4, v12\n\t"
    "v_mfma_f32_16x16x32_bf16 v[20:23], a[240:243], v[164:167], v[20:23]\n\t"
    "ds_read_b64_tr_b16 a[254:255], v0 offset:45056\n\t"
    "v_cvt_pk_bf16_f32 v7, v7, v4\n\t"
    "v_mfma_f32_16x16x32_bf16 v[20:23], a[244:247], v[188:191], v[20:23]\n\t"
    "buffer_load_dwordx4 v1, s[44:47], s53 offen nt lds\n\t"
    "v_mfma_f32_16x16x32_bf16 v[24:27], a[240:243], v[168:171], v[24:27]\n\t"
    "s_mov_b32 s53, 27776\n\t"
    "v_mfma_f32_16x16x32_bf16 v[24:27], a[244:247], v[192:195], v[24:27]\n\t"
    "s_add_u32 m0, s52, 0\n\t"
    "ds_write_b64 v14, v[6:7] offset:6912\n\t"
    "v_mfma_f32_16x16x32_bf16 v[28:31], a[240:243], v[172:175], v[28:31]\n\t"
    "v_accvgpr_read_b32 v6, a128\n\t"
    "v_accvgpr_read_b32 v4, a129\n\t"
    "v_mfma_f32_16x16x32_bf16 v[28:31], a[244:247], v[196:199], v[28:31]\n\t"
    "v_mul_f32 v6, v6, v13\n\t"
    "v_mul_f32 v4, v4, v13\n\t"
    "v_mfma_f32_16x16x32_bf16 v[32:35], a[240:243], v[176:179], v[32:35]\n\t"
    "v_cvt_pk_bf16_f32 v6, v6, v4\n\t"
    "v_accvgpr_read_b32 v7, a130\n\t"
    "v_mfma_f32_16x16x32_bf16 v[32:35], a[244:247], v[200:203], v[32:35]\n\t"
    "v_accvgpr_read_b32 v4, a131\n\t"
    "v_mul_f32 v7, v7, v13\n\t"
    "v_mfma_f32_16x16x32_bf16 v[36:39], a[240:243], v[180:183], v[36:39]\n\t"
    "v_mul_f32 v4, v4, v13\n\t"
    "v_cvt_pk_bf16_f32 v7, v7, v4\n\t"
    "v_mfma_f32_16x16x32_bf16 v[36:39], a[244:247], v[204:207], v[36:39]\n\t"
    "s_waitcnt lgkmcnt(1)\n\t"
    "v_xor_b32 v0, 32, v0\n\t"
    "ds_read_b64_tr_b16 a[240:241], v0 offset:512\n\t"
    "v_mfma_f32_16x16x32_bf16 v[16:19], a[248:251], v[208:211], v[16:19]\n\t"
    "ds_read_b64_tr_b16 a[242:243], v0 offset:6656\n\t"
    "v_mfma_f32_16x16x32_bf16 v[16:19], a[252:255], v[232:235], v[16:19]\n\t"
    "ds_read_b64_tr_b16 a[244:245], v0 offset:1536\n\t"
    "v_mfma_f32_16x16x32_bf16 v[20:23], a[248:251], v[212:215], v[20:23]\n\t"
    "ds_read_b64_tr_b16 a[246:247], v0 offset:7680\n\t"
    "v_mfma_f32_16x16x32_bf16 v[20:23], a[252:255], v[236:239], v[20:23]\n\t"
    "buffer_load_dwordx4 v1, s[44:47], s53 offen nt lds\n\t"
    "v_mfma_f32_16x16x32_bf16 v[24:27], a[248:251], v[216:219], v[24:27]\n\t"
    "s_add_u32 s53, s65, 128\n\t"
    "v_mfma_f32_16x16x32_bf16 v[24:27], a[252:255], v[240:243], v[24:27]\n\t"
    "s_addk_i32 m0, 1024\n\t"
    "ds_write_b64 v14, v[6:7] offset:7168\n\t"
    "v_mfma_f32_16x16x32_bf16 v[28:31], a[248:251], v[220:223], v[28:31]\n\t"
    "v_mfma_f32_16x16x32_bf16 v[28:31], a[252:255], v[244:247], v[28:31]\n\t"
    "v_mfma_f32_16x16x32_bf16 v[32:35], a[248:251], v[224:227], v[32:35]\n\t"
    "v_mfma_f32_16x16x32_bf16 v[32:35], a[252:255], v[248:251], v[32:35]\n\t"
    "v_mfma_f32_16x16x32_bf16 v[36:39], a[248:251], v[228:231], v[36:39]\n\t"
    "v_mfma_f32_16x16x32_bf16 v[36:39], a[252:255], v[252:255], v[36:39]\n\t"
    "s_waitcnt lgkmcnt(1)\n\t"
    "ds_read_b64_tr_b16 a[248:249], v0 offset:2560\n\t"
    "v_mfma_f32_16x16x32_bf16 v[40:43], a[240:243], v[64:67], 0\n\t"
    "ds_read_b64_tr_b16 a[250:251], v0 offset:8704\n\t"
    "v_mfma_f32_16x16x32_bf16 v[40:43], a[244:247], a[144:147], v[40:43]\n\t"
    "ds_read_b64_tr_b16 a[252:253], v0 offset:49664\n\t"
    "s_add_u32 s40, s40, 37748736\n\t"
    "s_addc_u32 s41, s41, 0\n\t"
    "s_cmp_lt_u32 s58, 3\n\t"
    "s_cselect_b32 s42, 147456, 0\n\t"
    "v_mfma_f32_16x16x32_bf16 v[44:47], a[240:243], v[68:71], 0\n\t"
    "ds_read_b64_tr_b16 a[254:255], v0 offset:55808\n\t"
    "v_mfma_f32_16x16x32_bf16 v[44:47], a[244:247], a[148:151], v[44:47]\n\t"
    "buffer_load_dwordx4 v1, s[44:47], s53 offen nt lds\n\t"
    "v_mfma_f32_16x16x32_bf16 v[48:51], a[240:243], v[72:75], 0\n\t"
    "s_add_u32 s53, s75, 128\n\t"
    "v_mfma_f32_16x16x32_bf16 v[48:51], a[244:247], a[152:155], v[48:51]\n\t"
    "s_addk_i32 m0, 1024\n\t"
    "s_add_u32 s48, s48, 37748736\n\t"
    "s_addc_u32 s49, s49, 0\n\t"
    "s_mov_b32 s50, 147456\n\t"
    "v_mfma_f32_16x16x32_bf16 v[52:55], a[240:243], v[76:79], 0\n\t"
    "v_mul_f32 v16, v16, v8\n\t"
    "v_mul_f32 v17, v17, v8\n\t"
    "v_mfma_f32_16x16x32_bf16 v[52:55], a[244:247], a[156:159], v[52:55]\n\t"
    "v_mul_f32 v18, v18, v8\n\t"
    "v_mul_f32 v19, v19, v8\n\t"
    "v_mfma_f32_16x16x32_bf16 v[56:59], a[240:243], v[80:83], 0\n\t"
    "v_cvt_pk_bf16_f32 v16, v16, v17\n\t"
    "v_cvt_pk_bf16_f32 v17, v18, v19\n\t"
    "v_mfma_f32_16x16x32_bf16 v[56:59], a[244:247], a[160:163], v[56:59]\n\t"
    "ds_write_b64 v14, v[16:17] offset:3456\n\t"
    "v_mfma_f32_16x16x32_bf16 v[60:63], a[240:243], v[84:87], 0\n\t"
    "v_mul_f32 v20, v20, v9\n\t"
    "v_mul_f32 v21, v21, v9\n\t"
    "v_mfma_f32_16x16x32_bf16 v[60:63], a[244:247], a[164:167], v[60:63]\n\t"
    "s_waitcnt lgkmcnt(1)\n\t"
    "ds_read_b64_tr_b16 a[240:241], v0 offset:50688\n\t"
    "v_mul_f32 v22, v22, v9\n\t"
    "v_mfma_f32_16x16x32_bf16 v[40:43], a[248:251], v[88:91], v[40:43]\n\t"
    "ds_read_b64_tr_b16 a[242:243], v0 offset:56832\n\t"
    "v_mul_f32 v23, v23, v9\n\t"
    "v_mfma_f32_16x16x32_bf16 v[40:43], a[252:255], a[168:171], v[40:43]\n\t"
    "ds_read_b64_tr_b16 a[244:245], v0 offset:51712\n\t"
    "v_cvt_pk_bf16_f32 v20, v20, v21\n\t"
    "v_mfma_f32_16x16x32_bf16 v[44:47], a[248:251], v[92:95], v[44:47]\n\t"
    "ds_read_b64_tr_b16 a[246:247], v0 offset:57856\n\t"
    "v_cvt_pk_bf16_f32 v21, v22, v23\n\t"
    "v_mfma_f32_16x16x32_bf16 v[44:47], a[252:255], a[172:175], v[44:47]\n\t"
    "buffer_load_dwordx4 v1, s[44:47], s53 offen nt lds\n\t"
    "v_mfma_f32_16x16x32_bf16 v[48:51], a[248:251], v[96:99], v[48:51]\n\t"
    "s_mov_b32 s53, 256\n\t"
    "v_mfma_f32_16x16x32_bf16 v[48:51], a[252:255], a[176:179], v[48:51]\n\t"
    "s_add_u32 m0, s52, 86016\n\t"
    "ds_write_b64 v14, v[20:21] offset:3712\n\t"
    "v_mfma_f32_16x16x32_bf16 v[52:55], a[248:251], v[100:103], v[52:55]\n\t"
    "v_mul_f32 v24, v24, v10\n\t"
    "v_mul_f32 v25, v25, v10\n\t"
    "v_mfma_f32_16x16x32_bf16 v[52:55], a[252:255], a[180:183], v[52:55]\n\t"
    "v_mul_f32 v26, v26, v10\n\t"
    "v_mul_f32 v27, v27, v10\n\t"
    "v_mfma_f32_16x16x32_bf16 v[56:59], a[248:251], v[104:107], v[56:59]\n\t"
    "v_cvt_pk_bf16_f32 v24, v24, v25\n\t"
    "v_cvt_pk_bf16_f32 v25, v26, v27\n\t"
    "v_mfma_f32_16x16x32_bf16 v[56:59], a[252:255], a[184:187], v[56:59]\n\t"
    "v_mfma_f32_16x16x32_bf16 v[60:63], a[248:251], v[108:111], v[60:63]\n\t"
    "v_mfma_f32_16x16x32_bf16 v[60:63], a[252:255], a[188:191], v[60:63]\n\t"
    "s_waitcnt lgkmcnt(1)\n\t"
    "ds_read_b64_tr_b16 a[248:249], v0 offset:12800\n\t"
    "v_mfma_f32_16x16x32_bf16 v[40:43], a[240:243], v[112:115], v[40:43]\n\t"
    "ds_read_b64_tr_b16 a[250:251], v0 offset:18944\n\t"
    "v_mfma_f32_16x16x32_bf16 v[40:43], a[244:247], a[192:195], v[40:43]\n\t"
    "ds_read_b64_tr_b16 a[252:253], v0 offset:13824\n\t"
    "v_mfma_f32_16x16x32_bf16 v[44:47], a[240:243], v[116:119], v[44:47]\n\t"
    "ds_read_b64_tr_b16 a[254:255], v0 offset:19968\n\t"
    "v_mfma_f32_16x16x32_bf16 v[44:47], a[244:247], a[196:199], v[44:47]\n\t"
    "buffer_load_dwordx4 v1, s[44:47], s53 offen nt lds\n\t"
    "v_mfma_f32_16x16x32_bf16 v[48:51], a[240:243], v[120:123], v[48:51]\n\t"
    "s_addk_i32 s53, 3072\n\t"
    "v_mfma_f32_16x16x32_bf16 v[48:51], a[244:247], a[200:203], v[48:51]\n\t"
    "s_addk_i32 m0, 1024\n\t"
    "ds_write_b64 v14, v[24:25] offset:5760\n\t"
    "v_mfma_f32_16x16x32_bf16 v[52:55], a[240:243], v[124:127], v[52:55]\n\t"
    "v_mul_f32 v28, v28, v11\n\t"
    "v_mul_f32 v29, v29, v11\n\t"
    "v_mfma_f32_16x16x32_bf16 v[52:55], a[244:247], a[204:207], v[52:55]\n\t"
    "v_mul_f32 v30, v30, v11\n\t"
    "v_mul_f32 v31, v31, v11\n\t"
    "v_mfma_f32_16x16x32_bf16 v[56:59], a[240:243], v[128:131], v[56:59]\n\t"
    "v_cvt_pk_bf16_f32 v28, v28, v29\n\t"
    "v_cvt_pk_bf16_f32 v29, v30, v31\n\t"
    "v_mfma_f32_16x16x32_bf16 v[56:59], a[244:247], a[208:211], v[56:59]\n\t"
    "v_mfma_f32_16x16x32_bf16 v[60:63], a[240:243], v[132:135], v[60:63]\n\t"
    "v_mfma_f32_16x16x32_bf16 v[60:63], a[244:247], a[212:215], v[60:63]\n\t"
    "s_waitcnt lgkmcnt(1)\n\t"
    "ds_read_b64_tr_b16 a[240:241], v0 offset:14848\n\t"
    "v_mfma_f32_16x16x32_bf16 v[40:43], a[248:251], v[136:139], v[40:43]\n\t"
    "ds_read_b64_tr_b16 a[242:243], v0 offset:20992\n\t"
    "v_mfma_f32_16x16x32_bf16 v[40:43], a[252:255], a[216:219], v[40:43]\n\t"
    "ds_read_b64_tr_b16 a[244:245], v0 offset:37376\n\t"
    "v_mfma_f32_16x16x32_bf16 v[44:47], a[248:251], v[140:143], v[44:47]\n\t"
    "ds_read_b64_tr_b16 a[246:247], v0 offset:43520\n\t"
    "v_mfma_f32_16x16x32_bf16 v[44:47], a[252:255], a[220:223], v[44:47]\n\t"
    "buffer_load_dwordx4 v1, s[44:47], s53 offen nt lds\n\t"
    "v_mfma_f32_16x16x32_bf16 v[48:51], a[248:251], v[144:147], v[48:51]\n\t"
    "s_addk_i32 s53, 3072\n\t"
    "v_mfma_f32_16x16x32_bf16 v[48:51], a[252:255], a[224:227], v[48:51]\n\t"
    "s_addk_i32 m0, 1024\n\t"
    "ds_write_b64 v14, v[28:29] offset:6016\n\t"
    "v_mfma_f32_16x16x32_bf16 v[52:55], a[248:251], v[148:151], v[52:55]\n\t"
    "v_mul_f32 v32, v32, v12\n\t"
    "v_mul_f32 v33, v33, v12\n\t"
    "v_mfma_f32_16x16x32_bf16 v[52:55], a[252:255], a[228:231], v[52:55]\n\t"
    "v_mul_f32 v34, v34, v12\n\t"
    "v_mul_f32 v35, v35, v12\n\t"
    "v_mfma_f32_16x16x32_bf16 v[56:59], a[248:251], v[152:155], v[56:59]\n\t"
    "v_cvt_pk_bf16_f32 v32, v32, v33\n\t"
    "v_cvt_pk_bf16_f32 v33, v34, v35\n\t"
    "v_mfma_f32_16x16x32_bf16 v[56:59], a[252:255], a[232:235], v[56:59]\n\t"
    "v_mfma_f32_16x16x32_bf16 v[60:63], a[248:251], v[156:159], v[60:63]\n\t"
    "v_mfma_f32_16x16x32_bf16 v[60:63], a[252:255], a[236:239], v[60:63]\n\t"
    "s_waitcnt lgkmcnt(1)\n\t"
    "ds_read_b64_tr_b16 a[248:249], v0 offset:38400\n\t"
    "v_mfma_f32_16x16x32_bf16 v[40:43], a[240:243], v[160:163], v[40:43]\n\t"
    "ds_read_b64_tr_b16 a[250:251], v0 offset:44544\n\t"
    "v_mfma_f32_16x16x32_bf16 v[40:43], a[244:247], v[184:187], v[40:43]\n\t"
    "ds_read_b64_tr_b16 a[252:253], v0 offset:39424\n\t"
    "v_mfma_f32_16x16x32_bf16 v[44:47], a[240:243], v[164:167], v[44:47]\n\t"
    "ds_read_b64_tr_b16 a[254:255], v0 offset:45568\n\t"
    "v_mfma_f32_16x16x32_bf16 v[44:47], a[244:247], v[188:191], v[44:47]\n\t"
    "buffer_load_dwordx4 v1, s[44:47], s53 offen nt lds\n\t"
    "v_mfma_f32_16x16x32_bf16 v[48:51], a[240:243], v[168:171], v[48:51]\n\t"
    "s_mov_b32 s53, 9472\n\t"
    "v_mfma_f32_16x16x32_bf16 v[48:51], a[244:247], v[192:195], v[48:51]\n\t"
    "s_add_u32 m0, s52, 36864\n\t"
    "ds_write_b64 v14, v[32:33] offset:8064\n\t"
    "v_mfma_f32_16x16x32_bf16 v[52:55], a[240:243], v[172:175], v[52:55]\n\t"
    "v_mul_f32 v36, v36, v13\n\t"
    "v_mul_f32 v37, v37, v13\n\t"
    "v_mfma_f32_16x16x32_bf16 v[52:55], a[244:247], v[196:199], v[52:55]\n\t"
    "v_mul_f32 v38, v38, v13\n\t"
    "v_mul_f32 v39, v39, v13\n\t"
    "v_mfma_f32_16x16x32_bf16 v[56:59], a[240:243], v[176:179], v[56:59]\n\t"
    "v_cvt_pk_bf16_f32 v36, v36, v37\n\t"
    "v_cvt_pk_bf16_f32 v37, v38, v39\n\t"
    "v_mfma_f32_16x16x32_bf16 v[56:59], a[244:247], v[200:203], v[56:59]\n\t"
    "v_mfma_f32_16x16x32_bf16 v[60:63], a[240:243], v[180:183], v[60:63]\n\t"
    "v_mfma_f32_16x16x32_bf16 v[60:63], a[244:247], v[204:207], v[60:63]\n\t"
    "s_waitcnt lgkmcnt(1)\n\t"
    "v_xor_b32 v0, 32, v0\n\t"
    "ds_read_b64_tr_b16 a[240:241], v0 offset:512\n\t"
    "v_mfma_f32_16x16x32_bf16 v[40:43], a[248:251], v[208:211], v[40:43]\n\t"
    "ds_read_b64_tr_b16 a[242:243], v0 offset:6656\n\t"
    "v_mfma_f32_16x16x32_bf16 v[40:43], a[252:255], v[232:235], v[40:43]\n\t"
    "ds_read_b64_tr_b16 a[244:245], v0 offset:1536\n\t"
    "v_mfma_f32_16x16x32_bf16 v[44:47], a[248:251], v[212:215], v[44:47]\n\t"
    "ds_read_b64_tr_b16 a[246:247], v0 offset:7680\n\t"
    "v_mfma_f32_16x16x32_bf16 v[44:47], a[252:255], v[236:239], v[44:47]\n\t"
    "buffer_load_dwordx4 v1, s[44:47], s53 offen nt lds\n\t"
    "v_mfma_f32_16x16x32_bf16 v[48:51], a[248:251], v[216:219], v[48:51]\n\t"
    "s_addk_i32 s53, 3072\n\t"
    "v_mfma_f32_16x16x32_bf16 v[48:51], a[252:255], v[240:243], v[48:51]\n\t"
    "s_addk_i32 m0, 1024\n\t"
    "ds_write_b64 v14, v[36:37] offset:8320\n\t"
    "v_mfma_f32_16x16x32_bf16 v[52:55], a[248:251], v[220:223], v[52:55]\n\t"
    "v_mul_f32 v40, v40, v8\n\t"
    "v_mul_f32 v41, v41, v8\n\t"
    "v_mfma_f32_16x16x32_bf16 v[52:55], a[252:255], v[244:247], v[52:55]\n\t"
    "v_mul_f32 v42, v42, v8\n\t"
    "v_mul_f32 v43, v43, v8\n\t"
    "v_mfma_f32_16x16x32_bf16 v[56:59], a[248:251], v[224:227], v[56:59]\n\t"
    "v_cvt_pk_bf16_f32 v40, v40, v41\n\t"
    "v_cvt_pk_bf16_f32 v41, v42, v43\n\t"
    "v_mfma_f32_16x16x32_bf16 v[56:59], a[252:255], v[248:251], v[56:59]\n\t"
    "v_accvgpr_write_b32 a132, v40\n\t"
    "v_accvgpr_write_b32 a133, v41\n\t"
    "v_mfma_f32_16x16x32_bf16 v[60:63], a[248:251], v[228:231], v[60:63]\n\t"
    "v_mul_f32 v44, v44, v9\n\t"
    "v_mul_f32 v45, v45, v9\n\t"
    "v_mfma_f32_16x16x32_bf16 v[60:63], a[252:255], v[252:255], v[60:63]\n\t"
    "s_waitcnt lgkmcnt(1)\n\t"
    "ds_read_b64_tr_b16 a[248:249], v0 offset:2560\n\t"
    "v_mul_f32 v46, v46, v9\n\t"
    "v_mfma_f32_16x16x32_bf16 v[16:19], a[240:243], v[64:67], 0\n\t"
    "ds_read_b64_tr_b16 a[250:251], v0 offset:8704\n\t"
    "v_mul_f32 v47, v47, v9\n\t"
    "v_mfma_f32_16x16x32_bf16 v[16:19], a[244:247], a[144:147], v[16:19]\n\t"
    "ds_read_b64_tr_b16 a[252:253], v0 offset:49664\n\t"
    "v_cvt_pk_bf16_f32 v44, v44, v45\n\t"
    "v_mfma_f32_16x16x32_bf16 v[20:23], a[240:243], v[68:71], 0\n\t"
    "ds_read_b64_tr_b16 a[254:255], v0 offset:55808\n\t"
    "v_cvt_pk_bf16_f32 v45, v46, v47\n\t"
    "v_mfma_f32_16x16x32_bf16 v[20:23], a[244:247], a[148:151], v[20:23]\n\t"
    "buffer_load_dwordx4 v1, s[44:47], s53 offen nt lds\n\t"
    "v_mfma_f32_16x16x32_bf16 v[24:27], a[240:243], v[72:75], 0\n\t"
    "s_addk_i32 s53, 3072\n\t"
    "v_accvgpr_write_b32 a134, v44\n\t"
    "v_accvgpr_write_b32 a135, v45\n\t"
    "v_mfma_f32_16x16x32_bf16 v[24:27], a[244:247], a[152:155], v[24:27]\n\t"
    "s_addk_i32 m0, 1024\n\t"
    "v_mul_f32 v48, v48, v10\n\t"
    "v_mul_f32 v49, v49, v10\n\t"
    "v_mfma_f32_16x16x32_bf16 v[28:31], a[240:243], v[76:79], 0\n\t"
    "v_mul_f32 v50, v50, v10\n\t"
    "v_mul_f32 v51, v51, v10\n\t"
    "v_mfma_f32_16x16x32_bf16 v[28:31], a[244:247], a[156:159], v[28:31]\n\t"
    "v_cvt_pk_bf16_f32 v48, v48, v49\n\t"
    "v_cvt_pk_bf16_f32 v49, v50, v51\n\t"
    "v_mfma_f32_16x16x32_bf16 v[32:35], a[240:243], v[80:83], 0\n\t"
    "v_accvgpr_write_b32 a136, v48\n\t"
    "v_accvgpr_write_b32 a137, v49\n\t"
    "v_mfma_f32_16x16x32_bf16 v[32:35], a[244:247], a[160:163], v[32:35]\n\t"
    "v_mul_f32 v52, v52, v11\n\t"
    "v_mul_f32 v53, v53, v11\n\t"
    "v_mfma_f32_16x16x32_bf16 v[36:39], a[240:243], v[84:87], 0\n\t"
    "v_mul_f32 v54, v54, v11\n\t"
    "v_mul_f32 v55, v55, v11\n\t"
    "v_mfma_f32_16x16x32_bf16 v[36:39], a[244:247], a[164:167], v[36:39]\n\t"
    "s_waitcnt lgkmcnt(0)\n\t"
    "ds_read_b64_tr_b16 a[240:241], v0 offset:50688\n\t"
    "v_cvt_pk_bf16_f32 v52, v52, v53\n\t"
    "v_mfma_f32_16x16x32_bf16 v[16:19], a[248:251], v[88:91], v[16:19]\n\t"
    "ds_read_b64_tr_b16 a[242:243], v0 offset:56832\n\t"
    "v_cvt_pk_bf16_f32 v53, v54, v55\n\t"
    "v_mfma_f32_16x16x32_bf16 v[16:19], a[252:255], a[168:171], v[16:19]\n\t"
    "ds_read_b64_tr_b16 a[244:245], v0 offset:51712\n\t"
    "v_accvgpr_write_b32 a138, v52\n\t"
    "v_mfma_f32_16x16x32_bf16 v[20:23], a[248:251], v[92:95], v[20:23]\n\t"
    "ds_read_b64_tr_b16 a[246:247], v0 offset:57856\n\t"
    "v_accvgpr_write_b32 a139, v53\n\t"
    "v_mfma_f32_16x16x32_bf16 v[20:23], a[252:255], a[172:175], v[20:23]\n\t"
    "buffer_load_dwordx4 v1, s[44:47], s53 offen nt lds\n\t"
    "v_mfma_f32_16x16x32_bf16 v[24:27], a[248:251], v[96:99], v[24:27]\n\t"
    "s_mov_b32 s53, 18688\n\t"
    "v_mul_f32 v56, v56, v12\n\t"
    "v_mul_f32 v57, v57, v12\n\t"
    "v_mfma_f32_16x16x32_bf16 v[24:27], a[252:255], a[176:179], v[24:27]\n\t"
    "s_add_u32 m0, s52, 61440\n\t"
    "v_mul_f32 v58, v58, v12\n\t"
    "v_mul_f32 v59, v59, v12\n\t"
    "v_mfma_f32_16x16x32_bf16 v[28:31], a[248:251], v[100:103], v[28:31]\n\t"
    "v_cvt_pk_bf16_f32 v56, v56, v57\n\t"
    "v_cvt_pk_bf16_f32 v57, v58, v59\n\t"
    "v_mfma_f32_16x16x32_bf16 v[28:31], a[252:255], a[180:183], v[28:31]\n\t"
    "v_accvgpr_write_b32 a140, v56\n\t"
    "v_accvgpr_write_b32 a141, v57\n\t"
    "v_mfma_f32_16x16x32_bf16 v[32:35], a[248:251], v[104:107], v[32:35]\n\t"
    "v_mul_f32 v60, v60, v13\n\t"
    "v_mul_f32 v61, v61, v13\n\t"
    "v_mfma_f32_16x16x32_bf16 v[32:35], a[252:255], a[184:187], v[32:35]\n\t"
    "v_mul_f32 v62, v62, v13\n\t"
    "v_mul_f32 v63, v63, v13\n\t"
    "v_mfma_f32_16x16x32_bf16 v[36:39], a[248:251], v[108:111], v[36:39]\n\t"
    "v_cvt_pk_bf16_f32 v60, v60, v61\n\t"
    "v_cvt_pk_bf16_f32 v61, v62, v63\n\t"
    "v_mfma_f32_16x16x32_bf16 v[36:39], a[252:255], a[188:191], v[36:39]\n\t"
    "s_waitcnt lgkmcnt(0)\n\t"
    "s_barrier\n\t"
    "ds_read_b64_tr_b16 a[248:249], v0 offset:12800\n\t"
    "v_mfma_f32_16x16x32_bf16 v[16:19], a[240:243], v[112:115], v[16:19]\n\t"
    "ds_read_b64_tr_b16 a[250:251], v0 offset:18944\n\t"
    "v_accvgpr_write_b32 a142, v60\n\t"
    "v_mfma_f32_16x16x32_bf16 v[16:19], a[244:247], a[192:195], v[16:19]\n\t"
    "ds_read_b64_tr_b16 a[252:253], v0 offset:13824\n\t"
    "v_accvgpr_write_b32 a143, v61\n\t"
    "v_mfma_f32_16x16x32_bf16 v[20:23], a[240:243], v[116:119], v[20:23]\n\t"
    "ds_read_b64_tr_b16 a[254:255], v0 offset:19968\n\t"
    "v_mfma_f32_16x16x32_bf16 v[20:23], a[244:247], a[196:199], v[20:23]\n\t"
    "buffer_load_dwordx4 v1, s[44:47], s53 offen nt lds\n\t"
    "v_mfma_f32_16x16x32_bf16 v[24:27], a[240:243], v[120:123], v[24:27]\n\t"
    "s_addk_i32 s53, 3072\n\t"
    "v_mfma_f32_16x16x32_bf16 v[24:27], a[244:247], a[200:203], v[24:27]\n\t"
    "s_addk_i32 m0, 1024\n\t"
    "v_mfma_f32_16x16x32_bf16 v[28:31], a[240:243], v[124:127], v[28:31]\n\t"
    "v_mfma_f32_16x16x32_bf16 v[28:31], a[244:247], a[204:207], v[28:31]\n\t"
    "v_mfma_f32_16x16x32_bf16 v[32:35], a[240:243], v[128:131], v[32:35]\n\t"
    "v_mfma_f32_16x16x32_bf16 v[32:35], a[244:247], a[208:211], v[32:35]\n\t"
    "v_mfma_f32_16x16x32_bf16 v[36:39], a[240:243], v[132:135], v[36:39]\n\t"
    "v_mfma_f32_16x16x32_bf16 v[36:39], a[244:247], a[212:215], v[36:39]\n\t"
    "s_waitcnt lgkmcnt(0)\n\t"
    "ds_read_b64_tr_b16 a[240:241], v0 offset:14848\n\t"
    "v_mfma_f32_16x16x32_bf16 v[16:19], a[248:251], v[136:139], v[16:19]\n\t"
    "ds_read_b64_tr_b16 a[242:243], v0 offset:20992\n\t"
    "v_mfma_f32_16x16x32_bf16 v[16:19], a[252:255], a[216:219], v[16:19]\n\t"
    "ds_read_b64_tr_b16 a[244:245], v0 offset:37376\n\t"
    "v_mfma_f32_16x16x32_bf16 v[20:23], a[248:251], v[140:143], v[20:23]\n\t"
    "ds_read_b64_tr_b16 a[246:247], v0 offset:43520\n\t"
    "v_mfma_f32_16x16x32_bf16 v[20:23], a[252:255], a[220:223], v[20:23]\n\t"
    "buffer_load_dwordx4 v1, s[44:47], s53 offen nt lds\n\t"
    "v_mfma_f32_16x16x32_bf16 v[24:27], a[248:251], v[144:147], v[24:27]\n\t"
    "s_addk_i32 s53, 3072\n\t"
    "v_mfma_f32_16x16x32_bf16 v[24:27], a[252:255], a[224:227], v[24:27]\n\t"
    "s_addk_i32 m0, 1024\n\t"
    "v_mfma_f32_16x16x32_bf16 v[28:31], a[248:251], v[148:151], v[28:31]\n\t"
    "v_mfma_f32_16x16x32_bf16 v[28:31], a[252:255], a[228:231], v[28:31]\n\t"
    "v_mfma_f32_16x16x32_bf16 v[32:35], a[248:251], v[152:155], v[32:35]\n\t"
    "v_mfma_f32_16x16x32_bf16 v[32:35], a[252:255], a[232:235], v[32:35]\n\t"
    "v_mfma_f32_16x16x32_bf16 v[36:39], a[248:251], v[156:159], v[36:39]\n\t"
    "v_mfma_f32_16x16x32_bf16 v[36:39], a[252:255], a[236:239], v[36:39]\n\t"
    "s_waitcnt lgkmcnt(0)\n\t"
    "ds_read_b64_tr_b16 a[248:249], v0 offset:38400\n\t"
    "v_mfma_f32_16x16x32_bf16 v[16:19], a[240:243], v[160:163], v[16:19]\n\t"
    "ds_read_b64_tr_b16 a[250:251], v0 offset:44544\n\t"
    "v_mfma_f32_16x16x32_bf16 v[16:19], a[244:247], v[184:187], v[16:19]\n\t"
    "ds_read_b64_tr_b16 a[252:253], v0 offset:39424\n\t"
    "v_mfma_f32_16x16x32_bf16 v[20:23], a[240:243], v[164:167], v[20:23]\n\t"
    "ds_read_b64_tr_b16 a[254:255], v0 offset:45568\n\t"
    "v_mfma_f32_16x16x32_bf16 v[20:23], a[244:247], v[188:191], v[20:23]\n\t"
    "buffer_load_dwordx4 v1, s[44:47], s53 offen nt lds\n\t"
    "v_mfma_f32_16x16x32_bf16 v[24:27], a[240:243], v[168:171], v[24:27]\n\t"
    "s_mov_b32 s53, 27904\n\t"
    "v_mfma_f32_16x16x32_bf16 v[24:27], a[244:247], v[192:195], v[24:27]\n\t"
    "s_add_u32 m0, s52, 73728\n\t"
    "v_mfma_f32_16x16x32_bf16 v[28:31], a[240:243], v[172:175], v[28:31]\n\t"
    "v_mfma_f32_16x16x32_bf16 v[28:31], a[244:247], v[196:199], v[28:31]\n\t"
    "v_mfma_f32_16x16x32_bf16 v[32:35], a[240:243], v[176:179], v[32:35]\n\t"
    "v_mfma_f32_16x16x32_bf16 v[32:35], a[244:247], v[200:203], v[32:35]\n\t"
    "v_mfma_f32_16x16x32_bf16 v[36:39], a[240:243], v[180:183], v[36:39]\n\t"
    "v_mfma_f32_16x16x32_bf16 v[36:39], a[244:247], v[204:207], v[36:39]\n\t"
    "s_waitcnt vmcnt(9) lgkmcnt(0)\n\t"
    "s_barrier\n\t"
    "v_add_u32 v0, 0xffff1000, v0\n\t"
    "v_xor_b32 v0, 32, v0\n\t"
    "ds_read_b64_tr_b16 a[240:241], v0 offset:12288\n\t"
    "v_mfma_f32_16x16x32_bf16 v[16:19], a[248:251], v[208:211], v[16:19]\n\t"
    "ds_read_b64_tr_b16 a[242:243], v0 offset:18432\n\t"
    "v_mfma_f32_16x16x32_bf16 v[16:19], a[252:255], v[232:235], v[16:19]\n\t"
    "ds_read_b64_tr_b16 a[244:245], v0 offset:13312\n\t"
    "v_mfma_f32_16x16x32_bf16 v[20:23], a[248:251], v[212:215], v[20:23]\n\t"
    "ds_read_b64_tr_b16 a[246:247], v0 offset:19456\n\t"
    "v_mfma_f32_16x16x32_bf16 v[20:23], a[252:255], v[236:239], v[20:23]\n\t"
    "v_mfma_f32_16x16x32_bf16 v[24:27], a[248:251], v[216:219], v[24:27]\n\t"
    "v_mfma_f32_16x16x32_bf16 v[24:27], a[252:255], v[240:243], v[24:27]\n\t"
    "buffer_load_dwordx4 v1, s[44:47], s53 offen nt lds\n\t"
    "v_mfma_f32_16x16x32_bf16 v[28:31], a[248:251], v[220:223], v[28:31]\n\t"
    "s_add_u32 s53, s65, 256\n\t"
    "v_mul_f32 v16, v16, v8\n\t"
    "v_mul_f32 v17, v17, v8\n\t"
    "v_mfma_f32_16x16x32_bf16 v[28:31], a[252:255], v[244:247], v[28:31]\n\t"
    "s_addk_i32 m0, 1024\n\t"
    "v_mul_f32 v18, v18, v8\n\t"
    "v_mul_f32 v19, v19, v8\n\t"
    "v_cvt_pk_bf16_f32 v16, v16, v17\n\t"
    "v_cvt_pk_bf16_f32 v17, v18, v19\n\t"
    "v_mul_f32 v20, v20, v9\n\t"
    "v_mul_f32 v21, v21, v9\n\t"
    "v_mfma_f32_16x16x32_bf16 v[32:35], a[248:251], v[224:227], v[32:35]\n\t"
    "v_mul_f32 v22, v22, v9\n\t"
    "v_mul_f32 v23, v23, v9\n\t"
    "v_mfma_f32_16x16x32_bf16 v[32:35], a[252:255], v[248:251], v[32:35]\n\t"
    "v_cvt_pk_bf16_f32 v20, v20, v21\n\t"
    "v_cvt_pk_bf16_f32 v21, v22, v23\n\t"
    "v_mfma_f32_16x16x32_bf16 v[36:39], a[248:251], v[228:231], v[36:39]\n\t"
    "v_mul_f32 v24, v24, v10\n\t"
    "v_mul_f32 v25, v25, v10\n\t"
    "v_mfma_f32_16x16x32_bf16 v[36:39], a[252:255], v[252:255], v[36:39]\n\t"
    "s_waitcnt lgkmcnt(0)\n\t"
    "ds_read_b64_tr_b16 a[248:249], v0 offset:14336\n\t"
    "v_mul_f32 v26, v26, v10\n\t"
    "v_mfma_f32_16x16x32_bf16 v[40:43], a[240:243], v[64:67], 0\n\t"
    "ds_read_b64_tr_b16 a[250:251], v0 offset:20480\n\t"
    "v_mul_f32 v27, v27, v10\n\t"
    "v_mfma_f32_16x16x32_bf16 v[40:43], a[244:247], a[144:147], v[40:43]\n\t"
    "ds_read_b64_tr_b16 a[252:253], v0 offset:24576\n\t"
    "v_cvt_pk_bf16_f32 v24, v24, v25\n\t"
    "v_mfma_f32_16x16x32_bf16 v[44:47], a[240:243], v[68:71], 0\n\t"
    "ds_read_b64_tr_b16 a[254:255], v0 offset:30720\n\t"
    "v_cvt_pk_bf16_f32 v25, v26, v27\n\t"
    "v_mfma_f32_16x16x32_bf16 v[44:47], a[244:247], a[148:151], v[44:47]\n\t"
    "ds_write_b64 v14, a[132:133] offset:0\n\t"
    "v_mfma_f32_16x16x32_bf16 v[48:51], a[240:243], v[72:75], 0\n\t"
    "v_mul_f32 v28, v28, v11\n\t"
    "v_mul_f32 v29, v29, v11\n\t"
    "v_mfma_f32_16x16x32_bf16 v[48:51], a[244:247], a[152:155], v[48:51]\n\t"
    "buffer_load_dwordx4 v1, s[44:47], s53 offen nt lds\n\t"
    "v_mfma_f32_16x16x32_bf16 v[52:55], a[240:243], v[76:79], 0\n\t"
    "s_add_u32 s53, s75, 256\n\t"
    "v_mul_f32 v30, v30, v11\n\t"
    "v_mul_f32 v31, v31, v11\n\t"
    "v_mfma_f32_16x16x32_bf16 v[52:55], a[244:247], a[156:159], v[52:55]\n\t"
    "s_addk_i32 m0, 1024\n\t"
    "ds_write_b64 v14, a[134:135] offset:256\n\t"
    "v_mfma_f32_16x16x32_bf16 v[56:59], a[240:243], v[80:83], 0\n\t"
    "v_cvt_pk_bf16_f32 v28, v28, v29\n\t"
    "v_cvt_pk_bf16_f32 v29, v30, v31\n\t"
    "v_mfma_f32_16x16x32_bf16 v[56:59], a[244:247], a[160:163], v[56:59]\n\t"
    "v_mul_f32 v32, v32, v12\n\t"
    "v_mul_f32 v33, v33, v12\n\t"
    "v_mfma_f32_16x16x32_bf16 v[60:63], a[240:243], v[84:87], 0\n\t"
    "v_mul_f32 v34, v34, v12\n\t"
    "v_mul_f32 v35, v35, v12\n\t"
    "v_mfma_f32_16x16x32_bf16 v[60:63], a[244:247], a[164:167], v[60:63]\n\t"
    "s_waitcnt lgkmcnt(2)\n\t"
    "ds_read_b64_tr_b16 a[240:241], v0 offset:25600\n\t"
    "v_cvt_pk_bf16_f32 v32, v32, v33\n\t"
    "v_mfma_f32_16x16x32_bf16 v[40:43], a[248:251], v[88:91], v[40:43]\n\t"
    "ds_read_b64_tr_b16 a[242:243], v0 offset:31744\n\t"
    "v_cvt_pk_bf16_f32 v33, v34, v35\n\t"
    "v_mfma_f32_16x16x32_bf16 v[40:43], a[252:255], a[168:171], v[40:43]\n\t"
    "ds_read_b64_tr_b16 a[244:245], v0 offset:26624\n\t"
    "v_mul_f32 v36, v36, v13\n\t"
    "v_mfma_f32_16x16x32_bf16 v[44:47], a[248:251], v[92:95], v[44:47]\n\t"
    "ds_read_b64_tr_b16 a[246:247], v0 offset:32768\n\t"
    "v_mul_f32 v37, v37, v13\n\t"
    "v_mfma_f32_16x16x32_bf16 v[44:47], a[252:255], a[172:175], v[44:47]\n\t"
    "ds_write_b64 v14, v[16:17] offset:1152\n\t"
    "v_mfma_f32_16x16x32_bf16 v[48:51], a[248:251], v[96:99], v[48:51]\n\t"
    "v_mul_f32 v38, v38, v13\n\t"
    "v_mul_f32 v39, v39, v13\n\t"
    "v_mfma_f32_16x16x32_bf16 v[48:51], a[252:255], a[176:179], v[48:51]\n\t"
    "buffer_load_dwordx4 v1, s[44:47], s53 offen nt lds\n\t"
    "v_mfma_f32_16x16x32_bf16 v[52:55], a[248:251], v[100:103], v[52:55]\n\t"
    "s_mov_b32 s53, s90\n\t"
    "v_cvt_pk_bf16_f32 v36, v36, v37\n\t"
    "v_cvt_pk_bf16_f32 v37, v38, v39\n\t"
    "v_mfma_f32_16x16x32_bf16 v[52:55], a[252:255], a[180:183], v[52:55]\n\t"
    "s_add_u32 m0, s52, 98304\n\t"
    "ds_write_b64 v14, v[20:21] offset:1408\n\t"
    "v_mfma_f32_16x16x32_bf16 v[56:59], a[248:251], v[104:107], v[56:59]\n\t"
    "ds_read_b128 a[96:99], v15 offset:0\n\t"
    "ds_read_b128 a[100:103], v15 offset:128\n\t"
    "v_mfma_f32_16x16x32_bf16 v[56:59], a[252:255], a[184:187], v[56:59]\n\t"
    "ds_read_b128 a[104:107], v15 offset:256\n\t"
    "ds_read_b128 a[108:111], v15 offset:384\n\t"
    "v_mfma_f32_16x16x32_bf16 v[60:63], a[248:251], v[108:111], v[60:63]\n\t"
    "v_mfma_f32_16x16x32_bf16 v[60:63], a[252:255], a[188:191], v[60:63]\n\t"
    "s_waitcnt lgkmcnt(6)\n\t"
    "ds_read_b64_tr_b16 a[248:249], v0 offset:49152\n\t"
    "v_mfma_f32_16x16x32_bf16 v[40:43], a[240:243], v[112:115], v[40:43]\n\t"
    "ds_read_b64_tr_b16 a[250:251], v0 offset:55296\n\t"
    "v_mfma_f32_16x16x32_bf16 v[40:43], a[244:247], a[192:195], v[40:43]\n\t"
    "ds_read_b64_tr_b16 a[252:253], v0 offset:50176\n\t"
    "v_mfma_f32_16x16x32_bf16 v[44:47], a[240:243], v[116:119], v[44:47]\n\t"
    "ds_read_b64_tr_b16 a[254:255], v0 offset:56320\n\t"
    "v_mfma_f32_16x16x32_bf16 v[44:47], a[244:247], a[196:199], v[44:47]\n\t"
    "ds_write_b64 v14, a[136:137] offset:2304\n\t"
    "v_mfma_f32_16x16x32_bf16 v[48:51], a[240:243], v[120:123], v[48:51]\n\t"
    "v_mfma_f32_16x16x32_bf16 v[48:51], a[244:247], a[200:203], v[48:51]\n\t"
    "ds_write_b64 v14, a[138:139] offset:2560\n\t"
    "v_mfma_f32_16x16x32_bf16 v[52:55], a[240:243], v[124:127], v[52:55]\n\t"
    "v_mfma_f32_16x16x32_bf16 v[52:55], a[244:247], a[204:207], v[52:55]\n\t"
    "buffer_load_dwordx4 v1, s[36:39], s53 offen nt lds\n\t"
    "v_mfma_f32_16x16x32_bf16 v[56:59], a[240:243], v[128:131], v[56:59]\n\t"
    "s_addk_i32 m0, 896\n\t"
    "v_mfma_f32_16x16x32_bf16 v[56:59], a[244:247], a[208:211], v[56:59]\n\t"
    "ds_write_b64 v14, v[24:25] offset:3456\n\t"
    "v_mfma_f32_16x16x32_bf16 v[60:63], a[240:243], v[132:135], v[60:63]\n\t"
    "v_mfma_f32_16x16x32_bf16 v[60:63], a[244:247], a[212:215], v[60:63]\n\t"
    "s_waitcnt lgkmcnt(3)\n\t"
    "ds_read_b64_tr_b16 a[240:241], v0 offset:51200\n\t"
    "v_mfma_f32_16x16x32_bf16 v[40:43], a[248:251], v[136:139], v[40:43]\n\t"
    "ds_read_b64_tr_b16 a[242:243], v0 offset:57344\n\t"
    "v_mfma_f32_16x16x32_bf16 v[40:43], a[252:255], a[216:219], v[40:43]\n\t"
    "ds_read_b64_tr_b16 a[244:245], v0 offset:0\n\t"
    "v_mfma_f32_16x16x32_bf16 v[44:47], a[248:251], v[140:143], v[44:47]\n\t"
    "ds_read_b64_tr_b16 a[246:247], v0 offset:6144\n\t"
    "v_mfma_f32_16x16x32_bf16 v[44:47], a[252:255], a[220:223], v[44:47]\n\t"
    "ds_write_b64 v14, v[28:29] offset:3712\n\t"
    "v_mfma_f32_16x16x32_bf16 v[48:51], a[248:251], v[144:147], v[48:51]\n\t"
    "ds_read_b128 a[112:115], v15 offset:2304\n\t"
    "ds_read_b128 a[116:119], v15 offset:2432\n\t"
    "v_mfma_f32_16x16x32_bf16 v[48:51], a[252:255], a[224:227], v[48:51]\n\t"
    "ds_read_b128 a[120:123], v15 offset:2560\n\t"
    "ds_read_b128 a[124:127], v15 offset:2688\n\t"
    "v_mfma_f32_16x16x32_bf16 v[52:55], a[248:251], v[148:151], v[52:55]\n\t"
    "v_mfma_f32_16x16x32_bf16 v[52:55], a[252:255], a[228:231], v[52:55]\n\t"
    "buffer_load_dwordx4 v1, s[36:39], s53 offen offset:128 nt lds\n\t"
    "v_mfma_f32_16x16x32_bf16 v[56:59], a[248:251], v[152:155], v[56:59]\n\t"
    "s_addk_i32 m0, 896\n\t"
    "v_mfma_f32_16x16x32_bf16 v[56:59], a[252:255], a[232:235], v[56:59]\n\t"
    "ds_write_b64 v14, a[140:141] offset:4608\n\t"
    "v_mfma_f32_16x16x32_bf16 v[60:63], a[248:251], v[156:159], v[60:63]\n\t"
    "v_mfma_f32_16x16x32_bf16 v[60:63], a[252:255], a[236:239], v[60:63]\n\t"
    "s_waitcnt lgkmcnt(6)\n\t"
    "ds_read_b64_tr_b16 a[248:249], v0 offset:1024\n\t"
    "v_mfma_f32_16x16x32_bf16 v[40:43], a[240:243], v[160:163], v[40:43]\n\t"
    "ds_read_b64_tr_b16 a[250:251], v0 offset:7168\n\t"
    "v_mfma_f32_16x16x32_bf16 v[40:43], a[244:247], v[184:187], v[40:43]\n\t"
    "ds_read_b64_tr_b16 a[252:253], v0 offset:2048\n\t"
    "v_mfma_f32_16x16x32_bf16 v[44:47], a[240:243], v[164:167], v[44:47]\n\t"
    "ds_read_b64_tr_b16 a[254:255], v0 offset:8192\n\t"
    "v_mfma_f32_16x16x32_bf16 v[44:47], a[244:247], v[188:191], v[44:47]\n\t"
    "ds_write_b64 v14, a[142:143] offset:4864\n\t"
    "v_mfma_f32_16x16x32_bf16 v[48:51], a[240:243], v[168:171], v[48:51]\n\t"
    "v_mfma_f32_16x16x32_bf16 v[48:51], a[244:247], v[192:195], v[48:51]\n\t"
    "ds_write_b64 v14, v[32:33] offset:5760\n\t"
    "v_mfma_f32_16x16x32_bf16 v[52:55], a[240:243], v[172:175], v[52:55]\n\t"
    "v_mfma_f32_16x16x32_bf16 v[52:55], a[244:247], v[196:199], v[52:55]\n\t"
    "buffer_load_dwordx4 v1, s[36:39], s53 offen offset:256 nt lds\n\t"
    "v_mfma_f32_16x16x32_bf16 v[56:59], a[240:243], v[176:179], v[56:59]\n\t"
    "s_mov_b32 s53, s91\n\t"
    "v_mfma_f32_16x16x32_bf16 v[56:59], a[244:247], v[200:203], v[56:59]\n\t"
    "s_add_u32 m0, s52, 110592\n\t"
    "ds_write_b64 v14, v[36:37] offset:6016\n\t"
    "v_mfma_f32_16x16x32_bf16 v[60:63], a[240:243], v[180:183], v[60:63]\n\t"
    "ds_read_b128 a[128:131], v15 offset:4608\n\t"
    "ds_read_b128 a[132:135], v15 offset:4736\n\t"
    "v_mfma_f32_16x16x32_bf16 v[60:63], a[244:247], v[204:207], v[60:63]\n\t"
    "s_waitcnt lgkmcnt(5)\n\t"
    "v_xor_b32 v0, 32, v0\n\t"
    "ds_read_b64_tr_b16 a[240:241], v0 offset:12288\n\t"
    "v_mfma_f32_16x16x32_bf16 v[40:43], a[248:251], v[208:211], v[40:43]\n\t"
    "ds_read_b64_tr_b16 a[242:243], v0 offset:18432\n\t"
    "ds_read_b128 a[136:139], v15 offset:4864\n\t"
    "v_mfma_f32_16x16x32_bf16 v[40:43], a[252:255], v[232:235], v[40:43]\n\t"
    "ds_read_b64_tr_b16 a[244:245], v0 offset:13312\n\t"
    "ds_read_b128 a[140:143], v15 offset:4992\n\t"
    "v_mfma_f32_16x16x32_bf16 v[44:47], a[248:251], v[212:215], v[44:47]\n\t"
    "ds_read_b64_tr_b16 a[246:247], v0 offset:19456\n\t"
    "v_mfma_f32_16x16x32_bf16 v[44:47], a[252:255], v[236:239], v[44:47]\n\t"
    "s_mov_b32 s54, 24576\n\t"
    "v_mfma_f32_16x16x32_bf16 v[48:51], a[248:251], v[216:219], v[48:51]\n\t"
    "v_mfma_f32_16x16x32_bf16 v[48:51], a[252:255], v[240:243], v[48:51]\n\t"
    "v_mul_f32 v40, v40, v8\n\t"
    "v_mul_f32 v41, v41, v8\n\t"
    "v_mul_f32 v42, v42, v8\n\t"
    "v_mul_f32 v43, v43, v8\n\t"
    "v_cvt_pk_bf16_f32 v40, v40, v41\n\t"
    "v_cvt_pk_bf16_f32 v41, v42, v43\n\t"
    "v_mfma_f32_16x16x32_bf16 v[52:55], a[248:251], v[220:223], v[52:55]\n\t"
    "v_mfma_f32_16x16x32_bf16 v[52:55], a[252:255], v[244:247], v[52:55]\n\t"
    "v_mfma_f32_16x16x32_bf16 v[56:59], a[248:251], v[224:227], v[56:59]\n\t"
    "v_mfma_f32_16x16x32_bf16 v[56:59], a[252:255], v[248:251], v[56:59]\n\t"
    "v_mfma_f32_16x16x32_bf16 v[60:63], a[248:251], v[228:231], v[60:63]\n\t"
    "v_mfma_f32_16x16x32_bf16 v[60:63], a[252:255], v[252:255], v[60:63]\n\t"
    "s_waitcnt lgkmcnt(0)\n\t"
    "ds_read_b64_tr_b16 a[248:249], v0 offset:14336\n\t"
    "v_mfma_f32_16x16x32_bf16 v[16:19], a[240:243], v[64:67], 0\n\t"
    "ds_read_b64_tr_b16 a[250:251], v0 offset:20480\n\t"
    "v_mfma_f32_16x16x32_bf16 v[16:19], a[244:247], a[144:147], v[16:19]\n\t"
    "ds_read_b64_tr_b16 a[252:253], v0 offset:24576\n\t"
    "v_mfma_f32_16x16x32_bf16 v[20:23], a[240:243], v[68:71], 0\n\t"
    "ds_read_b64_tr_b16 a[254:255], v0 offset:30720\n\t"
    "v_mfma_f32_16x16x32_bf16 v[20:23], a[244:247], a[148:151], v[20:23]\n\t"
    "buffer_store_dwordx4 a[128:131], v3, s[48:51], s54 offen offset:0 sc1\n\t"
    "v_mfma_f32_16x16x32_bf16 v[24:27], a[240:243], v[72:75], 0\n\t"
    "v_mfma_f32_16x16x32_bf16 v[24:27], a[244:247], a[152:155], v[24:27]\n\t"
    "ds_write_b64 v14, v[40:41] offset:2304\n\t"
    "v_mul_f32 v44, v44, v9\n\t"
    "v_mul_f32 v45, v45, v9\n\t"
    "v_mul_f32 v46, v46, v9\n\t"
    "v_mul_f32 v47, v47, v9\n\t"
    "v_mfma_f32_16x16x32_bf16 v[28:31], a[240:243], v[76:79], 0\n\t"
    "v_cvt_pk_bf16_f32 v44, v44, v45\n\t"
    "v_cvt_pk_bf16_f32 v45, v46, v47\n\t"
    "v_mfma_f32_16x16x32_bf16 v[28:31], a[244:247], a[156:159], v[28:31]\n\t"
    "v_mfma_f32_16x16x32_bf16 v[32:35], a[240:243], v[80:83], 0\n\t"
    "v_mfma_f32_16x16x32_bf16 v[32:35], a[244:247], a[160:163], v[32:35]\n\t"
    "v_mfma_f32_16x16x32_bf16 v[36:39], a[240:243], v[84:87], 0\n\t"
    "v_mfma_f32_16x16x32_bf16 v[36:39], a[244:247], a[164:167], v[36:39]\n\t"
    "s_waitcnt lgkmcnt(1)\n\t"
    "ds_read_b64_tr_b16 a[240:241], v0 offset:25600\n\t"
    "v_mfma_f32_16x16x32_bf16 v[16:19], a[248:251], v[88:91], v[16:19]\n\t"
    "ds_read_b64_tr_b16 a[242:243], v0 offset:31744\n\t"
    "v_mfma_f32_16x16x32_bf16 v[16:19], a[252:255], a[168:171], v[16:19]\n\t"
    "ds_read_b64_tr_b16 a[244:245], v0 offset:26624\n\t"
    "v_mfma_f32_16x16x32_bf16 v[20:23], a[248:251], v[92:95], v[20:23]\n\t"
    "ds_read_b64_tr_b16 a[246:247], v0 offset:32768\n\t"
    "v_mfma_f32_16x16x32_bf16 v[20:23], a[252:255], a[172:175], v[20:23]\n\t"
    "buffer_store_dwordx4 a[132:135], v3, s[48:51], s54 offen offset:3072 sc1\n\t"
    "v_mfma_f32_16x16x32_bf16 v[24:27], a[248:251], v[96:99], v[24:27]\n\t"
    "v_mfma_f32_16x16x32_bf16 v[24:27], a[252:255], a[176:179], v[24:27]\n\t"
    "ds_write_b64 v14, v[44:45] offset:2560\n\t"
    "v_mul_f32 v48, v48, v10\n\t"
    "v_mfma_f32_16x16x32_bf16 v[28:31], a[248:251], v[100:103], v[28:31]\n\t"
    "v_mul_f32 v49, v49, v10\n\t"
    "v_mul_f32 v50, v50, v10\n\t"
    "v_mfma_f32_16x16x32_bf16 v[28:31], a[252:255], a[180:183], v[28:31]\n\t"
    "v_mul_f32 v51, v51, v10\n\t"
    "v_cvt_pk_bf16_f32 v48, v48, v49\n\t"
    "v_mfma_f32_16x16x32_bf16 v[32:35], a[248:251], v[104:107], v[32:35]\n\t"
    "v_cvt_pk_bf16_f32 v49, v50, v51\n\t"
    "v_mfma_f32_16x16x32_bf16 v[32:35], a[252:255], a[184:187], v[32:35]\n\t"
    "v_mfma_f32_16x16x32_bf16 v[36:39], a[248:251], v[108:111], v[36:39]\n\t"
    "v_mfma_f32_16x16x32_bf16 v[36:39], a[252:255], a[188:191], v[36:39]\n\t"
    "s_waitcnt lgkmcnt(1)\n\t"
    "ds_read_b64_tr_b16 a[248:249], v0 offset:49152\n\t"
    "v_mfma_f32_16x16x32_bf16 v[16:19], a[240:243], v[112:115], v[16:19]\n\t"
    "ds_read_b64_tr_b16 a[250:251], v0 offset:55296\n\t"
    "v_mfma_f32_16x16x32_bf16 v[16:19], a[244:247], a[192:195], v[16:19]\n\t"
    "ds_read_b64_tr_b16 a[252:253], v0 offset:50176\n\t"
    "v_mfma_f32_16x16x32_bf16 v[20:23], a[240:243], v[116:119], v[20:23]\n\t"
    "ds_read_b64_tr_b16 a[254:255], v0 offset:56320\n\t"
    "v_mfma_f32_16x16x32_bf16 v[20:23], a[244:247], a[196:199], v[20:23]\n\t"
    "buffer_store_dwordx4 a[136:139], v3, s[48:51], s65 offen offset:0 sc1\n\t"
    "v_mfma_f32_16x16x32_bf16 v[24:27], a[240:243], v[120:123], v[24:27]\n\t"
    "v_mfma_f32_16x16x32_bf16 v[24:27], a[244:247], a[200:203], v[24:27]\n\t"
    "ds_write_b64 v14, v[48:49] offset:4608\n\t"
    "v_mfma_f32_16x16x32_bf16 v[28:31], a[240:243], v[124:127], v[28:31]\n\t"
    "v_mul_f32 v52, v52, v11\n\t"
    "v_mul_f32 v53, v53, v11\n\t"
    "v_mfma_f32_16x16x32_bf16 v[28:31], a[244:247], a[204:207], v[28:31]\n\t"
    "v_mul_f32 v54, v54, v11\n\t"
    "v_mul_f32 v55, v55, v11\n\t"
    "v_mfma_f32_16x16x32_bf16 v[32:35], a[240:243], v[128:131], v[32:35]\n\t"
    "v_cvt_pk_bf16_f32 v52, v52, v53\n\t"
    "v_cvt_pk_bf16_f32 v53, v54, v55\n\t"
    "v_mfma_f32_16x16x32_bf16 v[32:35], a[244:247], a[208:211], v[32:35]\n\t"
    "v_mfma_f32_16x16x32_bf16 v[36:39], a[240:243], v[132:135], v[36:39]\n\t"
    "v_mfma_f32_16x16x32_bf16 v[36:39], a[244:247], a[212:215], v[36:39]\n\t"
    "s_waitcnt lgkmcnt(1)\n\t"
    "ds_read_b64_tr_b16 a[240:241], v0 offset:51200\n\t"
    "v_mfma_f32_16x16x32_bf16 v[16:19], a[248:251], v[136:139], v[16:19]\n\t"
    "ds_read_b64_tr_b16 a[242:243], v0 offset:57344\n\t"
    "v_mfma_f32_16x16x32_bf16 v[16:19], a[252:255], a[216:219], v[16:19]\n\t"
    "ds_read_b64_tr_b16 a[244:245], v0 offset:0\n\t"
    "v_mfma_f32_16x16x32_bf16 v[20:23], a[248:251], v[140:143], v[20:23]\n\t"
    "ds_read_b64_tr_b16 a[246:247], v0 offset:6144\n\t"
    "v_mfma_f32_16x16x32_bf16 v[20:23], a[252:255], a[220:223], v[20:23]\n\t"
    "buffer_load_dwordx4 v1, s[36:39], s53 offen nt lds\n\t"
    "v_mfma_f32_16x16x32_bf16 v[24:27], a[248:251], v[144:147], v[24:27]\n\t"
    "s_addk_i32 m0, 896\n\t"
    "v_mfma_f32_16x16x32_bf16 v[24:27], a[252:255], a[224:227], v[24:27]\n\t"
    "ds_write_b64 v14, v[52:53] offset:4864\n\t"
    "v_mfma_f32_16x16x32_bf16 v[28:31], a[248:251], v[148:151], v[28:31]\n\t"
    "v_mul_f32 v56, v56, v12\n\t"
    "v_mul_f32 v57, v57, v12\n\t"
    "v_mfma_f32_16x16x32_bf16 v[28:31], a[252:255], a[228:231], v[28:31]\n\t"
    "v_mul_f32 v58, v58, v12\n\t"
    "v_mul_f32 v59, v59, v12\n\t"
    "v_mfma_f32_16x16x32_bf16 v[32:35], a[248:251], v[152:155], v[32:35]\n\t"
    "v_cvt_pk_bf16_f32 v56, v56, v57\n\t"
    "v_cvt_pk_bf16_f32 v57, v58, v59\n\t"
    "v_mfma_f32_16x16x32_bf16 v[32:35], a[252:255], a[232:235], v[32:35]\n\t"
    "v_mfma_f32_16x16x32_bf16 v[36:39], a[248:251], v[156:159], v[36:39]\n\t"
    "v_mfma_f32_16x16x32_bf16 v[36:39], a[252:255], a[236:239], v[36:39]\n\t"
    "s_waitcnt lgkmcnt(1)\n\t"
    "ds_read_b64_tr_b16 a[248:249], v0 offset:1024\n\t"
    "v_mfma_f32_16x16x32_bf16 v[16:19], a[240:243], v[160:163], v[16:19]\n\t"
    "ds_read_b64_tr_b16 a[250:251], v0 offset:7168\n\t"
    "v_mfma_f32_16x16x32_bf16 v[16:19], a[244:247], v[184:187], v[16:19]\n\t"
    "ds_read_b64_tr_b16 a[252:253], v0 offset:2048\n\t"
    "v_mfma_f32_16x16x32_bf16 v[20:23], a[240:243], v[164:167], v[20:23]\n\t"
    "ds_read_b64_tr_b16 a[254:255], v0 offset:8192\n\t"
    "v_mfma_f32_16x16x32_bf16 v[20:23], a[244:247], v[188:191], v[20:23]\n\t"
    "buffer_store_dwordx4 a[140:143], v3, s[48:51], s75 offen offset:0 sc1\n\t"
    "v_mfma_f32_16x16x32_bf16 v[24:27], a[240:243], v[168:171], v[24:27]\n\t"
    "s_mov_b32 s54, 0\n\t"
    "v_mfma_f32_16x16x32_bf16 v[24:27], a[244:247], v[192:195], v[24:27]\n\t"
    "ds_write_b64 v14, v[56:57] offset:6912\n\t"
    "v_mfma_f32_16x16x32_bf16 v[28:31], a[240:243], v[172:175], v[28:31]\n\t"
    "v_mul_f32 v60, v60, v13\n\t"
    "v_mul_f32 v61, v61, v13\n\t"
    "v_mfma_f32_16x16x32_bf16 v[28:31], a[244:247], v[196:199], v[28:31]\n\t"
    "v_mul_f32 v62, v62, v13\n\t"
    "v_mul_f32 v63, v63, v13\n\t"
    "v_mfma_f32_16x16x32_bf16 v[32:35], a[240:243], v[176:179], v[32:35]\n\t"
    "v_cvt_pk_bf16_f32 v60, v60, v61\n\t"
    "v_cvt_pk_bf16_f32 v61, v62, v63\n\t"
    "v_mfma_f32_16x16x32_bf16 v[32:35], a[244:247], v[200:203], v[32:35]\n\t"
    "v_mfma_f32_16x16x32_bf16 v[36:39], a[240:243], v[180:183], v[36:39]\n\t"
    "v_mfma_f32_16x16x32_bf16 v[36:39], a[244:247], v[204:207], v[36:39]\n\t"
    "s_waitcnt lgkmcnt(1)\n\t"
    "v_xor_b32 v0, 32, v0\n\t"
    "ds_read_b64_tr_b16 a[240:241], v0 offset:12800\n\t"
    "v_mfma_f32_16x16x32_bf16 v[16:19], a[248:251], v[208:211], v[16:19]\n\t"
    "ds_read_b64_tr_b16 a[242:243], v0 offset:18944\n\t"
    "v_mfma_f32_16x16x32_bf16 v[16:19], a[252:255], v[232:235], v[16:19]\n\t"
    "ds_read_b64_tr_b16 a[244:245], v0 offset:13824\n\t"
    "v_mfma_f32_16x16x32_bf16 v[20:23], a[248:251], v[212:215], v[20:23]\n\t"
    "ds_read_b64_tr_b16 a[246:247], v0 offset:19968\n\t"
    "v_mfma_f32_16x16x32_bf16 v[20:23], a[252:255], v[236:239], v[20:23]\n\t"
    "buffer_store_dwordx4 a[96:99], v3, s[48:51], s54 offen offset:0 sc1\n\t"
    "v_mfma_f32_16x16x32_bf16 v[24:27], a[248:251], v[216:219], v[24:27]\n\t"
    "v_mfma_f32_16x16x32_bf16 v[24:27], a[252:255], v[240:243], v[24:27]\n\t"
    "ds_write_b64 v14, v[60:61] offset:7168\n\t"
    "v_mfma_f32_16x16x32_bf16 v[28:31], a[248:251], v[220:223], v[28:31]\n\t"
    "v_mul_f32 v16, v16, v8\n\t"
    "v_mul_f32 v17, v17, v8\n\t"
    "v_mfma_f32_16x16x32_bf16 v[28:31], a[252:255], v[244:247], v[28:31]\n\t"
    "v_mul_f32 v18, v18, v8\n\t"
    "v_mul_f32 v19, v19, v8\n\t"
    "v_mfma_f32_16x16x32_bf16 v[32:35], a[248:251], v[224:227], v[32:35]\n\t"
    "v_cvt_pk_bf16_f32 v16, v16, v17\n\t"
    "v_cvt_pk_bf16_f32 v17, v18, v19\n\t"
    "v_mfma_f32_16x16x32_bf16 v[32:35], a[252:255], v[248:251], v[32:35]\n\t"
    "buffer_store_dwordx4 a[100:103], v3, s[48:51], s54 offen offset:3072 sc1\n\t"
    "v_mfma_f32_16x16x32_bf16 v[36:39], a[248:251], v[228:231], v[36:39]\n\t"
    "s_mov_b32 s54, 6144\n\t"
    "v_mfma_f32_16x16x32_bf16 v[36:39], a[252:255], v[252:255], v[36:39]\n\t"
    "s_waitcnt lgkmcnt(1)\n\t"
    "ds_read_b64_tr_b16 a[248:249], v0 offset:14848\n\t"
    "v_mfma_f32_16x16x32_bf16 v[40:43], a[240:243], v[64:67], 0\n\t"
    "ds_read_b64_tr_b16 a[250:251], v0 offset:20992\n\t"
    "v_mfma_f32_16x16x32_bf16 v[40:43], a[244:247], a[144:147], v[40:43]\n\t"
    "ds_read_b64_tr_b16 a[252:253], v0 offset:25088\n\t"
    "v_mfma_f32_16x16x32_bf16 v[44:47], a[240:243], v[68:71], 0\n\t"
    "ds_read_b64_tr_b16 a[254:255], v0 offset:31232\n\t"
    "v_mfma_f32_16x16x32_bf16 v[44:47], a[244:247], a[148:151], v[44:47]\n\t"
    "v_mfma_f32_16x16x32_bf16 v[48:51], a[240:243], v[72:75], 0\n\t"
    "v_mfma_f32_16x16x32_bf16 v[48:51], a[244:247], a[152:155], v[48:51]\n\t"
    "ds_write_b64 v14, v[16:17] offset:3456\n\t"
    "v_mfma_f32_16x16x32_bf16 v[52:55], a[240:243], v[76:79], 0\n\t"
    "v_mul_f32 v20, v20, v9\n\t"
    "v_mul_f32 v21, v21, v9\n\t"
    "v_mfma_f32_16x16x32_bf16 v[52:55], a[244:247], a[156:159], v[52:55]\n\t"
    "v_mul_f32 v22, v22, v9\n\t"
    "v_mul_f32 v23, v23, v9\n\t"
    "v_mfma_f32_16x16x32_bf16 v[56:59], a[240:243], v[80:83], 0\n\t"
    "v_cvt_pk_bf16_f32 v20, v20, v21\n\t"
    "v_cvt_pk_bf16_f32 v21, v22, v23\n\t"
    "v_mfma_f32_16x16x32_bf16 v[56:59], a[244:247], a[160:163], v[56:59]\n\t"
    "buffer_load_dwordx4 v1, s[36:39], s53 offen offset:128 nt lds\n\t"
    "v_mfma_f32_16x16x32_bf16 v[60:63], a[240:243], v[84:87], 0\n\t"
    "s_addk_i32 m0, 896\n\t"
    "v_mfma_f32_16x16x32_bf16 v[60:63], a[244:247], a[164:167], v[60:63]\n\t"
    "s_waitcnt lgkmcnt(1)\n\t"
    "ds_read_b64_tr_b16 a[240:241], v0 offset:26112\n\t"
    "v_mfma_f32_16x16x32_bf16 v[40:43], a[248:251], v[88:91], v[40:43]\n\t"
    "ds_read_b64_tr_b16 a[242:243], v0 offset:32256\n\t"
    "v_mfma_f32_16x16x32_bf16 v[40:43], a[252:255], a[168:171], v[40:43]\n\t"
    "ds_read_b64_tr_b16 a[244:245], v0 offset:27136\n\t"
    "v_mfma_f32_16x16x32_bf16 v[44:47], a[248:251], v[92:95], v[44:47]\n\t"
    "ds_read_b64_tr_b16 a[246:247], v0 offset:33280\n\t"
    "v_mfma_f32_16x16x32_bf16 v[44:47], a[252:255], a[172:175], v[44:47]\n\t"
    "v_mfma_f32_16x16x32_bf16 v[48:51], a[248:251], v[96:99], v[48:51]\n\t"
    "v_mfma_f32_16x16x32_bf16 v[48:51], a[252:255], a[176:179], v[48:51]\n\t"
    "ds_write_b64 v14, v[20:21] offset:3712\n\t"
    "v_mfma_f32_16x16x32_bf16 v[52:55], a[248:251], v[100:103], v[52:55]\n\t"
    "v_mul_f32 v24, v24, v10\n\t"
    "v_mul_f32 v25, v25, v10\n\t"
    "v_mfma_f32_16x16x32_bf16 v[52:55], a[252:255], a[180:183], v[52:55]\n\t"
    "v_mul_f32 v26, v26, v10\n\t"
    "v_mul_f32 v27, v27, v10\n\t"
    "v_mfma_f32_16x16x32_bf16 v[56:59], a[248:251], v[104:107], v[56:59]\n\t"
    "v_cvt_pk_bf16_f32 v24, v24, v25\n\t"
    "v_cvt_pk_bf16_f32 v25, v26, v27\n\t"
    "v_mfma_f32_16x16x32_bf16 v[56:59], a[252:255], a[184:187], v[56:59]\n\t"
    "buffer_store_dwordx4 a[104:107], v3, s[48:51], s54 offen offset:0 sc1\n\t"
    "v_mfma_f32_16x16x32_bf16 v[60:63], a[248:251], v[108:111], v[60:63]\n\t"
    "v_mfma_f32_16x16x32_bf16 v[60:63], a[252:255], a[188:191], v[60:63]\n\t"
    "s_waitcnt lgkmcnt(1)\n\t"
    "ds_read_b64_tr_b16 a[248:249], v0 offset:49664\n\t"
    "v_mfma_f32_16x16x32_bf16 v[40:43], a[240:243], v[112:115], v[40:43]\n\t"
    "ds_read_b64_tr_b16 a[250:251], v0 offset:55808\n\t"
    "v_mfma_f32_16x16x32_bf16 v[40:43], a[244:247], a[192:195], v[40:43]\n\t"
    "ds_read_b64_tr_b16 a[252:253], v0 offset:50688\n\t"
    "v_mfma_f32_16x16x32_bf16 v[44:47], a[240:243], v[116:119], v[44:47]\n\t"
    "ds_read_b64_tr_b16 a[254:255], v0 offset:56832\n\t"
    "v_mfma_f32_16x16x32_bf16 v[44:47], a[244:247], a[196:199], v[44:47]\n\t"
    "ds_write_b64 v14, v[24:25] offset:5760\n\t"
    "v_mfma_f32_16x16x32_bf16 v[48:51], a[240:243], v[120:123], v[48:51]\n\t"
    "v_mul_f32 v28, v28, v11\n\t"
    "v_mul_f32 v29, v29, v11\n\t"
    "v_mfma_f32_16x16x32_bf16 v[48:51], a[244:247], a[200:203], v[48:51]\n\t"
    "v_mul_f32 v30, v30, v11\n\t"
    "v_mul_f32 v31, v31, v11\n\t"
    "v_mfma_f32_16x16x32_bf16 v[52:55], a[240:243], v[124:127], v[52:55]\n\t"
    "v_cvt_pk_bf16_f32 v28, v28, v29\n\t"
    "v_cvt_pk_bf16_f32 v29, v30, v31\n\t"
    "v_mfma_f32_16x16x32_bf16 v[52:55], a[244:247], a[204:207], v[52:55]\n\t"
    "v_mfma_f32_16x16x32_bf16 v[56:59], a[240:243], v[128:131], v[56:59]\n\t"
    "v_mfma_f32_16x16x32_bf16 v[56:59], a[244:247], a[208:211], v[56:59]\n\t"
    "buffer_store_dwordx4 a[108:111], v3, s[48:51], s54 offen offset:3072 sc1\n\t"
    "v_mfma_f32_16x16x32_bf16 v[60:63], a[240:243], v[132:135], v[60:63]\n\t"
    "s_mov_b32 s54, 12288\n\t"
    "v_mfma_f32_16x16x32_bf16 v[60:63], a[244:247], a[212:215], v[60:63]\n\t"
    "s_waitcnt lgkmcnt(1)\n\t"
    "ds_read_b64_tr_b16 a[240:241], v0 offset:51712\n\t"
    "v_mfma_f32_16x16x32_bf16 v[40:43], a[248:251], v[136:139], v[40:43]\n\t"
    "ds_read_b64_tr_b16 a[242:243], v0 offset:57856\n\t"
    "v_mfma_f32_16x16x32_bf16 v[40:43], a[252:255], a[216:219], v[40:43]\n\t"
    "ds_read_b64_tr_b16 a[244:245], v0 offset:512\n\t"
    "v_mfma_f32_16x16x32_bf16 v[44:47], a[248:251], v[140:143], v[44:47]\n\t"
    "ds_read_b64_tr_b16 a[246:247], v0 offset:6656\n\t"
    "v_mfma_f32_16x16x32_bf16 v[44:47], a[252:255], a[220:223], v[44:47]\n\t"
    "ds_write_b64 v14, v[28:29] offset:6016\n\t"
    "v_mfma_f32_16x16x32_bf16 v[48:51], a[248:251], v[144:147], v[48:51]\n\t"
    "v_mul_f32 v32, v32, v12\n\t"
    "v_mul_f32 v33, v33, v12\n\t"
    "v_mfma_f32_16x16x32_bf16 v[48:51], a[252:255], a[224:227], v[48:51]\n\t"
    "v_mul_f32 v34, v34, v12\n\t"
    "v_mul_f32 v35, v35, v12\n\t"
    "v_mfma_f32_16x16x32_bf16 v[52:55], a[248:251], v[148:151], v[52:55]\n\t"
    "v_cvt_pk_bf16_f32 v32, v32, v33\n\t"
    "v_cvt_pk_bf16_f32 v33, v34, v35\n\t"
    "v_mfma_f32_16x16x32_bf16 v[52:55], a[252:255], a[228:231], v[52:55]\n\t"
    "buffer_store_dwordx4 a[112:115], v3, s[48:51], s54 offen offset:0 sc1\n\t"
    "v_mfma_f32_16x16x32_bf16 v[56:59], a[248:251], v[152:155], v[56:59]\n\t"
    "v_mfma_f32_16x16x32_bf16 v[56:59], a[252:255], a[232:235], v[56:59]\n\t"
    "v_mfma_f32_16x16x32_bf16 v[60:63], a[248:251], v[156:159], v[60:63]\n\t"
    "v_mfma_f32_16x16x32_bf16 v[60:63], a[252:255], a[236:239], v[60:63]\n\t"
    "s_waitcnt lgkmcnt(1)\n\t"
    "ds_read_b64_tr_b16 a[248:249], v0 offset:1536\n\t"
    "v_mfma_f32_16x16x32_bf16 v[40:43], a[240:243], v[160:163], v[40:43]\n\t"
    "ds_read_b64_tr_b16 a[250:251], v0 offset:7680\n\t"
    "v_mfma_f32_16x16x32_bf16 v[40:43], a[244:247], v[184:187], v[40:43]\n\t"
    "ds_read_b64_tr_b16 a[252:253], v0 offset:2560\n\t"
    "v_mfma_f32_16x16x32_bf16 v[44:47], a[240:243], v[164:167], v[44:47]\n\t"
    "ds_read_b64_tr_b16 a[254:255], v0 offset:8704\n\t"
    "v_mfma_f32_16x16x32_bf16 v[44:47], a[244:247], v[188:191], v[44:47]\n\t"
    "ds_write_b64 v14, v[32:33] offset:8064\n\t"
    "v_mfma_f32_16x16x32_bf16 v[48:51], a[240:243], v[168:171], v[48:51]\n\t"
    "v_mul_f32 v36, v36, v13\n\t"
    "v_mul_f32 v37, v37, v13\n\t"
    "v_mfma_f32_16x16x32_bf16 v[48:51], a[244:247], v[192:195], v[48:51]\n\t"
    "v_mul_f32 v38, v38, v13\n\t"
    "v_mul_f32 v39, v39, v13\n\t"
    "v_mfma_f32_16x16x32_bf16 v[52:55], a[240:243], v[172:175], v[52:55]\n\t"
    "v_cvt_pk_bf16_f32 v36, v36, v37\n\t"
    "v_cvt_pk_bf16_f32 v37, v38, v39\n\t"
    "v_mfma_f32_16x16x32_bf16 v[52:55], a[244:247], v[196:199], v[52:55]\n\t"
    "buffer_load_dwordx4 v1, s[36:39], s53 offen offset:256 nt lds\n\t"
    "v_mfma_f32_16x16x32_bf16 v[56:59], a[240:243], v[176:179], v[56:59]\n\t"
    "s_mov_b32 s53, s92\n\t"
    "v_mfma_f32_16x16x32_bf16 v[56:59], a[244:247], v[200:203], v[56:59]\n\t"
    "s_add_u32 m0, s52, 24576\n\t"
    "v_mfma_f32_16x16x32_bf16 v[60:63], a[240:243], v[180:183], v[60:63]\n\t"
    "v_mfma_f32_16x16x32_bf16 v[60:63], a[244:247], v[204:207], v[60:63]\n\t"
    "s_waitcnt lgkmcnt(1)\n\t"
    "v_xor_b32 v0, 32, v0\n\t"
    "ds_read_b64_tr_b16 a[240:241], v0 offset:12800\n\t"
    "v_mfma_f32_16x16x32_bf16 v[40:43], a[248:251], v[208:211], v[40:43]\n\t"
    "ds_read_b64_tr_b16 a[242:243], v0 offset:18944\n\t"
    "v_mfma_f32_16x16x32_bf16 v[40:43], a[252:255], v[232:235], v[40:43]\n\t"
    "ds_read_b64_tr_b16 a[244:245], v0 offset:13824\n\t"
    "v_mfma_f32_16x16x32_bf16 v[44:47], a[248:251], v[212:215], v[44:47]\n\t"
    "ds_read_b64_tr_b16 a[246:247], v0 offset:19968\n\t"
    "v_mfma_f32_16x16x32_bf16 v[44:47], a[252:255], v[236:239], v[44:47]\n\t"
    "ds_write_b64 v14, v[36:37] offset:8320\n\t"
    "v_mfma_f32_16x16x32_bf16 v[48:51], a[248:251], v[216:219], v[48:51]\n\t"
    "v_mfma_f32_16x16x32_bf16 v[48:51], a[252:255], v[240:243], v[48:51]\n\t"
    "v_mfma_f32_16x16x32_bf16 v[52:55], a[248:251], v[220:223], v[52:55]\n\t"
    "v_mul_f32 v40, v40, v8\n\t"
    "v_mul_f32 v41, v41, v8\n\t"
    "v_mfma_f32_16x16x32_bf16 v[52:55], a[252:255], v[244:247], v[52:55]\n\t"
    "buffer_store_dwordx4 a[116:119], v3, s[48:51], s54 offen offset:3072 sc1\n\t"
    "v_mfma_f32_16x16x32_bf16 v[56:59], a[248:251], v[224:227], v[56:59]\n\t"
    "s_mov_b32 s54, 18432\n\t"
    "v_mul_f32 v42, v42, v8\n\t"
    "v_mul_f32 v43, v43, v8\n\t"
    "v_mfma_f32_16x16x32_bf16 v[56:59], a[252:255], v[248:251], v[56:59]\n\t"
    "v_cvt_pk_bf16_f32 v40, v40, v41\n\t"
    "v_cvt_pk_bf16_f32 v41, v42, v43\n\t"
    "v_mfma_f32_16x16x32_bf16 v[60:63], a[248:251], v[228:231], v[60:63]\n\t"
    "v_accvgpr_write_b32 a132, v40\n\t"
    "v_accvgpr_write_b32 a133, v41\n\t"
    "v_mfma_f32_16x16x32_bf16 v[60:63], a[252:255], v[252:255], v[60:63]\n\t"
    "s_waitcnt lgkmcnt(1)\n\t"
    "ds_read_b64_tr_b16 a[248:249], v0 offset:14848\n\t"
    "v_mul_f32 v44, v44, v9\n\t"
    "v_mfma_f32_16x16x32_bf16 v[16:19], a[240:243], v[64:67], 0\n\t"
    "ds_read_b64_tr_b16 a[250:251], v0 offset:20992\n\t"
    "v_mul_f32 v45, v45, v9\n\t"
    "v_mfma_f32_16x16x32_bf16 v[16:19], a[244:247], a[144:147], v[16:19]\n\t"
    "ds_read_b64_tr_b16 a[252:253], v0 offset:25088\n\t"
    "v_mul_f32 v46, v46, v9\n\t"
    "v_mfma_f32_16x16x32_bf16 v[20:23], a[240:243], v[68:71], 0\n\t"
    "ds_read_b64_tr_b16 a[254:255], v0 offset:31232\n\t"
    "v_mul_f32 v47, v47, v9\n\t"
    "v_mfma_f32_16x16x32_bf16 v[20:23], a[244:247], a[148:151], v[20:23]\n\t"
    "v_cvt_pk_bf16_f32 v44, v44, v45\n\t"
    "v_cvt_pk_bf16_f32 v45, v46, v47\n\t"
    "v_mfma_f32_16x16x32_bf16 v[24:27], a[240:243], v[72:75], 0\n\t"
    "v_accvgpr_write_b32 a134, v44\n\t"
    "v_accvgpr_write_b32 a135, v45\n\t"
    "v_mfma_f32_16x16x32_bf16 v[24:27], a[244:247], a[152:155], v[24:27]\n\t"
    "v_mul_f32 v48, v48, v10\n\t"
    "v_mul_f32 v49, v49, v10\n\t"
    "v_mfma_f32_16x16x32_bf16 v[28:31], a[240:243], v[76:79], 0\n\t"
    "v_mul_f32 v50, v50, v10\n\t"
    "v_mul_f32 v51, v51, v10\n\t"
    "v_mfma_f32_16x16x32_bf16 v[28:31], a[244:247], a[156:159], v[28:31]\n\t"
    "buffer_store_dwordx4 a[120:123], v3, s[48:51], s54 offen offset:0 sc1\n\t"
    "v_mfma_f32_16x16x32_bf16 v[32:35], a[240:243], v[80:83], 0\n\t"
    "v_cvt_pk_bf16_f32 v48, v48, v49\n\t"
    "v_cvt_pk_bf16_f32 v49, v50, v51\n\t"
    "v_mfma_f32_16x16x32_bf16 v[32:35], a[244:247], a[160:163], v[32:35]\n\t"
    "v_accvgpr_write_b32 a136, v48\n\t"
    "v_accvgpr_write_b32 a137, v49\n\t"
    "v_mfma_f32_16x16x32_bf16 v[36:39], a[240:243], v[84:87], 0\n\t"
    "v_mul_f32 v52, v52, v11\n\t"
    "v_mul_f32 v53, v53, v11\n\t"
    "v_mfma_f32_16x16x32_bf16 v[36:39], a[244:247], a[164:167], v[36:39]\n\t"
    "s_waitcnt lgkmcnt(0)\n\t"
    "ds_read_b64_tr_b16 a[240:241], v0 offset:26112\n\t"
    "v_mul_f32 v54, v54, v11\n\t"
    "v_mfma_f32_16x16x32_bf16 v[16:19], a[248:251], v[88:91], v[16:19]\n\t"
    "ds_read_b64_tr_b16 a[242:243], v0 offset:32256\n\t"
    "v_mul_f32 v55, v55, v11\n\t"
    "v_mfma_f32_16x16x32_bf16 v[16:19], a[252:255], a[168:171], v[16:19]\n\t"
    "ds_read_b64_tr_b16 a[244:245], v0 offset:27136\n\t"
    "v_cvt_pk_bf16_f32 v52, v52, v53\n\t"
    "v_mfma_f32_16x16x32_bf16 v[20:23], a[248:251], v[92:95], v[20:23]\n\t"
    "ds_read_b64_tr_b16 a[246:247], v0 offset:33280\n\t"
    "v_cvt_pk_bf16_f32 v53, v54, v55\n\t"
    "v_mfma_f32_16x16x32_bf16 v[20:23], a[252:255], a[172:175], v[20:23]\n\t"
    "v_accvgpr_write_b32 a138, v52\n\t"
    "v_accvgpr_write_b32 a139, v53\n\t"
    "v_mfma_f32_16x16x32_bf16 v[24:27], a[248:251], v[96:99], v[24:27]\n\t"
    "v_mul_f32 v56, v56, v12\n\t"
    "v_mul_f32 v57, v57, v12\n\t"
    "v_mfma_f32_16x16x32_bf16 v[24:27], a[252:255], a[176:179], v[24:27]\n\t"
    "buffer_store_dwordx4 a[124:127], v3, s[48:51], s54 offen offset:3072 sc1\n\t"
    "v_mfma_f32_16x16x32_bf16 v[28:31], a[248:251], v[100:103], v[28:31]\n\t"
    "v_mul_f32 v58, v58, v12\n\t"
    "v_mul_f32 v59, v59, v12\n\t"
    "v_mfma_f32_16x16x32_bf16 v[28:31], a[252:255], a[180:183], v[28:31]\n\t"
    "v_cvt_pk_bf16_f32 v56, v56, v57\n\t"
    "v_cvt_pk_bf16_f32 v57, v58, v59\n\t"
    "v_mfma_f32_16x16x32_bf16 v[32:35], a[248:251], v[104:107], v[32:35]\n\t"
    "v_accvgpr_write_b32 a140, v56\n\t"
    "v_accvgpr_write_b32 a141, v57\n\t"
    "v_mfma_f32_16x16x32_bf16 v[32:35], a[252:255], a[184:187], v[32:35]\n\t"
    "v_mul_f32 v60, v60, v13\n\t"
    "v_mul_f32 v61, v61, v13\n\t"
    "v_mfma_f32_16x16x32_bf16 v[36:39], a[248:251], v[108:111], v[36:39]\n\t"
    "v_mul_f32 v62, v62, v13\n\t"
    "v_mul_f32 v63, v63, v13\n\t"
    "v_mfma_f32_16x16x32_bf16 v[36:39], a[252:255], a[188:191], v[36:39]\n\t"
    "s_waitcnt lgkmcnt(0)\n\t"
    "s_barrier\n\t"
    "ds_read_b64_tr_b16 a[248:249], v0 offset:49664\n\t"
    "v_mfma_f32_16x16x32_bf16 v[16:19], a[240:243], v[112:115], v[16:19]\n\t"
    "ds_read_b64_tr_b16 a[250:251], v0 offset:55808\n\t"
    "v_cvt_pk_bf16_f32 v60, v60, v61\n\t"
    "v_mfma_f32_16x16x32_bf16 v[16:19], a[244:247], a[192:195], v[16:19]\n\t"
    "ds_read_b64_tr_b16 a[252:253], v0 offset:50688\n\t"
    "v_cvt_pk_bf16_f32 v61, v62, v63\n\t"
    "v_mfma_f32_16x16x32_bf16 v[20:23], a[240:243], v[116:119], v[20:23]\n\t"
    "ds_read_b64_tr_b16 a[254:255], v0 offset:56832\n\t"
    "v_accvgpr_write_b32 a142, v60\n\t"
    "v_mfma_f32_16x16x32_bf16 v[20:23], a[244:247], a[196:199], v[20:23]\n\t"
    "v_accvgpr_write_b32 a143, v61\n\t"
    "v_mfma_f32_16x16x32_bf16 v[24:27], a[240:243], v[120:123], v[24:27]\n\t"
    "v_mfma_f32_16x16x32_bf16 v[24:27], a[244:247], a[200:203], v[24:27]\n\t"
    "v_mfma_f32_16x16x32_bf16 v[28:31], a[240:243], v[124:127], v[28:31]\n\t"
    "v_mfma_f32_16x16x32_bf16 v[28:31], a[244:247], a[204:207], v[28:31]\n\t"
    "v_mfma_f32_16x16x32_bf16 v[32:35], a[240:243], v[128:131], v[32:35]\n\t"
    "v_mfma_f32_16x16x32_bf16 v[32:35], a[244:247], a[208:211], v[32:35]\n\t"
    "v_mfma_f32_16x16x32_bf16 v[36:39], a[240:243], v[132:135], v[36:39]\n\t"
    "v_mfma_f32_16x16x32_bf16 v[36:39], a[244:247], a[212:215], v[36:39]\n\t"
    "s_waitcnt lgkmcnt(0)\n\t"
    "ds_read_b64_tr_b16 a[240:241], v0 offset:51712\n\t"
    "v_mfma_f32_16x16x32_bf16 v[16:19], a[248:251], v[136:139], v[16:19]\n\t"
    "ds_read_b64_tr_b16 a[242:243], v0 offset:57856\n\t"
    "v_mfma_f32_16x16x32_bf16 v[16:19], a[252:255], a[216:219], v[16:19]\n\t"
    "ds_read_b64_tr_b16 a[244:245], v0 offset:512\n\t"
    "v_mfma_f32_16x16x32_bf16 v[20:23], a[248:251], v[140:143], v[20:23]\n\t"
    "ds_read_b64_tr_b16 a[246:247], v0 offset:6656\n\t"
    "v_mfma_f32_16x16x32_bf16 v[20:23], a[252:255], a[220:223], v[20:23]\n\t"
    "buffer_load_dwordx4 v1, s[36:39], s53 offen nt lds\n\t"
    "v_mfma_f32_16x16x32_bf16 v[24:27], a[248:251], v[144:147], v[24:27]\n\t"
    "s_addk_i32 m0, 896\n\t"
    "v_mfma_f32_16x16x32_bf16 v[24:27], a[252:255], a[224:227], v[24:27]\n\t"
    "v_mfma_f32_16x16x32_bf16 v[28:31], a[248:251], v[148:151], v[28:31]\n\t"
    "v_mfma_f32_16x16x32_bf16 v[28:31], a[252:255], a[228:231], v[28:31]\n\t"
    "buffer_load_dwordx4 v1, s[36:39], s53 offen offset:128 nt lds\n\t"
    "v_mfma_f32_16x16x32_bf16 v[32:35], a[248:251], v[152:155], v[32:35]\n\t"
    "s_addk_i32 m0, 896\n\t"
    "v_mfma_f32_16x16x32_bf16 v[32:35], a[252:255], a[232:235], v[32:35]\n\t"
    "v_mfma_f32_16x16x32_bf16 v[36:39], a[248:251], v[156:159], v[36:39]\n\t"
    "v_mfma_f32_16x16x32_bf16 v[36:39], a[252:255], a[236:239], v[36:39]\n\t"
    "s_waitcnt lgkmcnt(0)\n\t"
    "ds_read_b64_tr_b16 a[248:249], v0 offset:1536\n\t"
    "v_mfma_f32_16x16x32_bf16 v[16:19], a[240:243], v[160:163], v[16:19]\n\t"
    "ds_read_b64_tr_b16 a[250:251], v0 offset:7680\n\t"
    "v_mfma_f32_16x16x32_bf16 v[16:19], a[244:247], v[184:187], v[16:19]\n\t"
    "ds_read_b64_tr_b16 a[252:253], v0 offset:2560\n\t"
    "v_mfma_f32_16x16x32_bf16 v[20:23], a[240:243], v[164:167], v[20:23]\n\t"
    "ds_read_b64_tr_b16 a[254:255], v0 offset:8704\n\t"
    "v_mfma_f32_16x16x32_bf16 v[20:23], a[244:247], v[188:191], v[20:23]\n\t"
    "v_mfma_f32_16x16x32_bf16 v[24:27], a[240:243], v[168:171], v[24:27]\n\t"
    "v_mfma_f32_16x16x32_bf16 v[24:27], a[244:247], v[192:195], v[24:27]\n\t"
    "buffer_load_dwordx4 v1, s[36:39], s53 offen offset:256 nt lds\n\t"
    "v_mfma_f32_16x16x32_bf16 v[28:31], a[240:243], v[172:175], v[28:31]\n\t"
    "s_mov_b32 s53, s93\n\t"
    "v_mfma_f32_16x16x32_bf16 v[28:31], a[244:247], v[196:199], v[28:31]\n\t"
    "s_add_u32 m0, s52, 49152\n\t"
    "v_mfma_f32_16x16x32_bf16 v[32:35], a[240:243], v[176:179], v[32:35]\n\t"
    "v_mfma_f32_16x16x32_bf16 v[32:35], a[244:247], v[200:203], v[32:35]\n\t"
    "v_mfma_f32_16x16x32_bf16 v[36:39], a[240:243], v[180:183], v[36:39]\n\t"
    "v_mfma_f32_16x16x32_bf16 v[36:39], a[244:247], v[204:207], v[36:39]\n\t"
    "s_waitcnt vmcnt(21) lgkmcnt(0)\n\t"
    "s_barrier\n\t"
    "v_add_u32 v0, 0x9000, v0\n\t"
    "v_xor_b32 v0, 32, v0\n\t"
    "ds_read_b64_tr_b16 a[240:241], v0 offset:49152\n\t"
    "v_mfma_f32_16x16x32_bf16 v[16:19], a[248:251], v[208:211], v[16:19]\n\t"
    "ds_read_b64_tr_b16 a[242:243], v0 offset:55296\n\t"
    "v_mfma_f32_16x16x32_bf16 v[16:19], a[252:255], v[232:235], v[16:19]\n\t"
    "ds_read_b64_tr_b16 a[244:245], v0 offset:50176\n\t"
    "v_mfma_f32_16x16x32_bf16 v[20:23], a[248:251], v[212:215], v[20:23]\n\t"
    "ds_read_b64_tr_b16 a[246:247], v0 offset:56320\n\t"
    "v_mfma_f32_16x16x32_bf16 v[20:23], a[252:255], v[236:239], v[20:23]\n\t"
    "buffer_load_dwordx4 v1, s[36:39], s53 offen nt lds\n\t"
    "v_mfma_f32_16x16x32_bf16 v[24:27], a[248:251], v[216:219], v[24:27]\n\t"
    "s_addk_i32 m0, 896\n\t"
    "v_mfma_f32_16x16x32_bf16 v[24:27], a[252:255], v[240:243], v[24:27]\n\t"
    "v_mul_f32 v16, v16, v8\n\t"
    "v_mul_f32 v17, v17, v8\n\t"
    "v_mul_f32 v18, v18, v8\n\t"
    "v_mul_f32 v19, v19, v8\n\t"
    "v_cvt_pk_bf16_f32 v16, v16, v17\n\t"
    "v_cvt_pk_bf16_f32 v17, v18, v19\n\t"
    "v_mul_f32 v20, v20, v9\n\t"
    "v_mul_f32 v21, v21, v9\n\t"
    "v_mfma_f32_16x16x32_bf16 v[28:31], a[248:251], v[220:223], v[28:31]\n\t"
    "v_mul_f32 v22, v22, v9\n\t"
    "v_mul_f32 v23, v23, v9\n\t"
    "v_mfma_f32_16x16x32_bf16 v[28:31], a[252:255], v[244:247], v[28:31]\n\t"
    "v_cvt_pk_bf16_f32 v20, v20, v21\n\t"
    "v_cvt_pk_bf16_f32 v21, v22, v23\n\t"
    "v_mfma_f32_16x16x32_bf16 v[32:35], a[248:251], v[224:227], v[32:35]\n\t"
    "v_mul_f32 v24, v24, v10\n\t"
    "v_mul_f32 v25, v25, v10\n\t"
    "v_mfma_f32_16x16x32_bf16 v[32:35], a[252:255], v[248:251], v[32:35]\n\t"
    "v_mul_f32 v26, v26, v10\n\t"
    "v_mul_f32 v27, v27, v10\n\t"
    "v_mfma_f32_16x16x32_bf16 v[36:39], a[248:251], v[228:231], v[36:39]\n\t"
    "v_cvt_pk_bf16_f32 v24, v24, v25\n\t"
    "v_cvt_pk_bf16_f32 v25, v26, v27\n\t"
    "v_mfma_f32_16x16x32_bf16 v[36:39], a[252:255], v[252:255], v[36:39]\n\t"
    "s_waitcnt lgkmcnt(0)\n\t"
    "ds_read_b64_tr_b16 a[248:249], v0 offset:51200\n\t"
    "v_mul_f32 v28, v28, v11\n\t"
    "v_mfma_f32_16x16x32_bf16 v[40:43], a[240:243], v[64:67], 0\n\t"
    "ds_read_b64_tr_b16 a[250:251], v0 offset:57344\n\t"
    "v_mul_f32 v29, v29, v11\n\t"
    "v_mfma_f32_16x16x32_bf16 v[40:43], a[244:247], a[144:147], v[40:43]\n\t"
    "ds_read_b64_tr_b16 a[252:253], v0 offset:0\n\t"
    "v_mul_f32 v30, v30, v11\n\t"
    "v_mfma_f32_16x16x32_bf16 v[44:47], a[240:243], v[68:71], 0\n\t"
    "ds_read_b64_tr_b16 a[254:255], v0 offset:6144\n\t"
    "v_mul_f32 v31, v31, v11\n\t"
    "v_mfma_f32_16x16x32_bf16 v[44:47], a[244:247], a[148:151], v[44:47]\n\t"
    "buffer_load_dwordx4 v1, s[36:39], s53 offen offset:128 nt lds\n\t"
    "v_mfma_f32_16x16x32_bf16 v[48:51], a[240:243], v[72:75], 0\n\t"
    "s_addk_i32 m0, 896\n\t"
    "v_cvt_pk_bf16_f32 v28, v28, v29\n\t"
    "v_cvt_pk_bf16_f32 v29, v30, v31\n\t"
    "v_mfma_f32_16x16x32_bf16 v[48:51], a[244:247], a[152:155], v[48:51]\n\t"
    "ds_write_b64 v14, a[132:133] offset:0\n\t"
    "v_mfma_f32_16x16x32_bf16 v[52:55], a[240:243], v[76:79], 0\n\t"
    "v_mul_f32 v32, v32, v12\n\t"
    "v_mul_f32 v33, v33, v12\n\t"
    "v_mfma_f32_16x16x32_bf16 v[52:55], a[244:247], a[156:159], v[52:55]\n\t"
    "ds_write_b64 v14, a[134:135] offset:256\n\t"
    "v_mfma_f32_16x16x32_bf16 v[56:59], a[240:243], v[80:83], 0\n\t"
    "v_mul_f32 v34, v34, v12\n\t"
    "v_mul_f32 v35, v35, v12\n\t"
    "v_mfma_f32_16x16x32_bf16 v[56:59], a[244:247], a[160:163], v[56:59]\n\t"
    "buffer_load_dwordx4 v1, s[36:39], s53 offen offset:256 nt lds\n\t"
    "v_mfma_f32_16x16x32_bf16 v[60:63], a[240:243], v[84:87], 0\n\t"
    "s_mov_b32 s53, 0\n\t"
    "v_cvt_pk_bf16_f32 v32, v32, v33\n\t"
    "v_cvt_pk_bf16_f32 v33, v34, v35\n\t"
    "v_mfma_f32_16x16x32_bf16 v[60:63], a[244:247], a[164:167], v[60:63]\n\t"
    "s_waitcnt lgkmcnt(2)\n\t"
    "ds_read_b64_tr_b16 a[240:241], v0 offset:1024\n\t"
    "v_mul_f32 v36, v36, v13\n\t"
    "v_mfma_f32_16x16x32_bf16 v[40:43], a[248:251], v[88:91], v[40:43]\n\t"
    "ds_read_b64_tr_b16 a[242:243], v0 offset:7168\n\t"
    "v_mul_f32 v37, v37, v13\n\t"
    "v_mfma_f32_16x16x32_bf16 v[40:43], a[252:255], a[168:171], v[40:43]\n\t"
    "ds_read_b64_tr_b16 a[244:245], v0 offset:2048\n\t"
    "v_mul_f32 v38, v38, v13\n\t"
    "v_mfma_f32_16x16x32_bf16 v[44:47], a[248:251], v[92:95], v[44:47]\n\t"
    "ds_read_b64_tr_b16 a[246:247], v0 offset:8192\n\t"
    "v_mul_f32 v39, v39, v13\n\t"
    "v_mfma_f32_16x16x32_bf16 v[44:47], a[252:255], a[172:175], v[44:47]\n\t"
    "s_add_u32 m0, s52, 0\n\t"
    "ds_write_b64 v14, v[16:17] offset:1152\n\t"
    "v_mfma_f32_16x16x32_bf16 v[48:51], a[248:251], v[96:99], v[48:51]\n\t"
    "v_cvt_pk_bf16_f32 v36, v36, v37\n\t"
    "v_cvt_pk_bf16_f32 v37, v38, v39\n\t"
    "v_mfma_f32_16x16x32_bf16 v[48:51], a[252:255], a[176:179], v[48:51]\n\t"
    "ds_write_b64 v14, v[20:21] offset:1408\n\t"
    "v_mfma_f32_16x16x32_bf16 v[52:55], a[248:251], v[100:103], v[52:55]\n\t"
    "ds_read_b128 a[96:99], v15 offset:0\n\t"
    "ds_read_b128 a[100:103], v15 offset:128\n\t"
    "v_mfma_f32_16x16x32_bf16 v[52:55], a[252:255], a[180:183], v[52:55]\n\t"
    "buffer_load_dwordx4 v1, s[40:43], s53 offen nt lds\n\t"
    "v_mfma_f32_16x16x32_bf16 v[56:59], a[248:251], v[104:107], v[56:59]\n\t"
    "s_addk_i32 m0, 896\n\t"
    "ds_read_b128 a[104:107], v15 offset:256\n\t"
    "ds_read_b128 a[108:111], v15 offset:384\n\t"
    "v_mfma_f32_16x16x32_bf16 v[56:59], a[252:255], a[184:187], v[56:59]\n\t"
    "ds_write_b64 v14, a[136:137] offset:2304\n\t"
    "v_mfma_f32_16x16x32_bf16 v[60:63], a[248:251], v[108:111], v[60:63]\n\t"
    "v_mfma_f32_16x16x32_bf16 v[60:63], a[252:255], a[188:191], v[60:63]\n\t"
    "s_waitcnt lgkmcnt(7)\n\t"
    "ds_read_b64_tr_b16 a[248:249], v0 offset:24576\n\t"
    "v_mfma_f32_16x16x32_bf16 v[40:43], a[240:243], v[112:115], v[40:43]\n\t"
    "ds_read_b64_tr_b16 a[250:251], v0 offset:30720\n\t"
    "v_mfma_f32_16x16x32_bf16 v[40:43], a[244:247], a[192:195], v[40:43]\n\t"
    "ds_read_b64_tr_b16 a[252:253], v0 offset:25600\n\t"
    "v_mfma_f32_16x16x32_bf16 v[44:47], a[240:243], v[116:119], v[44:47]\n\t"
    "ds_read_b64_tr_b16 a[254:255], v0 offset:31744\n\t"
    "v_mfma_f32_16x16x32_bf16 v[44:47], a[244:247], a[196:199], v[44:47]\n\t"
    "buffer_load_dwordx4 v1, s[40:43], s53 offen offset:128 nt lds\n\t"
    "v_mfma_f32_16x16x32_bf16 v[48:51], a[240:243], v[120:123], v[48:51]\n\t"
    "s_addk_i32 m0, 896\n\t"
    "v_mfma_f32_16x16x32_bf16 v[48:51], a[244:247], a[200:203], v[48:51]\n\t"
    "ds_write_b64 v14, a[138:139] offset:2560\n\t"
    "v_mfma_f32_16x16x32_bf16 v[52:55], a[240:243], v[124:127], v[52:55]\n\t"
    "v_mfma_f32_16x16x32_bf16 v[52:55], a[244:247], a[204:207], v[52:55]\n\t"
    "ds_write_b64 v14, v[24:25] offset:3456\n\t"
    "v_mfma_f32_16x16x32_bf16 v[56:59], a[240:243], v[128:131], v[56:59]\n\t"
    "v_mfma_f32_16x16x32_bf16 v[56:59], a[244:247], a[208:211], v[56:59]\n\t"
    "v_mfma_f32_16x16x32_bf16 v[60:63], a[240:243], v[132:135], v[60:63]\n\t"
    "v_mfma_f32_16x16x32_bf16 v[60:63], a[244:247], a[212:215], v[60:63]\n\t"
    "s_waitcnt lgkmcnt(2)\n\t"
    "ds_read_b64_tr_b16 a[240:241], v0 offset:26624\n\t"
    "v_mfma_f32_16x16x32_bf16 v[40:43], a[248:251], v[136:139], v[40:43]\n\t"
    "ds_read_b64_tr_b16 a[242:243], v0 offset:32768\n\t"
    "v_mfma_f32_16x16x32_bf16 v[40:43], a[252:255], a[216:219], v[40:43]\n\t"
    "ds_read_b64_tr_b16 a[244:245], v0 offset:36864\n\t"
    "v_mfma_f32_16x16x32_bf16 v[44:47], a[248:251], v[140:143], v[44:47]\n\t"
    "ds_read_b64_tr_b16 a[246:247], v0 offset:43008\n\t"
    "v_mfma_f32_16x16x32_bf16 v[44:47], a[252:255], a[220:223], v[44:47]\n\t"
    "buffer_load_dwordx4 v1, s[40:43], s53 offen offset:256 nt lds\n\t"
    "v_mfma_f32_16x16x32_bf16 v[48:51], a[248:251], v[144:147], v[48:51]\n\t"
    "s_mov_b32 s53, 3072\n\t"
    "v_mfma_f32_16x16x32_bf16 v[48:51], a[252:255], a[224:227], v[48:51]\n\t"
    "s_add_u32 m0, s52, 12288\n\t"
    "ds_write_b64 v14, v[28:29] offset:3712\n\t"
    "v_mfma_f32_16x16x32_bf16 v[52:55], a[248:251], v[148:151], v[52:55]\n\t"
    "ds_read_b128 a[112:115], v15 offset:2304\n\t"
    "ds_read_b128 a[116:119], v15 offset:2432\n\t"
    "v_mfma_f32_16x16x32_bf16 v[52:55], a[252:255], a[228:231], v[52:55]\n\t"
    "ds_read_b128 a[120:123], v15 offset:2560\n\t"
    "ds_read_b128 a[124:127], v15 offset:2688\n\t"
    "v_mfma_f32_16x16x32_bf16 v[56:59], a[248:251], v[152:155], v[56:59]\n\t"
    "v_mfma_f32_16x16x32_bf16 v[56:59], a[252:255], a[232:235], v[56:59]\n\t"
    "ds_write_b64 v14, a[140:141] offset:4608\n\t"
    "v_mfma_f32_16x16x32_bf16 v[60:63], a[248:251], v[156:159], v[60:63]\n\t"
    "v_mfma_f32_16x16x32_bf16 v[60:63], a[252:255], a[236:239], v[60:63]\n\t"
    "s_waitcnt lgkmcnt(6)\n\t"
    "ds_read_b64_tr_b16 a[248:249], v0 offset:37888\n\t"
    "v_mfma_f32_16x16x32_bf16 v[40:43], a[240:243], v[160:163], v[40:43]\n\t"
    "ds_read_b64_tr_b16 a[250:251], v0 offset:44032\n\t"
    "v_mfma_f32_16x16x32_bf16 v[40:43], a[244:247], v[184:187], v[40:43]\n\t"
    "ds_read_b64_tr_b16 a[252:253], v0 offset:38912\n\t"
    "v_mfma_f32_16x16x32_bf16 v[44:47], a[240:243], v[164:167], v[44:47]\n\t"
    "ds_read_b64_tr_b16 a[254:255], v0 offset:45056\n\t"
    "v_mfma_f32_16x16x32_bf16 v[44:47], a[244:247], v[188:191], v[44:47]\n\t"
    "ds_write_b64 v14, a[142:143] offset:4864\n\t"
    "v_mfma_f32_16x16x32_bf16 v[48:51], a[240:243], v[168:171], v[48:51]\n\t"
    "v_mfma_f32_16x16x32_bf16 v[48:51], a[244:247], v[192:195], v[48:51]\n\t"
    "ds_write_b64 v14, v[32:33] offset:5760\n\t"
    "v_mfma_f32_16x16x32_bf16 v[52:55], a[240:243], v[172:175], v[52:55]\n\t"
    "v_mfma_f32_16x16x32_bf16 v[52:55], a[244:247], v[196:199], v[52:55]\n\t"
    "v_mfma_f32_16x16x32_bf16 v[56:59], a[240:243], v[176:179], v[56:59]\n\t"
    "v_mfma_f32_16x16x32_bf16 v[56:59], a[244:247], v[200:203], v[56:59]\n\t"
    "ds_write_b64 v14, v[36:37] offset:6016\n\t"
    "v_mfma_f32_16x16x32_bf16 v[60:63], a[240:243], v[180:183], v[60:63]\n\t"
    "ds_read_b128 a[128:131], v15 offset:4608\n\t"
    "ds_read_b128 a[132:135], v15 offset:4736\n\t"
    "v_mfma_f32_16x16x32_bf16 v[60:63], a[244:247], v[204:207], v[60:63]\n\t"
    "s_waitcnt lgkmcnt(5)\n\t"
    "v_xor_b32 v0, 32, v0\n\t"
    "ds_read_b64_tr_b16 a[240:241], v0 offset:49152\n\t"
    "v_mfma_f32_16x16x32_bf16 v[40:43], a[248:251], v[208:211], v[40:43]\n\t"
    "ds_read_b64_tr_b16 a[242:243], v0 offset:55296\n\t"
    "ds_read_b128 a[136:139], v15 offset:4864\n\t"
    "v_mfma_f32_16x16x32_bf16 v[40:43], a[252:255], v[232:235], v[40:43]\n\t"
    "ds_read_b64_tr_b16 a[244:245], v0 offset:50176\n\t"
    "ds_read_b128 a[140:143], v15 offset:4992\n\t"
    "v_mfma_f32_16x16x32_bf16 v[44:47], a[248:251], v[212:215], v[44:47]\n\t"
    "ds_read_b64_tr_b16 a[246:247], v0 offset:56320\n\t"
    "s_mov_b32 s54, 0\n\t"
    "v_mfma_f32_16x16x32_bf16 v[44:47], a[252:255], v[236:239], v[44:47]\n\t"
    "buffer_load_dwordx4 v1, s[40:43], s53 offen nt lds\n\t"
    "v_mfma_f32_16x16x32_bf16 v[48:51], a[248:251], v[216:219], v[48:51]\n\t"
    "s_addk_i32 m0, 896\n\t"
    "v_mul_f32 v40, v40, v8\n\t"
    "v_mul_f32 v41, v41, v8\n\t"
    "v_mfma_f32_16x16x32_bf16 v[48:51], a[252:255], v[240:243], v[48:51]\n\t"
    "buffer_store_dwordx4 a[96:99], v3, s[48:51], s54 offen offset:128 sc1\n\t"
    "v_mul_f32 v42, v42, v8\n\t"
    "v_mul_f32 v43, v43, v8\n\t"
    "v_cvt_pk_bf16_f32 v40, v40, v41\n\t"
    "v_cvt_pk_bf16_f32 v41, v42, v43\n\t"
    "v_mfma_f32_16x16x32_bf16 v[52:55], a[248:251], v[220:223], v[52:55]\n\t"
    "v_mfma_f32_16x16x32_bf16 v[52:55], a[252:255], v[244:247], v[52:55]\n\t"
    "v_mfma_f32_16x16x32_bf16 v[56:59], a[248:251], v[224:227], v[56:59]\n\t"
    "v_mfma_f32_16x16x32_bf16 v[56:59], a[252:255], v[248:251], v[56:59]\n\t"
    "v_mfma_f32_16x16x32_bf16 v[60:63], a[248:251], v[228:231], v[60:63]\n\t"
    "v_mfma_f32_16x16x32_bf16 v[60:63], a[252:255], v[252:255], v[60:63]\n\t"
    "s_waitcnt lgkmcnt(0)\n\t"
    "ds_read_b64_tr_b16 a[248:249], v0 offset:51200\n\t"
    "v_mfma_f32_16x16x32_bf16 v[16:19], a[240:243], v[64:67], 0\n\t"
    "ds_read_b64_tr_b16 a[250:251], v0 offset:57344\n\t"
    "v_mfma_f32_16x16x32_bf16 v[16:19], a[244:247], a[144:147], v[16:19]\n\t"
    "ds_read_b64_tr_b16 a[252:253], v0 offset:0\n\t"
    "v_mfma_f32_16x16x32_bf16 v[20:23], a[240:243], v[68:71], 0\n\t"
    "ds_read_b64_tr_b16 a[254:255], v0 offset:6144\n\t"
    "v_mfma_f32_16x16x32_bf16 v[20:23], a[244:247], a[148:151], v[20:23]\n\t"
    "buffer_store_dwordx4 a[100:103], v3, s[48:51], s54 offen offset:3200 sc1\n\t"
    "v_mfma_f32_16x16x32_bf16 v[24:27], a[240:243], v[72:75], 0\n\t"
    "s_mov_b32 s54, 6144\n\t"
    "v_mfma_f32_16x16x32_bf16 v[24:27], a[244:247], a[152:155], v[24:27]\n\t"
    "ds_write_b64 v14, v[40:41] offset:2304\n\t"
    "v_mul_f32 v44, v44, v9\n\t"
    "v_mul_f32 v45, v45, v9\n\t"
    "v_mul_f32 v46, v46, v9\n\t"
    "v_mul_f32 v47, v47, v9\n\t"
    "v_mfma_f32_16x16x32_bf16 v[28:31], a[240:243], v[76:79], 0\n\t"
    "v_cvt_pk_bf16_f32 v44, v44, v45\n\t"
    "v_cvt_pk_bf16_f32 v45, v46, v47\n\t"
    "v_mfma_f32_16x16x32_bf16 v[28:31], a[244:247], a[156:159], v[28:31]\n\t"
    "v_mfma_f32_16x16x32_bf16 v[32:35], a[240:243], v[80:83], 0\n\t"
    "v_mfma_f32_16x16x32_bf16 v[32:35], a[244:247], a[160:163], v[32:35]\n\t"
    "v_mfma_f32_16x16x32_bf16 v[36:39], a[240:243], v[84:87], 0\n\t"
    "v_mfma_f32_16x16x32_bf16 v[36:39], a[244:247], a[164:167], v[36:39]\n\t"
    "s_waitcnt lgkmcnt(1)\n\t"
    "ds_read_b64_tr_b16 a[240:241], v0 offset:1024\n\t"
    "v_mfma_f32_16x16x32_bf16 v[16:19], a[248:251], v[88:91], v[16:19]\n\t"
    "ds_read_b64_tr_b16 a[242:243], v0 offset:7168\n\t"
    "v_mfma_f32_16x16x32_bf16 v[16:19], a[252:255], a[168:171], v[16:19]\n\t"
    "ds_read_b64_tr_b16 a[244:245], v0 offset:2048\n\t"
    "v_mfma_f32_16x16x32_bf16 v[20:23], a[248:251], v[92:95], v[20:23]\n\t"
    "ds_read_b64_tr_b16 a[246:247], v0 offset:8192\n\t"
    "v_mfma_f32_16x16x32_bf16 v[20:23], a[252:255], a[172:175], v[20:23]\n\t"
    "buffer_load_dwordx4 v1, s[40:43], s53 offen offset:128 nt lds\n\t"
    "v_mfma_f32_16x16x32_bf16 v[24:27], a[248:251], v[96:99], v[24:27]\n\t"
    "s_addk_i32 m0, 896\n\t"
    "v_mfma_f32_16x16x32_bf16 v[24:27], a[252:255], a[176:179], v[24:27]\n\t"
    "ds_write_b64 v14, v[44:45] offset:2560\n\t"
    "v_mul_f32 v48, v48, v10\n\t"
    "v_mfma_f32_16x16x32_bf16 v[28:31], a[248:251], v[100:103], v[28:31]\n\t"
    "v_mul_f32 v49, v49, v10\n\t"
    "v_mul_f32 v50, v50, v10\n\t"
    "v_mfma_f32_16x16x32_bf16 v[28:31], a[252:255], a[180:183], v[28:31]\n\t"
    "v_mul_f32 v51, v51, v10\n\t"
    "v_cvt_pk_bf16_f32 v48, v48, v49\n\t"
    "v_mfma_f32_16x16x32_bf16 v[32:35], a[248:251], v[104:107], v[32:35]\n\t"
    "v_cvt_pk_bf16_f32 v49, v50, v51\n\t"
    "v_mfma_f32_16x16x32_bf16 v[32:35], a[252:255], a[184:187], v[32:35]\n\t"
    "v_mfma_f32_16x16x32_bf16 v[36:39], a[248:251], v[108:111], v[36:39]\n\t"
    "v_mfma_f32_16x16x32_bf16 v[36:39], a[252:255], a[188:191], v[36:39]\n\t"
    "s_waitcnt lgkmcnt(1)\n\t"
    "ds_read_b64_tr_b16 a[248:249], v0 offset:24576\n\t"
    "v_mfma_f32_16x16x32_bf16 v[16:19], a[240:243], v[112:115], v[16:19]\n\t"
    "ds_read_b64_tr_b16 a[250:251], v0 offset:30720\n\t"
    "v_mfma_f32_16x16x32_bf16 v[16:19], a[244:247], a[192:195], v[16:19]\n\t"
    "ds_read_b64_tr_b16 a[252:253], v0 offset:25600\n\t"
    "v_mfma_f32_16x16x32_bf16 v[20:23], a[240:243], v[116:119], v[20:23]\n\t"
    "ds_read_b64_tr_b16 a[254:255], v0 offset:31744\n\t"
    "v_mfma_f32_16x16x32_bf16 v[20:23], a[244:247], a[196:199], v[20:23]\n\t"
    "buffer_store_dwordx4 a[104:107], v3, s[48:51], s54 offen offset:128 sc1\n\t"
    "v_mfma_f32_16x16x32_bf16 v[24:27], a[240:243], v[120:123], v[24:27]\n\t"
    "v_mfma_f32_16x16x32_bf16 v[24:27], a[244:247], a[200:203], v[24:27]\n\t"
    "ds_write_b64 v14, v[48:49] offset:4608\n\t"
    "v_mfma_f32_16x16x32_bf16 v[28:31], a[240:243], v[124:127], v[28:31]\n\t"
    "v_mul_f32 v52, v52, v11\n\t"
    "v_mul_f32 v53, v53, v11\n\t"
    "v_mfma_f32_16x16x32_bf16 v[28:31], a[244:247], a[204:207], v[28:31]\n\t"
    "v_mul_f32 v54, v54, v11\n\t"
    "v_mul_f32 v55, v55, v11\n\t"
    "v_mfma_f32_16x16x32_bf16 v[32:35], a[240:243], v[128:131], v[32:35]\n\t"
    "v_cvt_pk_bf16_f32 v52, v52, v53\n\t"
    "v_cvt_pk_bf16_f32 v53, v54, v55\n\t"
    "v_mfma_f32_16x16x32_bf16 v[32:35], a[244:247], a[208:211], v[32:35]\n\t"
    "v_mfma_f32_16x16x32_bf16 v[36:39], a[240:243], v[132:135], v[36:39]\n\t"
    "v_mfma_f32_16x16x32_bf16 v[36:39], a[244:247], a[212:215], v[36:39]\n\t"
    "s_waitcnt lgkmcnt(1)\n\t"
    "ds_read_b64_tr_b16 a[240:241], v0 offset:26624\n\t"
    "v_mfma_f32_16x16x32_bf16 v[16:19], a[248:251], v[136:139], v[16:19]\n\t"
    "ds_read_b64_tr_b16 a[242:243], v0 offset:32768\n\t"
    "v_mfma_f32_16x16x32_bf16 v[16:19], a[252:255], a[216:219], v[16:19]\n\t"
    "ds_read_b64_tr_b16 a[244:245], v0 offset:36864\n\t"
    "v_mfma_f32_16x16x32_bf16 v[20:23], a[248:251], v[140:143], v[20:23]\n\t"
    "ds_read_b64_tr_b16 a[246:247], v0 offset:43008\n\t"
    "v_mfma_f32_16x16x32_bf16 v[20:23], a[252:255], a[220:223], v[20:23]\n\t"
    "buffer_load_dwordx4 v1, s[40:43], s53 offen offset:256 nt lds\n\t"
    "v_mfma_f32_16x16x32_bf16 v[24:27], a[248:251], v[144:147], v[24:27]\n\t"
    "s_mov_b32 s53, 6144\n\t"
    "v_mfma_f32_16x16x32_bf16 v[24:27], a[252:255], a[224:227], v[24:27]\n\t"
    "s_add_u32 m0, s52, 36864\n\t"
    "ds_write_b64 v14, v[52:53] offset:4864\n\t"
    "v_mfma_f32_16x16x32_bf16 v[28:31], a[248:251], v[148:151], v[28:31]\n\t"
    "v_mul_f32 v56, v56, v12\n\t"
    "v_mul_f32 v57, v57, v12\n\t"
    "v_mfma_f32_16x16x32_bf16 v[28:31], a[252:255], a[228:231], v[28:31]\n\t"
    "v_mul_f32 v58, v58, v12\n\t"
    "v_mul_f32 v59, v59, v12\n\t"
    "v_mfma_f32_16x16x32_bf16 v[32:35], a[248:251], v[152:155], v[32:35]\n\t"
    "v_cvt_pk_bf16_f32 v56, v56, v57\n\t"
    "v_cvt_pk_bf16_f32 v57, v58, v59\n\t"
    "v_mfma_f32_16x16x32_bf16 v[32:35], a[252:255], a[232:235], v[32:35]\n\t"
    "v_mfma_f32_16x16x32_bf16 v[36:39], a[248:251], v[156:159], v[36:39]\n\t"
    "v_mfma_f32_16x16x32_bf16 v[36:39], a[252:255], a[236:239], v[36:39]\n\t"
    "s_waitcnt lgkmcnt(1)\n\t"
    "ds_read_b64_tr_b16 a[248:249], v0 offset:37888\n\t"
    "v_mfma_f32_16x16x32_bf16 v[16:19], a[240:243], v[160:163], v[16:19]\n\t"
    "ds_read_b64_tr_b16 a[250:251], v0 offset:44032\n\t"
    "v_mfma_f32_16x16x32_bf16 v[16:19], a[244:247], v[184:187], v[16:19]\n\t"
    "ds_read_b64_tr_b16 a[252:253], v0 offset:38912\n\t"
    "v_mfma_f32_16x16x32_bf16 v[20:23], a[240:243], v[164:167], v[20:23]\n\t"
    "ds_read_b64_tr_b16 a[254:255], v0 offset:45056\n\t"
    "v_mfma_f32_16x16x32_bf16 v[20:23], a[244:247], v[188:191], v[20:23]\n\t"
    "buffer_store_dwordx4 a[108:111], v3, s[48:51], s54 offen offset:3200 sc1\n\t"
    "v_mfma_f32_16x16x32_bf16 v[24:27], a[240:243], v[168:171], v[24:27]\n\t"
    "s_mov_b32 s54, 12288\n\t"
    "v_mfma_f32_16x16x32_bf16 v[24:27], a[244:247], v[192:195], v[24:27]\n\t"
    "ds_write_b64 v14, v[56:57] offset:6912\n\t"
    "v_mfma_f32_16x16x32_bf16 v[28:31], a[240:243], v[172:175], v[28:31]\n\t"
    "v_mul_f32 v60, v60, v13\n\t"
    "v_mul_f32 v61, v61, v13\n\t"
    "v_mfma_f32_16x16x32_bf16 v[28:31], a[244:247], v[196:199], v[28:31]\n\t"
    "v_mul_f32 v62, v62, v13\n\t"
    "v_mul_f32 v63, v63, v13\n\t"
    "v_mfma_f32_16x16x32_bf16 v[32:35], a[240:243], v[176:179], v[32:35]\n\t"
    "v_cvt_pk_bf16_f32 v60, v60, v61\n\t"
    "v_cvt_pk_bf16_f32 v61, v62, v63\n\t"
    "v_mfma_f32_16x16x32_bf16 v[32:35], a[244:247], v[200:203], v[32:35]\n\t"
    "v_mfma_f32_16x16x32_bf16 v[36:39], a[240:243], v[180:183], v[36:39]\n\t"
    "v_mfma_f32_16x16x32_bf16 v[36:39], a[244:247], v[204:207], v[36:39]\n\t"
    "s_waitcnt lgkmcnt(1)\n\t"
    "v_xor_b32 v0, 32, v0\n\t"
    "ds_read_b64_tr_b16 a[240:241], v0 offset:49664\n\t"
    "v_mfma_f32_16x16x32_bf16 v[16:19], a[248:251], v[208:211], v[16:19]\n\t"
    "ds_read_b64_tr_b16 a[242:243], v0 offset:55808\n\t"
    "v_mfma_f32_16x16x32_bf16 v[16:19], a[252:255], v[232:235], v[16:19]\n\t"
    "ds_read_b64_tr_b16 a[244:245], v0 offset:50688\n\t"
    "v_mfma_f32_16x16x32_bf16 v[20:23], a[248:251], v[212:215], v[20:23]\n\t"
    "ds_read_b64_tr_b16 a[246:247], v0 offset:56832\n\t"
    "v_mfma_f32_16x16x32_bf16 v[20:23], a[252:255], v[236:239], v[20:23]\n\t"
    "buffer_store_dwordx4 a[112:115], v3, s[48:51], s54 offen offset:128 sc1\n\t"
    "v_mfma_f32_16x16x32_bf16 v[24:27], a[248:251], v[216:219], v[24:27]\n\t"
    "v_mfma_f32_16x16x32_bf16 v[24:27], a[252:255], v[240:243], v[24:27]\n\t"
    "ds_write_b64 v14, v[60:61] offset:7168\n\t"
    "v_mfma_f32_16x16x32_bf16 v[28:31], a[248:251], v[220:223], v[28:31]\n\t"
    "v_mul_f32 v16, v16, v8\n\t"
    "v_mul_f32 v17, v17, v8\n\t"
    "v_mfma_f32_16x16x32_bf16 v[28:31], a[252:255], v[244:247], v[28:31]\n\t"
    "v_mul_f32 v18, v18, v8\n\t"
    "v_mul_f32 v19, v19, v8\n\t"
    "v_mfma_f32_16x16x32_bf16 v[32:35], a[248:251], v[224:227], v[32:35]\n\t"
    "v_cvt_pk_bf16_f32 v16, v16, v17\n\t"
    "v_cvt_pk_bf16_f32 v17, v18, v19\n\t"
    "v_mfma_f32_16x16x32_bf16 v[32:35], a[252:255], v[248:251], v[32:35]\n\t"
    "v_mfma_f32_16x16x32_bf16 v[36:39], a[248:251], v[228:231], v[36:39]\n\t"
    "v_mfma_f32_16x16x32_bf16 v[36:39], a[252:255], v[252:255], v[36:39]\n\t"
    "s_waitcnt lgkmcnt(1)\n\t"
    "ds_read_b64_tr_b16 a[248:249], v0 offset:51712\n\t"
    "v_mfma_f32_16x16x32_bf16 v[40:43], a[240:243], v[64:67], 0\n\t"
    "ds_read_b64_tr_b16 a[250:251], v0 offset:57856\n\t"
    "v_mfma_f32_16x16x32_bf16 v[40:43], a[244:247], a[144:147], v[40:43]\n\t"
    "ds_read_b64_tr_b16 a[252:253], v0 offset:512\n\t"
    "v_mfma_f32_16x16x32_bf16 v[44:47], a[240:243], v[68:71], 0\n\t"
    "ds_read_b64_tr_b16 a[254:255], v0 offset:6656\n\t"
    "v_mfma_f32_16x16x32_bf16 v[44:47], a[244:247], a[148:151], v[44:47]\n\t"
    "buffer_store_dwordx4 a[116:119], v3, s[48:51], s54 offen offset:3200 sc1\n\t"
    "v_mfma_f32_16x16x32_bf16 v[48:51], a[240:243], v[72:75], 0\n\t"
    "s_mov_b32 s54, 18432\n\t"
    "v_mfma_f32_16x16x32_bf16 v[48:51], a[244:247], a[152:155], v[48:51]\n\t"
    "ds_write_b64 v14, v[16:17] offset:3456\n\t"
    "v_mfma_f32_16x16x32_bf16 v[52:55], a[240:243], v[76:79], 0\n\t"
    "v_mul_f32 v20, v20, v9\n\t"
    "v_mul_f32 v21, v21, v9\n\t"
    "v_mfma_f32_16x16x32_bf16 v[52:55], a[244:247], a[156:159], v[52:55]\n\t"
    "v_mul_f32 v22, v22, v9\n\t"
    "v_mul_f32 v23, v23, v9\n\t"
    "v_mfma_f32_16x16x32_bf16 v[56:59], a[240:243], v[80:83], 0\n\t"
    "v_cvt_pk_bf16_f32 v20, v20, v21\n\t"
    "v_cvt_pk_bf16_f32 v21, v22, v23\n\t"
    "v_mfma_f32_16x16x32_bf16 v[56:59], a[244:247], a[160:163], v[56:59]\n\t"
    "v_mfma_f32_16x16x32_bf16 v[60:63], a[240:243], v[84:87], 0\n\t"
    "v_mfma_f32_16x16x32_bf16 v[60:63], a[244:247], a[164:167], v[60:63]\n\t"
    "s_waitcnt lgkmcnt(1)\n\t"
    "ds_read_b64_tr_b16 a[240:241], v0 offset:1536\n\t"
    "v_mfma_f32_16x16x32_bf16 v[40:43], a[248:251], v[88:91], v[40:43]\n\t"
    "ds_read_b64_tr_b16 a[242:243], v0 offset:7680\n\t"
    "v_mfma_f32_16x16x32_bf16 v[40:43], a[252:255], a[168:171], v[40:43]\n\t"
    "ds_read_b64_tr_b16 a[244:245], v0 offset:2560\n\t"
    "v_mfma_f32_16x16x32_bf16 v[44:47], a[248:251], v[92:95], v[44:47]\n\t"
    "ds_read_b64_tr_b16 a[246:247], v0 offset:8704\n\t"
    "v_mfma_f32_16x16x32_bf16 v[44:47], a[252:255], a[172:175], v[44:47]\n\t"
    "buffer_store_dwordx4 a[120:123], v3, s[48:51], s54 offen offset:128 sc1\n\t"
    "v_mfma_f32_16x16x32_bf16 v[48:51], a[248:251], v[96:99], v[48:51]\n\t"
    "v_mfma_f32_16x16x32_bf16 v[48:51], a[252:255], a[176:179], v[48:51]\n\t"
    "ds_write_b64 v14, v[20:21] offset:3712\n\t"
    "v_mfma_f32_16x16x32_bf16 v[52:55], a[248:251], v[100:103], v[52:55]\n\t"
    "v_mul_f32 v24, v24, v10\n\t"
    "v_mul_f32 v25, v25, v10\n\t"
    "v_mfma_f32_16x16x32_bf16 v[52:55], a[252:255], a[180:183], v[52:55]\n\t"
    "v_mul_f32 v26, v26, v10\n\t"
    "v_mul_f32 v27, v27, v10\n\t"
    "v_mfma_f32_16x16x32_bf16 v[56:59], a[248:251], v[104:107], v[56:59]\n\t"
    "v_cvt_pk_bf16_f32 v24, v24, v25\n\t"
    "v_cvt_pk_bf16_f32 v25, v26, v27\n\t"
    "v_mfma_f32_16x16x32_bf16 v[56:59], a[252:255], a[184:187], v[56:59]\n\t"
    "v_mfma_f32_16x16x32_bf16 v[60:63], a[248:251], v[108:111], v[60:63]\n\t"
    "v_mfma_f32_16x16x32_bf16 v[60:63], a[252:255], a[188:191], v[60:63]\n\t"
    "s_waitcnt lgkmcnt(1)\n\t"
    "ds_read_b64_tr_b16 a[248:249], v0 offset:25088\n\t"
    "v_mfma_f32_16x16x32_bf16 v[40:43], a[240:243], v[112:115], v[40:43]\n\t"
    "ds_read_b64_tr_b16 a[250:251], v0 offset:31232\n\t"
    "v_mfma_f32_16x16x32_bf16 v[40:43], a[244:247], a[192:195], v[40:43]\n\t"
    "ds_read_b64_tr_b16 a[252:253], v0 offset:26112\n\t"
    "v_mfma_f32_16x16x32_bf16 v[44:47], a[240:243], v[116:119], v[44:47]\n\t"
    "ds_read_b64_tr_b16 a[254:255], v0 offset:32256\n\t"
    "v_mfma_f32_16x16x32_bf16 v[44:47], a[244:247], a[196:199], v[44:47]\n\t"
    "buffer_store_dwordx4 a[124:127], v3, s[48:51], s54 offen offset:3200 sc1\n\t"
    "v_mfma_f32_16x16x32_bf16 v[48:51], a[240:243], v[120:123], v[48:51]\n\t"
    "s_mov_b32 s54, 24576\n\t"
    "v_mfma_f32_16x16x32_bf16 v[48:51], a[244:247], a[200:203], v[48:51]\n\t"
    "ds_write_b64 v14, v[24:25] offset:5760\n\t"
    "v_mfma_f32_16x16x32_bf16 v[52:55], a[240:243], v[124:127], v[52:55]\n\t"
    "v_mul_f32 v28, v28, v11\n\t"
    "v_mul_f32 v29, v29, v11\n\t"
    "v_mfma_f32_16x16x32_bf16 v[52:55], a[244:247], a[204:207], v[52:55]\n\t"
    "v_mul_f32 v30, v30, v11\n\t"
    "v_mul_f32 v31, v31, v11\n\t"
    "v_mfma_f32_16x16x32_bf16 v[56:59], a[240:243], v[128:131], v[56:59]\n\t"
    "v_cvt_pk_bf16_f32 v28, v28, v29\n\t"
    "v_cvt_pk_bf16_f32 v29, v30, v31\n\t"
    "v_mfma_f32_16x16x32_bf16 v[56:59], a[244:247], a[208:211], v[56:59]\n\t"
    "v_mfma_f32_16x16x32_bf16 v[60:63], a[240:243], v[132:135], v[60:63]\n\t"
    "v_mfma_f32_16x16x32_bf16 v[60:63], a[244:247], a[212:215], v[60:63]\n\t"
    "s_waitcnt lgkmcnt(1)\n\t"
    "ds_read_b64_tr_b16 a[240:241], v0 offset:27136\n\t"
    "v_mfma_f32_16x16x32_bf16 v[40:43], a[248:251], v[136:139], v[40:43]\n\t"
    "ds_read_b64_tr_b16 a[242:243], v0 offset:33280\n\t"
    "v_mfma_f32_16x16x32_bf16 v[40:43], a[252:255], a[216:219], v[40:43]\n\t"
    "ds_read_b64_tr_b16 a[244:245], v0 offset:37376\n\t"
    "v_mfma_f32_16x16x32_bf16 v[44:47], a[248:251], v[140:143], v[44:47]\n\t"
    "ds_read_b64_tr_b16 a[246:247], v0 offset:43520\n\t"
    "v_mfma_f32_16x16x32_bf16 v[44:47], a[252:255], a[220:223], v[44:47]\n\t"
    "buffer_store_dwordx4 a[128:131], v3, s[48:51], s54 offen offset:128 sc1\n\t"
    "v_mfma_f32_16x16x32_bf16 v[48:51], a[248:251], v[144:147], v[48:51]\n\t"
    "v_mfma_f32_16x16x32_bf16 v[48:51], a[252:255], a[224:227], v[48:51]\n\t"
    "ds_write_b64 v14, v[28:29] offset:6016\n\t"
    "v_mfma_f32_16x16x32_bf16 v[52:55], a[248:251], v[148:151], v[52:55]\n\t"
    "v_mul_f32 v32, v32, v12\n\t"
    "v_mul_f32 v33, v33, v12\n\t"
    "v_mfma_f32_16x16x32_bf16 v[52:55], a[252:255], a[228:231], v[52:55]\n\t"
    "v_mul_f32 v34, v34, v12\n\t"
    "v_mul_f32 v35, v35, v12\n\t"
    "v_mfma_f32_16x16x32_bf16 v[56:59], a[248:251], v[152:155], v[56:59]\n\t"
    "v_cvt_pk_bf16_f32 v32, v32, v33\n\t"
    "v_cvt_pk_bf16_f32 v33, v34, v35\n\t"
    "v_mfma_f32_16x16x32_bf16 v[56:59], a[252:255], a[232:235], v[56:59]\n\t"
    "v_mfma_f32_16x16x32_bf16 v[60:63], a[248:251], v[156:159], v[60:63]\n\t"
    "v_mfma_f32_16x16x32_bf16 v[60:63], a[252:255], a[236:239], v[60:63]\n\t"
    "s_waitcnt lgkmcnt(1)\n\t"
    "ds_read_b64_tr_b16 a[248:249], v0 offset:38400\n\t"
    "v_mfma_f32_16x16x32_bf16 v[40:43], a[240:243], v[160:163], v[40:43]\n\t"
    "ds_read_b64_tr_b16 a[250:251], v0 offset:44544\n\t"
    "v_mfma_f32_16x16x32_bf16 v[40:43], a[244:247], v[184:187], v[40:43]\n\t"
    "ds_read_b64_tr_b16 a[252:253], v0 offset:39424\n\t"
    "v_mfma_f32_16x16x32_bf16 v[44:47], a[240:243], v[164:167], v[44:47]\n\t"
    "ds_read_b64_tr_b16 a[254:255], v0 offset:45568\n\t"
    "v_mfma_f32_16x16x32_bf16 v[44:47], a[244:247], v[188:191], v[44:47]\n\t"
    "buffer_store_dwordx4 a[132:135], v3, s[48:51], s54 offen offset:3200 sc1\n\t"
    "v_mfma_f32_16x16x32_bf16 v[48:51], a[240:243], v[168:171], v[48:51]\n\t"
    "v_mfma_f32_16x16x32_bf16 v[48:51], a[244:247], v[192:195], v[48:51]\n\t"
    "ds_write_b64 v14, v[32:33] offset:8064\n\t"
    "v_mfma_f32_16x16x32_bf16 v[52:55], a[240:243], v[172:175], v[52:55]\n\t"
    "v_mul_f32 v36, v36, v13\n\t"
    "v_mul_f32 v37, v37, v13\n\t"
    "v_mfma_f32_16x16x32_bf16 v[52:55], a[244:247], v[196:199], v[52:55]\n\t"
    "v_mul_f32 v38, v38, v13\n\t"
    "v_mul_f32 v39, v39, v13\n\t"
    "v_mfma_f32_16x16x32_bf16 v[56:59], a[240:243], v[176:179], v[56:59]\n\t"
    "v_cvt_pk_bf16_f32 v36, v36, v37\n\t"
    "v_cvt_pk_bf16_f32 v37, v38, v39\n\t"
    "v_mfma_f32_16x16x32_bf16 v[56:59], a[244:247], v[200:203], v[56:59]\n\t"
    "v_mfma_f32_16x16x32_bf16 v[60:63], a[240:243], v[180:183], v[60:63]\n\t"
    "v_mfma_f32_16x16x32_bf16 v[60:63], a[244:247], v[204:207], v[60:63]\n\t"
    "s_waitcnt lgkmcnt(1)\n\t"
    "v_xor_b32 v0, 32, v0\n\t"
    "ds_read_b64_tr_b16 a[240:241], v0 offset:49664\n\t"
    "v_mfma_f32_16x16x32_bf16 v[40:43], a[248:251], v[208:211], v[40:43]\n\t"
    "ds_read_b64_tr_b16 a[242:243], v0 offset:55808\n\t"
    "v_mfma_f32_16x16x32_bf16 v[40:43], a[252:255], v[232:235], v[40:43]\n\t"
    "ds_read_b64_tr_b16 a[244:245], v0 offset:50688\n\t"
    "v_mfma_f32_16x16x32_bf16 v[44:47], a[248:251], v[212:215], v[44:47]\n\t"
    "ds_read_b64_tr_b16 a[246:247], v0 offset:56832\n\t"
    "v_mfma_f32_16x16x32_bf16 v[44:47], a[252:255], v[236:239], v[44:47]\n\t"
    "buffer_store_dwordx4 a[136:139], v3, s[48:51], s65 offen offset:128 sc1\n\t"
    "v_mfma_f32_16x16x32_bf16 v[48:51], a[248:251], v[216:219], v[48:51]\n\t"
    "v_mfma_f32_16x16x32_bf16 v[48:51], a[252:255], v[240:243], v[48:51]\n\t"
    "ds_write_b64 v14, v[36:37] offset:8320\n\t"
    "v_mfma_f32_16x16x32_bf16 v[52:55], a[248:251], v[220:223], v[52:55]\n\t"
    "v_mul_f32 v40, v40, v8\n\t"
    "v_mul_f32 v41, v41, v8\n\t"
    "v_mfma_f32_16x16x32_bf16 v[52:55], a[252:255], v[244:247], v[52:55]\n\t"
    "v_mul_f32 v42, v42, v8\n\t"
    "v_mul_f32 v43, v43, v8\n\t"
    "v_mfma_f32_16x16x32_bf16 v[56:59], a[248:251], v[224:227], v[56:59]\n\t"
    "v_cvt_pk_bf16_f32 v40, v40, v41\n\t"
    "v_cvt_pk_bf16_f32 v41, v42, v43\n\t"
    "v_mfma_f32_16x16x32_bf16 v[56:59], a[252:255], v[248:251], v[56:59]\n\t"
    "v_mul_f32 v44, v44, v9\n\t"
    "v_mul_f32 v45, v45, v9\n\t"
    "v_mfma_f32_16x16x32_bf16 v[60:63], a[248:251], v[228:231], v[60:63]\n\t"
    "v_mul_f32 v46, v46, v9\n\t"
    "v_mul_f32 v47, v47, v9\n\t"
    "v_mfma_f32_16x16x32_bf16 v[60:63], a[252:255], v[252:255], v[60:63]\n\t"
    "s_waitcnt lgkmcnt(1)\n\t"
    "ds_read_b64_tr_b16 a[248:249], v0 offset:51712\n\t"
    "v_cvt_pk_bf16_f32 v44, v44, v45\n\t"
    "v_mfma_f32_16x16x32_bf16 v[16:19], a[240:243], v[64:67], 0\n\t"
    "ds_read_b64_tr_b16 a[250:251], v0 offset:57856\n\t"
    "v_cvt_pk_bf16_f32 v45, v46, v47\n\t"
    "v_mfma_f32_16x16x32_bf16 v[16:19], a[244:247], a[144:147], v[16:19]\n\t"
    "ds_read_b64_tr_b16 a[252:253], v0 offset:512\n\t"
    "v_mul_f32 v48, v48, v10\n\t"
    "v_mfma_f32_16x16x32_bf16 v[20:23], a[240:243], v[68:71], 0\n\t"
    "ds_read_b64_tr_b16 a[254:255], v0 offset:6656\n\t"
    "v_mul_f32 v49, v49, v10\n\t"
    "v_mfma_f32_16x16x32_bf16 v[20:23], a[244:247], a[148:151], v[20:23]\n\t"
    "buffer_store_dwordx4 a[140:143], v3, s[48:51], s75 offen offset:128 sc1\n\t"
    "v_mfma_f32_16x16x32_bf16 v[24:27], a[240:243], v[72:75], 0\n\t"
    "v_mul_f32 v50, v50, v10\n\t"
    "v_mul_f32 v51, v51, v10\n\t"
    "v_mfma_f32_16x16x32_bf16 v[24:27], a[244:247], a[152:155], v[24:27]\n\t"
    "v_cvt_pk_bf16_f32 v48, v48, v49\n\t"
    "v_cvt_pk_bf16_f32 v49, v50, v51\n\t"
    "v_mfma_f32_16x16x32_bf16 v[28:31], a[240:243], v[76:79], 0\n\t"
    "v_mul_f32 v52, v52, v11\n\t"
    "v_mul_f32 v53, v53, v11\n\t"
    "v_mfma_f32_16x16x32_bf16 v[28:31], a[244:247], a[156:159], v[28:31]\n\t"
    "v_mul_f32 v54, v54, v11\n\t"
    "v_mul_f32 v55, v55, v11\n\t"
    "v_mfma_f32_16x16x32_bf16 v[32:35], a[240:243], v[80:83], 0\n\t"
    "v_cvt_pk_bf16_f32 v52, v52, v53\n\t"
    "v_cvt_pk_bf16_f32 v53, v54, v55\n\t"
    "v_mfma_f32_16x16x32_bf16 v[32:35], a[244:247], a[160:163], v[32:35]\n\t"
    "v_mul_f32 v56, v56, v12\n\t"
    "v_mul_f32 v57, v57, v12\n\t"
    "v_mfma_f32_16x16x32_bf16 v[36:39], a[240:243], v[84:87], 0\n\t"
    "v_mul_f32 v58, v58, v12\n\t"
    "v_mul_f32 v59, v59, v12\n\t"
    "v_mfma_f32_16x16x32_bf16 v[36:39], a[244:247], a[164:167], v[36:39]\n\t"
    "s_waitcnt lgkmcnt(0)\n\t"
    "ds_read_b64_tr_b16 a[240:241], v0 offset:1536\n\t"
    "v_cvt_pk_bf16_f32 v56, v56, v57\n\t"
    "v_mfma_f32_16x16x32_bf16 v[16:19], a[248:251], v[88:91], v[16:19]\n\t"
    "ds_read_b64_tr_b16 a[242:243], v0 offset:7680\n\t"
    "v_cvt_pk_bf16_f32 v57, v58, v59\n\t"
    "v_mfma_f32_16x16x32_bf16 v[16:19], a[252:255], a[168:171], v[16:19]\n\t"
    "ds_read_b64_tr_b16 a[244:245], v0 offset:2560\n\t"
    "v_mul_f32 v60, v60, v13\n\t"
    "v_mfma_f32_16x16x32_bf16 v[20:23], a[248:251], v[92:95], v[20:23]\n\t"
    "ds_read_b64_tr_b16 a[246:247], v0 offset:8704\n\t"
    "v_mul_f32 v61, v61, v13\n\t"
    "v_mfma_f32_16x16x32_bf16 v[20:23], a[252:255], a[172:175], v[20:23]\n\t"
    "v_mul_f32 v62, v62, v13\n\t"
    "v_mul_f32 v63, v63, v13\n\t"
    "v_mfma_f32_16x16x32_bf16 v[24:27], a[248:251], v[96:99], v[24:27]\n\t"
    "v_cvt_pk_bf16_f32 v60, v60, v61\n\t"
    "v_cvt_pk_bf16_f32 v61, v62, v63\n\t"
    "v_mfma_f32_16x16x32_bf16 v[24:27], a[252:255], a[176:179], v[24:27]\n\t"
    "v_mfma_f32_16x16x32_bf16 v[28:31], a[248:251], v[100:103], v[28:31]\n\t"
    "v_mfma_f32_16x16x32_bf16 v[28:31], a[252:255], a[180:183], v[28:31]\n\t"
    "v_mfma_f32_16x16x32_bf16 v[32:35], a[248:251], v[104:107], v[32:35]\n\t"
    "v_mfma_f32_16x16x32_bf16 v[32:35], a[252:255], a[184:187], v[32:35]\n\t"
    "v_mfma_f32_16x16x32_bf16 v[36:39], a[248:251], v[108:111], v[36:39]\n\t"
    "v_mfma_f32_16x16x32_bf16 v[36:39], a[252:255], a[188:191], v[36:39]\n\t"
    "s_waitcnt vmcnt(18) lgkmcnt(0)\n\t"
    "s_barrier\n\t"
    "ds_read_b64_tr_b16 a[248:249], v0 offset:25088\n\t"
    "v_mfma_f32_16x16x32_bf16 v[16:19], a[240:243], v[112:115], v[16:19]\n\t"
    "ds_read_b64_tr_b16 a[250:251], v0 offset:31232\n\t"
    "v_add_u32 v4, s63, v2\n\t"
    "v_mfma_f32_16x16x32_bf16 v[16:19], a[244:247], a[192:195], v[16:19]\n\t"
    "ds_read_b64_tr_b16 a[252:253], v0 offset:26112\n\t"
    "ds_read_b128 a[96:99], v4 offset:0\n\t"
    "v_mfma_f32_16x16x32_bf16 v[20:23], a[240:243], v[116:119], v[20:23]\n\t"
    "ds_read_b64_tr_b16 a[254:255], v0 offset:32256\n\t"
    "ds_read_b128 a[120:123], v4 offset:6144\n\t"
    "v_mfma_f32_16x16x32_bf16 v[20:23], a[244:247], a[196:199], v[20:23]\n\t"
    "buffer_load_dwordx4 v1, s[40:43], s53 offen nt lds\n\t"
    "v_mfma_f32_16x16x32_bf16 v[24:27], a[240:243], v[120:123], v[24:27]\n\t"
    "s_addk_i32 m0, 896\n\t"
    "ds_read_b128 a[100:103], v4 offset:512\n\t"
    "v_mfma_f32_16x16x32_bf16 v[24:27], a[244:247], a[200:203], v[24:27]\n\t"
    "v_mfma_f32_16x16x32_bf16 v[28:31], a[240:243], v[124:127], v[28:31]\n\t"
    "ds_read_b128 a[124:127], v4 offset:6656\n\t"
    "v_mfma_f32_16x16x32_bf16 v[28:31], a[244:247], a[204:207], v[28:31]\n\t"
    "v_mfma_f32_16x16x32_bf16 v[32:35], a[240:243], v[128:131], v[32:35]\n\t"
    "ds_read_b128 a[104:107], v4 offset:1024\n\t"
    "v_mfma_f32_16x16x32_bf16 v[32:35], a[244:247], a[208:211], v[32:35]\n\t"
    "v_mfma_f32_16x16x32_bf16 v[36:39], a[240:243], v[132:135], v[36:39]\n\t"
    "ds_read_b128 a[128:131], v4 offset:7168\n\t"
    "v_mfma_f32_16x16x32_bf16 v[36:39], a[244:247], a[212:215], v[36:39]\n\t"
    "s_waitcnt lgkmcnt(5)\n\t"
    "ds_read_b64_tr_b16 a[240:241], v0 offset:27136\n\t"
    "v_mfma_f32_16x16x32_bf16 v[16:19], a[248:251], v[136:139], v[16:19]\n\t"
    "ds_read_b64_tr_b16 a[242:243], v0 offset:33280\n\t"
    "ds_read_b128 a[108:111], v4 offset:1536\n\t"
    "v_mfma_f32_16x16x32_bf16 v[16:19], a[252:255], a[216:219], v[16:19]\n\t"
    "ds_read_b64_tr_b16 a[244:245], v0 offset:37376\n\t"
    "v_mfma_f32_16x16x32_bf16 v[20:23], a[248:251], v[140:143], v[20:23]\n\t"
    "ds_read_b64_tr_b16 a[246:247], v0 offset:43520\n\t"
    "ds_read_b128 a[132:135], v4 offset:7680\n\t"
    "v_mfma_f32_16x16x32_bf16 v[20:23], a[252:255], a[220:223], v[20:23]\n\t"
    "buffer_load_dwordx4 v1, s[40:43], s53 offen offset:128 nt lds\n\t"
    "v_mfma_f32_16x16x32_bf16 v[24:27], a[248:251], v[144:147], v[24:27]\n\t"
    "s_addk_i32 m0, 896\n\t"
    "ds_read_b128 a[112:115], v4 offset:2048\n\t"
    "v_mfma_f32_16x16x32_bf16 v[24:27], a[252:255], a[224:227], v[24:27]\n\t"
    "v_mfma_f32_16x16x32_bf16 v[28:31], a[248:251], v[148:151], v[28:31]\n\t"
    "ds_read_b128 a[136:139], v4 offset:8192\n\t"
    "v_mfma_f32_16x16x32_bf16 v[28:31], a[252:255], a[228:231], v[28:31]\n\t"
    "s_add_u32 s36, s36, 37748736\n\t"
    "s_addc_u32 s37, s37, 0\n\t"
    "s_cmp_lt_u32 s58, 2\n\t"
    "s_cselect_b32 s38, 245760, 0\n\t"
    "v_mfma_f32_16x16x32_bf16 v[32:35], a[248:251], v[152:155], v[32:35]\n\t"
    "ds_read_b128 a[116:119], v4 offset:2560\n\t"
    "v_mfma_f32_16x16x32_bf16 v[32:35], a[252:255], a[232:235], v[32:35]\n\t"
    "v_mfma_f32_16x16x32_bf16 v[36:39], a[248:251], v[156:159], v[36:39]\n\t"
    "ds_read_b128 a[140:143], v4 offset:8704\n\t"
    "v_mfma_f32_16x16x32_bf16 v[36:39], a[252:255], a[236:239], v[36:39]\n\t"
    "s_waitcnt lgkmcnt(5)\n\t"
    "ds_read_b64_tr_b16 a[248:249], v0 offset:38400\n\t"
    "v_mfma_f32_16x16x32_bf16 v[16:19], a[240:243], v[160:163], v[16:19]\n\t"
    "ds_read_b64_tr_b16 a[250:251], v0 offset:44544\n\t"
    "v_mfma_f32_16x16x32_bf16 v[16:19], a[244:247], v[184:187], v[16:19]\n\t"
    "ds_read_b64_tr_b16 a[252:253], v0 offset:39424\n\t"
    "v_mfma_f32_16x16x32_bf16 v[20:23], a[240:243], v[164:167], v[20:23]\n\t"
    "ds_read_b64_tr_b16 a[254:255], v0 offset:45568\n\t"
    "v_mfma_f32_16x16x32_bf16 v[20:23], a[244:247], v[188:191], v[20:23]\n\t"
    "buffer_load_dwordx4 v1, s[40:43], s53 offen offset:256 nt lds\n\t"
    "v_mfma_f32_16x16x32_bf16 v[24:27], a[240:243], v[168:171], v[24:27]\n\t"
    "v_mfma_f32_16x16x32_bf16 v[24:27], a[244:247], v[192:195], v[24:27]\n\t"
    "v_mfma_f32_16x16x32_bf16 v[28:31], a[240:243], v[172:175], v[28:31]\n\t"
    "v_mfma_f32_16x16x32_bf16 v[28:31], a[244:247], v[196:199], v[28:31]\n\t"
    "v_mfma_f32_16x16x32_bf16 v[32:35], a[240:243], v[176:179], v[32:35]\n\t"
    "v_mfma_f32_16x16x32_bf16 v[32:35], a[244:247], v[200:203], v[32:35]\n\t"
    "v_mfma_f32_16x16x32_bf16 v[36:39], a[240:243], v[180:183], v[36:39]\n\t"
    "v_mfma_f32_16x16x32_bf16 v[36:39], a[244:247], v[204:207], v[36:39]\n\t"
    "s_waitcnt vmcnt(12) lgkmcnt(0)\n\t"
    "s_barrier\n\t"
    "v_mov_b32 v59, v0\n\t"
    "v_mov_b32 v0, v2\n\t"
    "ds_read_b128 a[240:243], v0 offset:0\n\t"
    "ds_read_b128 a[244:247], v0 offset:512\n\t"
    "v_mfma_f32_16x16x32_bf16 v[16:19], a[248:251], v[208:211], v[16:19]\n\t"
    "v_mfma_f32_16x16x32_bf16 v[16:19], a[252:255], v[232:235], v[16:19]\n\t"
    "s_add_u32 s44, s44, 37748736\n\t"
    "s_addc_u32 s45, s45, 0\n\t"
    "s_cmp_lt_u32 s58, 3\n\t"
    "s_cselect_b32 s46, 147456, 0\n\t"
    "v_mfma_f32_16x16x32_bf16 v[20:23], a[248:251], v[212:215], v[20:23]\n\t"
    "v_mfma_f32_16x16x32_bf16 v[20:23], a[252:255], v[236:239], v[20:23]\n\t"
    "v_mfma_f32_16x16x32_bf16 v[24:27], a[248:251], v[216:219], v[24:27]\n\t"
    "v_mfma_f32_16x16x32_bf16 v[24:27], a[252:255], v[240:243], v[24:27]\n\t"
    "v_mfma_f32_16x16x32_bf16 v[28:31], a[248:251], v[220:223], v[28:31]\n\t"
    "v_mfma_f32_16x16x32_bf16 v[28:31], a[252:255], v[244:247], v[28:31]\n\t"
    "v_mfma_f32_16x16x32_bf16 v[32:35], a[248:251], v[224:227], v[32:35]\n\t"
    "v_mfma_f32_16x16x32_bf16 v[32:35], a[252:255], v[248:251], v[32:35]\n\t"
    "v_mfma_f32_16x16x32_bf16 v[36:39], a[248:251], v[228:231], v[36:39]\n\t"
    "v_mfma_f32_16x16x32_bf16 v[36:39], a[252:255], v[252:255], v[36:39]\n\t"
    "s_waitcnt lgkmcnt(0)\n\t"
    "ds_read_b128 a[248:251], v0 offset:1024\n\t"
    "ds_read_b128 a[252:255], v0 offset:1536\n\t"
    "s_add_u32 s58, s58, 1\n\t"
    "s_cmp_lt_u32 s58, 4\n\t"
    "s_cbranch_scc1 Lloop_%=\n\t"
    "Lloopend_%=:\n\t"
    "s_waitcnt lgkmcnt(0)\n\t"
    "v_mov_b32 v63, v3\n\t"
    "v_mul_f32 v16, v16, v8\n\t"
    "v_mul_f32 v17, v17, v8\n\t"
    "v_mul_f32 v18, v18, v8\n\t"
    "v_mul_f32 v19, v19, v8\n\t"
    "v_cvt_pk_bf16_f32 v16, v16, v17\n\t"
    "v_cvt_pk_bf16_f32 v17, v18, v19\n\t"
    "v_mul_f32 v20, v20, v9\n\t"
    "v_mul_f32 v21, v21, v9\n\t"
    "v_mul_f32 v22, v22, v9\n\t"
    "v_mul_f32 v23, v23, v9\n\t"
    "v_cvt_pk_bf16_f32 v20, v20, v21\n\t"
    "v_cvt_pk_bf16_f32 v21, v22, v23\n\t"
    "ds_write_b64 v14, v[40:41] offset:0\n\t"
    "ds_write_b64 v14, v[44:45] offset:256\n\t"
    "ds_write_b64 v14, v[16:17] offset:1152\n\t"
    "ds_write_b64 v14, v[20:21] offset:1408\n\t"
    "ds_read_b128 v[208:211], v15 offset:0\n\t"
    "ds_read_b128 v[212:215], v15 offset:128\n\t"
    "ds_read_b128 v[216:219], v15 offset:256\n\t"
    "ds_read_b128 v[220:223], v15 offset:384\n\t"
    "v_mul_f32 v24, v24, v10\n\t"
    "v_mul_f32 v25, v25, v10\n\t"
    "v_mul_f32 v26, v26, v10\n\t"
    "v_mul_f32 v27, v27, v10\n\t"
    "v_cvt_pk_bf16_f32 v24, v24, v25\n\t"
    "v_cvt_pk_bf16_f32 v25, v26, v27\n\t"
    "v_mul_f32 v28, v28, v11\n\t"
    "v_mul_f32 v29, v29, v11\n\t"
    "v_mul_f32 v30, v30, v11\n\t"
    "v_mul_f32 v31, v31, v11\n\t"
    "v_cvt_pk_bf16_f32 v28, v28, v29\n\t"
    "v_cvt_pk_bf16_f32 v29, v30, v31\n\t"
    "ds_write_b64 v14, v[48:49] offset:2304\n\t"
    "ds_write_b64 v14, v[52:53] offset:2560\n\t"
    "ds_write_b64 v14, v[24:25] offset:3456\n\t"
    "ds_write_b64 v14, v[28:29] offset:3712\n\t"
    "ds_read_b128 v[224:227], v15 offset:2304\n\t"
    "ds_read_b128 v[228:231], v15 offset:2432\n\t"
    "ds_read_b128 v[232:235], v15 offset:2560\n\t"
    "ds_read_b128 v[236:239], v15 offset:2688\n\t"
    "s_mov_b32 s54, 0\n\t"
    "s_waitcnt lgkmcnt(11)\n\t"
    "buffer_store_dwordx4 v[208:211], v63, s[48:51], s54 offen offset:256 sc1\n\t"
    "s_waitcnt lgkmcnt(10)\n\t"
    "buffer_store_dwordx4 v[212:215], v63, s[48:51], s54 offen offset:3328 sc1\n\t"
    "s_mov_b32 s54, 6144\n\t"
    "s_waitcnt lgkmcnt(9)\n\t"
    "buffer_store_dwordx4 v[216:219], v63, s[48:51], s54 offen offset:256 sc1\n\t"
    "s_waitcnt lgkmcnt(8)\n\t"
    "buffer_store_dwordx4 v[220:223], v63, s[48:51], s54 offen offset:3328 sc1\n\t"
    "v_mul_f32 v32, v32, v12\n\t"
    "v_mul_f32 v33, v33, v12\n\t"
    "v_mul_f32 v34, v34, v12\n\t"
    "v_mul_f32 v35, v35, v12\n\t"
    "v_cvt_pk_bf16_f32 v32, v32, v33\n\t"
    "v_cvt_pk_bf16_f32 v33, v34, v35\n\t"
    "v_mul_f32 v36, v36, v13\n\t"
    "v_mul_f32 v37, v37, v13\n\t"
    "v_mul_f32 v38, v38, v13\n\t"
    "v_mul_f32 v39, v39, v13\n\t"
    "v_cvt_pk_bf16_f32 v36, v36, v37\n\t"
    "v_cvt_pk_bf16_f32 v37, v38, v39\n\t"
    "ds_write_b64 v14, v[56:57] offset:4608\n\t"
    "ds_write_b64 v14, v[60:61] offset:4864\n\t"
    "ds_write_b64 v14, v[32:33] offset:5760\n\t"
    "ds_write_b64 v14, v[36:37] offset:6016\n\t"
    "ds_read_b128 v[240:243], v15 offset:4608\n\t"
    "ds_read_b128 v[244:247], v15 offset:4736\n\t"
    "ds_read_b128 v[248:251], v15 offset:4864\n\t"
    "ds_read_b128 v[252:255], v15 offset:4992\n\t"
    "s_mov_b32 s54, 12288\n\t"
    "s_waitcnt lgkmcnt(11)\n\t"
    "buffer_store_dwordx4 v[224:227], v63, s[48:51], s54 offen offset:256 sc1\n\t"
    "s_waitcnt lgkmcnt(10)\n\t"
    "buffer_store_dwordx4 v[228:231], v63, s[48:51], s54 offen offset:3328 sc1\n\t"
    "s_mov_b32 s54, 18432\n\t"
    "s_waitcnt lgkmcnt(9)\n\t"
    "buffer_store_dwordx4 v[232:235], v63, s[48:51], s54 offen offset:256 sc1\n\t"
    "s_waitcnt lgkmcnt(8)\n\t"
    "buffer_store_dwordx4 v[236:239], v63, s[48:51], s54 offen offset:3328 sc1\n\t"
    "s_mov_b32 s54, 24576\n\t"
    "s_waitcnt lgkmcnt(3)\n\t"
    "buffer_store_dwordx4 v[240:243], v63, s[48:51], s54 offen offset:256 sc1\n\t"
    "s_waitcnt lgkmcnt(2)\n\t"
    "buffer_store_dwordx4 v[244:247], v63, s[48:51], s54 offen offset:3328 sc1\n\t"
    "s_waitcnt lgkmcnt(1)\n\t"
    "buffer_store_dwordx4 v[248:251], v63, s[48:51], s65 offen offset:256 sc1\n\t"
    "s_waitcnt lgkmcnt(0)\n\t"
    "buffer_store_dwordx4 v[252:255], v63, s[48:51], s75 offen offset:256 sc1\n\t"
    "s_waitcnt vmcnt(0) lgkmcnt(0)\n\t"
    "s_mov_b32 %0, s60\n\t"
    : "=s"(flag)
    : "s"(Q + h0), "s"(K + h0), "s"(V + h0), "s"(O + h0), "s"(wid), "s"(ldsb)
      , "s"(bid)
    : "v0", "v1", "v2", "v3", "v4", "v5", "v6", "v7", "v8", "v9", "v10", "v11", "v12", "v13", "v14", "v15", "v16", "v17", "v18", "v19", "v20", "v21", "v22", "v23", "v24", "v25", "v26", "v27", "v28", "v29", "v30", "v31", "v32", "v33", "v34", "v35", "v36", "v37", "v38", "v39", "v40", "v41", "v42", "v43", "v44", "v45", "v46", "v47", "v48", "v49", "v50", "v51", "v52", "v53", "v54", "v55", "v56", "v57", "v58", "v59", "v60", "v61", "v62", "v63", "v64", "v65", "v66", "v67", "v68", "v69", "v70", "v71", "v72", "v73", "v74", "v75", "v76", "v77", "v78", "v79", "v80", "v81", "v82", "v83", "v84", "v85", "v86", "v87", "v88", "v89", "v90", "v91", "v92", "v93", "v94", "v95", "v96", "v97", "v98", "v99", "v100", "v101", "v102", "v103", "v104", "v105", "v106", "v107", "v108", "v109", "v110", "v111", "v112", "v113", "v114", "v115", "v116", "v117", "v118", "v119", "v120", "v121", "v122", "v123", "v124", "v125", "v126", "v127", "v128", "v129", "v130", "v131", "v132", "v133", "v134", "v135", "v136", "v137", "v138", "v139", "v140", "v141", "v142", "v143", "v144", "v145", "v146", "v147", "v148", "v149", "v150", "v151", "v152", "v153", "v154", "v155", "v156", "v157", "v158", "v159", "v160", "v161", "v162", "v163", "v164", "v165", "v166", "v167", "v168", "v169", "v170", "v171", "v172", "v173", "v174", "v175", "v176", "v177", "v178", "v179", "v180", "v181", "v182", "v183", "v184", "v185", "v186", "v187", "v188", "v189", "v190", "v191", "v192", "v193", "v194", "v195", "v196", "v197", "v198", "v199", "v200", "v201", "v202", "v203", "v204", "v205", "v206", "v207", "v208", "v209", "v210", "v211", "v212", "v213", "v214", "v215", "v216", "v217", "v218", "v219", "v220", "v221", "v222", "v223", "v224", "v225", "v226", "v227", "v228", "v229", "v230", "v231", "v232", "v233", "v234", "v235", "v236", "v237", "v238", "v239", "v240", "v241", "v242", "v243", "v244", "v245", "v246", "v247", "v248", "v249", "v250", "v251", "v252", "v253", "v254", "v255", "a0", "a1", "a2", "a3", "a4", "a5", "a6", "a7", "a8", "a9", "a10", "a11", "a12", "a13", "a14", "a15", "a16", "a17", "a18", "a19", "a20", "a21", "a22", "a23", "a24", "a25", "a26", "a27", "a28", "a29", "a30", "a31", "a32", "a33", "a34", "a35", "a36", "a37", "a38", "a39", "a40", "a41", "a42", "a43", "a44", "a45", "a46", "a47", "a48", "a49", "a50", "a51", "a52", "a53", "a54", "a55", "a56", "a57", "a58", "a59", "a60", "a61", "a62", "a63", "a64", "a65", "a66", "a67", "a68", "a69", "a70", "a71", "a72", "a73", "a74", "a75", "a76", "a77", "a78", "a79", "a80", "a81", "a82", "a83", "a84", "a85", "a86", "a87", "a88", "a89", "a90", "a91", "a92", "a93", "a94", "a95", "a96", "a97", "a98", "a99", "a100", "a101", "a102", "a103", "a104", "a105", "a106", "a107", "a108", "a109", "a110", "a111", "a112", "a113", "a114", "a115", "a116", "a117", "a118", "a119", "a120", "a121", "a122", "a123", "a124", "a125", "a126", "a127", "a128", "a129", "a130", "a131", "a132", "a133", "a134", "a135", "a136", "a137", "a138", "a139", "a140", "a141", "a142", "a143", "a144", "a145", "a146", "a147", "a148", "a149", "a150", "a151", "a152", "a153", "a154", "a155", "a156", "a157", "a158", "a159", "a160", "a161", "a162", "a163", "a164", "a165", "a166", "a167", "a168", "a169", "a170", "a171", "a172", "a173", "a174", "a175", "a176", "a177", "a178", "a179", "a180", "a181", "a182", "a183", "a184", "a185", "a186", "a187", "a188", "a189", "a190", "a191", "a192", "a193", "a194", "a195", "a196", "a197", "a198", "a199", "a200", "a201", "a202", "a203", "a204", "a205", "a206", "a207", "a208", "a209", "a210", "a211", "a212", "a213", "a214", "a215", "a216", "a217", "a218", "a219", "a220", "a221", "a222", "a223", "a224", "a225", "a226", "a227", "a228", "a229", "a230", "a231", "a232", "a233", "a234", "a235", "a236", "a237", "a238", "a239", "a240", "a241", "a242", "a243", "a244", "a245", "a246", "a247", "a248", "a249", "a250", "a251", "a252", "a253", "a254", "a255", "s34", "s35", "s36", "s37", "s38", "s39", "s40", "s41", "s42", "s43", "s44", "s45", "s46", "s47", "s48", "s49", "s50", "s51", "s52", "s53", "s54", "s55", "s56", "s57", "s58", "s59", "s60", "s61", "s62", "s63", "s64", "s65", "s66", "s67", "s68", "s69", "s70", "s71", "s72", "s73", "s74", "s75", "s76", "s77", "s78", "s79", "s80", "s81", "s82", "s83", "s84", "s85", "s86", "s87", "s88", "s89", "s90", "s91", "s92", "s93", "s94", "s95", "s96", "s97", "s98", "s99", "vcc", "scc", "memory");
    if (flag) {
        const int lane = __builtin_amdgcn_mbcnt_hi(~0u, __builtin_amdgcn_mbcnt_lo(~0u, 0u));
        for (int it = 0; it < NHEADS / NWG; ++it) {
            if (!((flag >> it) & 1u)) continue;
            const size_t h = (size_t)(bid + NWG * it) * SEQ * DH;
            for (int b = 0; b < 3; ++b) {
                const size_t r0 = (size_t)(group_row(wid, 4 * b + ((lane & 31) >> 3)) + (lane & 7)) * DH;
                slow_block(Q + h + r0, K + h, V + h, O + h + r0, lane);
            }
        }
    }
}

extern "C" void kernel_launch(void* const* d_in, const int* in_sizes, int n_in, void* d_out, int out_size, void* d_ws, size_t ws_size, hipStream_t stream) {
    constexpr int LDSB = 159744;
    (void)hipFuncSetAttribute((const void*)attn_fwd, hipFuncAttributeMaxDynamicSharedMemorySize, LDSB);
    hipLaunchKernelGGL(attn_fwd, dim3(NWG), dim3(256), LDSB, stream, (const unsigned short*)d_in[0], (const unsigned short*)d_in[1],
                       (const unsigned short*)d_in[2], (unsigned short*)d_out
                       );
}
```
